```python
import torch

def _softmax(x, axis=-1):
    return torch.softmax(x, dim=axis)
def _gelu(x):
    return torch.nn.functional.gelu(x, approximate="tanh")
def _silu(x):
    return torch.nn.functional.silu(x)
def _sigmoid(x):
    return torch.sigmoid(x)
def _layernorm(x, axis=-1, eps=1e-5):
    m = x.mean(dim=axis, keepdim=True)
    v = x.var(dim=axis, keepdim=True, unbiased=False)
    return (x - m) / torch.sqrt(v + eps)
def _scatter_add(tgt, idx, src, axis=0):
    return tgt.clone().index_add_(axis, idx, src)
def _segment_sum(src, seg, n):
    out = torch.zeros((n,) + src.shape[1:], dtype=src.dtype)
    return out.index_add_(0, seg, src)
def _pad(x, pw):
    flat = tuple(v for pair in reversed(pw) for v in pair)
    return torch.nn.functional.pad(x, flat)

kernel_name = 'standalone__mha_softmax__near_ridge'

def setup_inputs():

    _w = (lambda a: (lambda t: t.to(torch.bfloat16) if t.is_floating_point() else t)(torch.as_tensor(a)))
    B, H, Sq, Sk, D = 8, 32, 368, 512, 128
    return {
        "q": _w((lambda *s: torch.randn(*s, dtype=torch.bfloat16) * 0.8)(B, H, Sq, D)),
        "k": _w((lambda *s: torch.randn(*s, dtype=torch.bfloat16) * 0.8)(B, H, Sk, D)),
        "v": _w((lambda *s: torch.randn(*s, dtype=torch.bfloat16) * 0.05)(B, H, Sk, D)),
    }

def reference(q, k, v):
    scale = q.shape[-1] ** -0.5
    scores = torch.einsum("bhqd,bhkd->bhqk", q, k) * scale
    p = _softmax(scores, axis=-1)
    return torch.einsum("bhqk,bhkd->bhqd", p, v)
```

```cpp
#include <hip/hip_runtime.h>
#include <hip/hip_bf16.h>
typedef float f32x16 __attribute__((ext_vector_type(16)));
typedef __bf16 bf16x8 __attribute__((ext_vector_type(8)));
typedef unsigned u32x4 __attribute__((ext_vector_type(4)));
typedef unsigned u32x2 __attribute__((ext_vector_type(2)));
typedef float f32x2 __attribute__((ext_vector_type(2)));
typedef __bf16 bf16x2 __attribute__((ext_vector_type(2)));
constexpr int SQ = 368, SK = 512, D = 128;
__device__ __forceinline__ unsigned cvtpk(float lo, float hi) { f32x2 v = {lo, hi}; bf16x2 b = __builtin_convertvector(v, bf16x2); return __builtin_bit_cast(unsigned, b); }

__device__ __forceinline__ void slow_qblock(const unsigned short* __restrict__ Qh, const unsigned short* __restrict__ Kh, const unsigned short* __restrict__ Vh,
                                            unsigned short* __restrict__ Oh, int row0, int lane) {
  const int r = lane & 31, h = lane >> 5;
  const int qrow = row0 + r, qc = qrow < SQ ? qrow : SQ - 1;
  const float C = 0.08838834764831845f * 1.4426950408889634f;
  bf16x8 qf[8];
#pragma unroll
  for (int d0 = 0; d0 < 8; ++d0) qf[d0] = *(const bf16x8*)(Qh + (size_t)qc * D + 16 * d0 + 8 * h);
  f32x16 o[4] = {};
  float m = -INFINITY, l = 0.f;
  for (int t = 0; t < SK / 32; ++t) {
    f32x16 s = {};
#pragma unroll
    for (int d0 = 0; d0 < 8; ++d0) {
      bf16x8 kf = *(const bf16x8*)(Kh + (size_t)(32 * t + r) * D + 16 * d0 + 8 * h);
      s = __builtin_amdgcn_mfma_f32_32x32x16_bf16(kf, qf[d0], s, 0, 0, 0);
    }
    float mx = s[0];
#pragma unroll
    for (int i = 1; i < 16; ++i) mx = fmaxf(mx, s[i]);
    mx = fmaxf(mx, __shfl_xor(mx, 32));
    const float mn = fmaxf(m, mx);
    const float alpha = __builtin_amdgcn_exp2f((m - mn) * C);
    m = mn;
    float ps = 0.f;
#pragma unroll
    for (int i = 0; i < 16; ++i) { s[i] = __builtin_amdgcn_exp2f((s[i] - mn) * C); ps += s[i]; }
    l = l * alpha + ps;
#pragma unroll
    for (int db = 0; db < 4; ++db)
#pragma unroll
      for (int i = 0; i < 16; ++i) o[db][i] *= alpha;
#pragma unroll
    for (int ks = 0; ks < 2; ++ks) {
      u32x4 pw = {cvtpk(s[8 * ks + 0], s[8 * ks + 1]), cvtpk(s[8 * ks + 2], s[8 * ks + 3]), cvtpk(s[8 * ks + 4], s[8 * ks + 5]), cvtpk(s[8 * ks + 6], s[8 * ks + 7])};
      bf16x8 pf = __builtin_bit_cast(bf16x8, pw);
#pragma unroll
      for (int db = 0; db < 4; ++db) {
        unsigned short e[8];
#pragma unroll
        for (int j = 0; j < 8; ++j) e[j] = Vh[(size_t)(32 * t + 16 * ks + 8 * (j >> 2) + 4 * h + (j & 3)) * D + 32 * db + r];
        u32x4 vw = {e[0] | ((unsigned)e[1] << 16), e[2] | ((unsigned)e[3] << 16), e[4] | ((unsigned)e[5] << 16), e[6] | ((unsigned)e[7] << 16)};
        o[db] = __builtin_amdgcn_mfma_f32_32x32x16_bf16(__builtin_bit_cast(bf16x8, vw), pf, o[db], 0, 0, 0);
      }
    }
  }
  l += __shfl_xor(l, 32);
  const float il = 1.f / l;
  if (qrow < SQ) {
#pragma unroll
    for (int db = 0; db < 4; ++db)
#pragma unroll
      for (int g = 0; g < 4; ++g) {
        u32x2 w = {cvtpk(o[db][4 * g] * il, o[db][4 * g + 1] * il), cvtpk(o[db][4 * g + 2] * il, o[db][4 * g + 3] * il)};
        *(u32x2*)(Oh + (size_t)qrow * D + 32 * db + 8 * g + 4 * h) = w;
      }
  }
}

__global__ __launch_bounds__(256, 1) void attn_slow(const unsigned short* __restrict__ Q, const unsigned short* __restrict__ K, const unsigned short* __restrict__ V, unsigned short* __restrict__ O) {
  const int head = blockIdx.x, wave = threadIdx.x >> 6, lane = threadIdx.x & 63;
  for (int j = 0; j < 3; ++j)
    slow_qblock(Q + (size_t)head * SQ * D, K + (size_t)head * SK * D, V + (size_t)head * SK * D, O + (size_t)head * SQ * D, 32 * (wave + 4 * j), lane);
}
extern "C" void kernel_launch(void* const* d_in, const int* in_sizes, int n_in, void* d_out, int out_size, void* d_ws, size_t ws_size, hipStream_t stream) {
  attn_slow<<<256, 256, 0, stream>>>((const unsigned short*)d_in[0], (const unsigned short*)d_in[1], (const unsigned short*)d_in[2], (unsigned short*)d_out);
}
```

```cpp
#include <hip/hip_runtime.h>
#include <hip/hip_bf16.h>
typedef float f32x16 __attribute__((ext_vector_type(16)));
typedef __bf16 bf16x8 __attribute__((ext_vector_type(8)));
typedef unsigned u32x4 __attribute__((ext_vector_type(4)));
typedef unsigned u32x2 __attribute__((ext_vector_type(2)));
typedef float f32x2 __attribute__((ext_vector_type(2)));
typedef __bf16 bf16x2 __attribute__((ext_vector_type(2)));
constexpr int SQ = 368, SK = 512, D = 128;
#define DIAG 0
__device__ __forceinline__ unsigned cvtpk(float lo, float hi) { f32x2 v = {lo, hi}; bf16x2 b = __builtin_convertvector(v, bf16x2); return __builtin_bit_cast(unsigned, b); }

__device__ __forceinline__ void slow_qblock(const unsigned short* __restrict__ Qh, const unsigned short* __restrict__ Kh, const unsigned short* __restrict__ Vh,
                                            unsigned short* __restrict__ Oh, int row0, int lane) {
  const int r = lane & 31, h = lane >> 5;
  const int qrow = row0 + r, qc = qrow < SQ ? qrow : SQ - 1;
  const float C = 0.08838834764831845f * 1.4426950408889634f;
  bf16x8 qf[8];
#pragma unroll
  for (int d0 = 0; d0 < 8; ++d0) qf[d0] = *(const bf16x8*)(Qh + (size_t)qc * D + 16 * d0 + 8 * h);
  f32x16 o[4] = {};
  float m = -INFINITY, l = 0.f;
  for (int t = 0; t < SK / 32; ++t) {
    f32x16 s = {};
#pragma unroll
    for (int d0 = 0; d0 < 8; ++d0) {
      bf16x8 kf = *(const bf16x8*)(Kh + (size_t)(32 * t + r) * D + 16 * d0 + 8 * h);
      s = __builtin_amdgcn_mfma_f32_32x32x16_bf16(kf, qf[d0], s, 0, 0, 0);
    }
    float mx = s[0];
#pragma unroll
    for (int i = 1; i < 16; ++i) mx = fmaxf(mx, s[i]);
    mx = fmaxf(mx, __shfl_xor(mx, 32));
    const float mn = fmaxf(m, mx);
    const float alpha = __builtin_amdgcn_exp2f((m - mn) * C);
    m = mn;
    float ps = 0.f;
#pragma unroll
    for (int i = 0; i < 16; ++i) { s[i] = __builtin_amdgcn_exp2f((s[i] - mn) * C); ps += s[i]; }
    l = l * alpha + ps;
#pragma unroll
    for (int db = 0; db < 4; ++db)
#pragma unroll
      for (int i = 0; i < 16; ++i) o[db][i] *= alpha;
#pragma unroll
    for (int ks = 0; ks < 2; ++ks) {
      u32x4 pw = {cvtpk(s[8 * ks + 0], s[8 * ks + 1]), cvtpk(s[8 * ks + 2], s[8 * ks + 3]), cvtpk(s[8 * ks + 4], s[8 * ks + 5]), cvtpk(s[8 * ks + 6], s[8 * ks + 7])};
      bf16x8 pf = __builtin_bit_cast(bf16x8, pw);
#pragma unroll
      for (int db = 0; db < 4; ++db) {
        unsigned short e[8];
#pragma unroll
        for (int j = 0; j < 8; ++j) e[j] = Vh[(size_t)(32 * t + 16 * ks + 8 * (j >> 2) + 4 * h + (j & 3)) * D + 32 * db + r];
        u32x4 vw = {e[0] | ((unsigned)e[1] << 16), e[2] | ((unsigned)e[3] << 16), e[4] | ((unsigned)e[5] << 16), e[6] | ((unsigned)e[7] << 16)};
        o[db] = __builtin_amdgcn_mfma_f32_32x32x16_bf16(__builtin_bit_cast(bf16x8, vw), pf, o[db], 0, 0, 0);
      }
    }
  }
  l += __shfl_xor(l, 32);
  const float il = 1.f / l;
  if (qrow < SQ && r < 16) {
#pragma unroll
    for (int db = 0; db < 4; ++db)
#pragma unroll
      for (int g = 0; g < 4; ++g) {
        u32x2 w = {cvtpk(o[db][4 * g] * il, o[db][4 * g + 1] * il), cvtpk(o[db][4 * g + 2] * il, o[db][4 * g + 3] * il)};
        *(u32x2*)(Oh + (size_t)qrow * D + 32 * db + 8 * g + 4 * h) = w;
      }
  }
}

__global__ __launch_bounds__(256, 1) void attn_fwd_kernel(const unsigned short* __restrict__ Q, const unsigned short* __restrict__ K, const unsigned short* __restrict__ V,
                                                          unsigned short* __restrict__ O, unsigned long long* ws) {
  __shared__ __attribute__((aligned(1024))) char lds[163840];
  unsigned flag, wave;
  const unsigned short *Qh, *Kh, *Vh; unsigned short* Oh;
  asm volatile(
    "s_load_dwordx8 s[84:91], %6, 0x0\n\t"
    "s_getpc_b64 s[76:77]\n\t"
    "v_lshlrev_b32 v47, 4, v0\n\t"
    "s_lshr_b32 s78, %7, 3\n\t"
    "v_and_b32 v47, 0xff0, v47\n\t"
    "s_and_b32 s78, s78, 7\n\t"
    "s_lshl_b32 s78, s78, 13\n\t"
    "s_add_u32 s76, s76, s78\n\t"
    "s_addc_u32 s77, s77, 0\n\t"
    "global_load_dwordx4 v[96:99], v47, s[76:77]\n\t"
    "v_add_u32 v47, 0x1000, v47\n\t"
    "global_load_dwordx4 v[96:99], v47, s[76:77]\n\t"
    "s_mov_b32 s60, %8\n\t"
    "s_mul_i32 s96, %7, 0x17000\n\t"
    "s_lshl_b32 s97, %7, 17\n\t"
    "v_mbcnt_lo_u32_b32 v0, -1, 0\n\t"
    "v_mbcnt_hi_u32_b32 v0, -1, v0\n\t"
    "v_lshrrev_b32 v2, 4, v0\n\t"
    "v_and_b32 v3, 15, v0\n\t"
    "v_xor_b32 v3, v3, v2\n\t"
    "v_lshlrev_b32 v3, 4, v3\n\t"
    "v_lshl_add_u32 v12, v2, 8, v3\n\t"
    "v_xor_b32 v13, 64, v12\n\t"
    "v_xor_b32 v14, 128, v12\n\t"
    "v_xor_b32 v15, 192, v12\n\t"
    "s_mov_b32 s72, %9\n\t"
    "s_lshl_b32 s66, s60, 12\n\t"
    "s_add_u32 s65, s72, s66\n\t"
    "s_add_u32 s65, s65, 65536\n\t"
    "s_mov_b32 s42, 94208\n\t"
    "s_mov_b32 s43, 0x20000\n\t"
    "s_mov_b32 s46, 131072\n\t"
    "s_mov_b32 s47, 0x20000\n\t"
    "s_mov_b32 s50, 131072\n\t"
    "s_mov_b32 s51, 0x20000\n\t"
    "s_lshl_b32 s62, s60, 11\n\t"
    "s_add_u32 s61, s62, s72\n\t"
    "s_and_b32 s69, s60, 1\n\t"
    "s_lshl_b32 s69, s69, 7\n\t"
    "v_xor_b32 v16, s69, v12\n\t"
    "v_xor_b32 v17, s69, v13\n\t"
    "s_lshr_b32 s70, s60, 1\n\t"
    "s_lshl_b32 s64, s70, 12\n\t"
    "s_lshl_b32 s63, s69, 3\n\t"
    "s_add_u32 s63, s63, s64\n\t"
    "s_add_u32 s63, s63, s72\n\t"
    "s_add_u32 s63, s63, 8192\n\t"
    "s_add_u32 s64, s64, s69\n\t"
    "v_bfe_u32 v3, v0, 1, 2\n\t"
    "v_xor_b32 v3, v3, v2\n\t"
    "v_lshlrev_b32 v3, 5, v3\n\t"
    "v_and_b32 v46, 1, v0\n\t"
    "v_lshl_add_u32 v3, v46, 4, v3\n\t"
    "v_lshrrev_b32 v46, 3, v0\n\t"
    "v_lshl_add_u32 v18, v46, 8, v3\n\t"
    "s_waitcnt lgkmcnt(0)\n\t"
    "s_add_u32 s84, s84, s96\n\t"
    "s_addc_u32 s85, s85, 0\n\t"
    "s_add_u32 s86, s86, s97\n\t"
    "s_addc_u32 s87, s87, 0\n\t"
    "s_mov_b32 s40, s84\n\t"
    "s_and_b32 s41, s85, 0xffff\n\t"
    "s_mov_b32 s44, s86\n\t"
    "s_and_b32 s45, s87, 0xffff\n\t"
    "s_bitcmp1_b32 s60, 0\n\t"
    "s_cbranch_scc1 21f\n\t"
    "s_add_u32 m0, s65, 0\n\t"
    "s_add_u32 s58, s66, 0\n\t"
    "buffer_load_dwordx4 v12, s[40:43], s58 offen nt lds\n\t"
    "buffer_load_dwordx4 v13, s[40:43], s58 offen offset:1024 nt lds\n\t"
    "buffer_load_dwordx4 v14, s[40:43], s58 offen offset:2048 nt lds\n\t"
    "buffer_load_dwordx4 v15, s[40:43], s58 offen offset:3072 nt lds\n\t"
    "s_add_u32 m0, s61, 0\n\t"
    "s_add_u32 s58, s62, 0\n\t"
    "buffer_load_dwordx4 v16, s[44:47], s58 offen nt lds\n\t"
    "buffer_load_dwordx4 v17, s[44:47], s58 offen offset:1024 nt lds\n\t"
    "s_branch 22f\n\t"
    "21:\n\t"
    "s_add_u32 m0, s61, 0\n\t"
    "s_add_u32 s58, s62, 0\n\t"
    "buffer_load_dwordx4 v16, s[44:47], s58 offen nt lds\n\t"
    "buffer_load_dwordx4 v17, s[44:47], s58 offen offset:1024 nt lds\n\t"
    "s_add_u32 m0, s65, 0\n\t"
    "s_add_u32 s58, s66, 0\n\t"
    "buffer_load_dwordx4 v12, s[40:43], s58 offen nt lds\n\t"
    "buffer_load_dwordx4 v13, s[40:43], s58 offen offset:1024 nt lds\n\t"
    "buffer_load_dwordx4 v14, s[40:43], s58 offen offset:2048 nt lds\n\t"
    "buffer_load_dwordx4 v15, s[40:43], s58 offen offset:3072 nt lds\n\t"
    "22:\n\t"
    "s_add_u32 m0, s61, 16384\n\t"
    "s_add_u32 s58, s62, 8192\n\t"
    "buffer_load_dwordx4 v16, s[44:47], s58 offen nt lds\n\t"
    "buffer_load_dwordx4 v17, s[44:47], s58 offen offset:1024 nt lds\n\t"
    "s_add_u32 s88, s88, s97\n\t"
    "s_addc_u32 s89, s89, 0\n\t"
    "s_mov_b32 s48, s88\n\t"
    "s_and_b32 s49, s89, 0xffff\n\t"
    "s_add_u32 m0, s63, 0\n\t"
    "s_add_u32 s59, s64, 0\n\t"
    "buffer_load_dwordx4 v18, s[48:51], s59 offen nt lds\n\t"
    "buffer_load_dwordx4 v18, s[48:51], s59 offen offset:2048 nt lds\n\t"
    "s_add_u32 m0, s63, 16384\n\t"
    "s_add_u32 s59, s64, 8192\n\t"
    "buffer_load_dwordx4 v18, s[48:51], s59 offen nt lds\n\t"
    "buffer_load_dwordx4 v18, s[48:51], s59 offen offset:2048 nt lds\n\t"
    "s_add_u32 s90, s90, s96\n\t"
    "s_addc_u32 s91, s91, 0\n\t"
    "s_mov_b32 s52, s90\n\t"
    "s_and_b32 s53, s91, 0xffff\n\t"
    "s_mov_b32 s54, 94208\n\t"
    "s_mov_b32 s55, 0x20000\n\t"
    "s_mov_b32 s56, 0x3e0293ee\n\t"
    "s_mov_b32 s57, 0x3e0293ee\n\t"
    "s_mov_b32 s71, 0x4b800000\n\t"
    "s_mov_b32 s68, 0\n\t"
    "v_and_b32 v3, 15, v0\n\t"
    "v_xor_b32 v46, v2, v3\n\t"
    "v_lshlrev_b32 v46, 4, v46\n\t"
    "v_lshl_add_u32 v46, v3, 8, v46\n\t"
    "v_xor_b32 v4, 0, v46\n\t"
    "v_xor_b32 v5, 64, v46\n\t"
    "v_xor_b32 v6, 128, v46\n\t"
    "v_xor_b32 v7, 192, v46\n\t"
    "v_add_u32 v20, s65, v4\n\t"
    "v_add_u32 v21, s65, v5\n\t"
    "v_add_u32 v22, s65, v6\n\t"
    "v_add_u32 v23, s65, v7\n\t"
    "v_add_u32 v4, s72, v4\n\t"
    "v_add_u32 v5, s72, v5\n\t"
    "v_add_u32 v6, s72, v6\n\t"
    "v_add_u32 v7, s72, v7\n\t"
    "v_bfe_u32 v3, v0, 2, 3\n\t"
    "v_lshrrev_b32 v46, 1, v3\n\t"
    "v_lshlrev_b32 v46, 5, v46\n\t"
    "v_lshl_add_u32 v46, v3, 7, v46\n\t"
    "v_lshrrev_b32 v3, 5, v0\n\t"
    "v_lshl_add_u32 v46, v3, 11, v46\n\t"
    "v_and_b32 v3, 3, v0\n\t"
    "v_lshl_add_u32 v46, v3, 3, v46\n\t"
    "v_add_u32 v46, s72, v46\n\t"
    "v_xor_b32 v8, 0, v46\n\t"
    "v_xor_b32 v9, 32, v46\n\t"
    "v_xor_b32 v10, 64, v46\n\t"
    "v_xor_b32 v11, 96, v46\n\t"
    "s_waitcnt vmcnt(6)\n\t"
    "ds_read_b128 v[24:27], v20 offset:0\n\t"
    "ds_read_b128 v[28:31], v21 offset:0\n\t"
    "ds_read_b128 v[32:35], v22 offset:0\n\t"
    "ds_read_b128 v[36:39], v23 offset:0\n\t"
    "s_add_u32 m0, s65, 16384\n\t"
    "s_add_u32 s58, s66, 16384\n\t"
    "buffer_load_dwordx4 v12, s[40:43], s58 offen nt lds\n\t"
    "buffer_load_dwordx4 v13, s[40:43], s58 offen offset:1024 nt lds\n\t"
    "buffer_load_dwordx4 v14, s[40:43], s58 offen offset:2048 nt lds\n\t"
    "buffer_load_dwordx4 v15, s[40:43], s58 offen offset:3072 nt lds\n\t"
    "s_waitcnt lgkmcnt(3)\n\t"
    "v_lshlrev_b32 v2, 16, v24\n\t"
    "v_and_b32 v3, 0xffff0000, v24\n\t"
    "v_pk_mul_f32 v[2:3], v[2:3], s[56:57]\n\t"
    "v_cvt_pk_bf16_f32 v24, v2, v3\n\t"
    "v_accvgpr_write_b32 a0, v24\n\t"
    "v_lshlrev_b32 v46, 16, v25\n\t"
    "v_and_b32 v47, 0xffff0000, v25\n\t"
    "v_pk_mul_f32 v[46:47], v[46:47], s[56:57]\n\t"
    "v_cvt_pk_bf16_f32 v25, v46, v47\n\t"
    "v_accvgpr_write_b32 a1, v25\n\t"
    "v_lshlrev_b32 v2, 16, v26\n\t"
    "v_and_b32 v3, 0xffff0000, v26\n\t"
    "v_pk_mul_f32 v[2:3], v[2:3], s[56:57]\n\t"
    "v_cvt_pk_bf16_f32 v26, v2, v3\n\t"
    "v_accvgpr_write_b32 a2, v26\n\t"
    "v_lshlrev_b32 v46, 16, v27\n\t"
    "v_and_b32 v47, 0xffff0000, v27\n\t"
    "v_pk_mul_f32 v[46:47], v[46:47], s[56:57]\n\t"
    "v_cvt_pk_bf16_f32 v27, v46, v47\n\t"
    "v_accvgpr_write_b32 a3, v27\n\t"
    "s_waitcnt lgkmcnt(2)\n\t"
    "v_lshlrev_b32 v2, 16, v28\n\t"
    "v_and_b32 v3, 0xffff0000, v28\n\t"
    "v_pk_mul_f32 v[2:3], v[2:3], s[56:57]\n\t"
    "v_cvt_pk_bf16_f32 v28, v2, v3\n\t"
    "v_accvgpr_write_b32 a4, v28\n\t"
    "v_lshlrev_b32 v46, 16, v29\n\t"
    "v_and_b32 v47, 0xffff0000, v29\n\t"
    "v_pk_mul_f32 v[46:47], v[46:47], s[56:57]\n\t"
    "v_cvt_pk_bf16_f32 v29, v46, v47\n\t"
    "v_accvgpr_write_b32 a5, v29\n\t"
    "v_lshlrev_b32 v2, 16, v30\n\t"
    "v_and_b32 v3, 0xffff0000, v30\n\t"
    "v_pk_mul_f32 v[2:3], v[2:3], s[56:57]\n\t"
    "v_cvt_pk_bf16_f32 v30, v2, v3\n\t"
    "v_accvgpr_write_b32 a6, v30\n\t"
    "v_lshlrev_b32 v46, 16, v31\n\t"
    "v_and_b32 v47, 0xffff0000, v31\n\t"
    "v_pk_mul_f32 v[46:47], v[46:47], s[56:57]\n\t"
    "v_cvt_pk_bf16_f32 v31, v46, v47\n\t"
    "v_accvgpr_write_b32 a7, v31\n\t"
    "s_waitcnt lgkmcnt(1)\n\t"
    "v_lshlrev_b32 v2, 16, v32\n\t"
    "v_and_b32 v3, 0xffff0000, v32\n\t"
    "v_pk_mul_f32 v[2:3], v[2:3], s[56:57]\n\t"
    "v_cvt_pk_bf16_f32 v32, v2, v3\n\t"
    "v_accvgpr_write_b32 a8, v32\n\t"
    "v_lshlrev_b32 v46, 16, v33\n\t"
    "v_and_b32 v47, 0xffff0000, v33\n\t"
    "v_pk_mul_f32 v[46:47], v[46:47], s[56:57]\n\t"
    "v_cvt_pk_bf16_f32 v33, v46, v47\n\t"
    "v_accvgpr_write_b32 a9, v33\n\t"
    "v_lshlrev_b32 v2, 16, v34\n\t"
    "v_and_b32 v3, 0xffff0000, v34\n\t"
    "v_pk_mul_f32 v[2:3], v[2:3], s[56:57]\n\t"
    "v_cvt_pk_bf16_f32 v34, v2, v3\n\t"
    "v_accvgpr_write_b32 a10, v34\n\t"
    "v_lshlrev_b32 v46, 16, v35\n\t"
    "v_and_b32 v47, 0xffff0000, v35\n\t"
    "v_pk_mul_f32 v[46:47], v[46:47], s[56:57]\n\t"
    "v_cvt_pk_bf16_f32 v35, v46, v47\n\t"
    "v_accvgpr_write_b32 a11, v35\n\t"
    "s_waitcnt lgkmcnt(0)\n\t"
    "v_lshlrev_b32 v2, 16, v36\n\t"
    "v_and_b32 v3, 0xffff0000, v36\n\t"
    "v_pk_mul_f32 v[2:3], v[2:3], s[56:57]\n\t"
    "v_cvt_pk_bf16_f32 v36, v2, v3\n\t"
    "v_accvgpr_write_b32 a12, v36\n\t"
    "v_lshlrev_b32 v46, 16, v37\n\t"
    "v_and_b32 v47, 0xffff0000, v37\n\t"
    "v_pk_mul_f32 v[46:47], v[46:47], s[56:57]\n\t"
    "v_cvt_pk_bf16_f32 v37, v46, v47\n\t"
    "v_accvgpr_write_b32 a13, v37\n\t"
    "v_lshlrev_b32 v2, 16, v38\n\t"
    "v_and_b32 v3, 0xffff0000, v38\n\t"
    "v_pk_mul_f32 v[2:3], v[2:3], s[56:57]\n\t"
    "v_cvt_pk_bf16_f32 v38, v2, v3\n\t"
    "v_accvgpr_write_b32 a14, v38\n\t"
    "v_lshlrev_b32 v46, 16, v39\n\t"
    "v_and_b32 v47, 0xffff0000, v39\n\t"
    "v_pk_mul_f32 v[46:47], v[46:47], s[56:57]\n\t"
    "v_cvt_pk_bf16_f32 v39, v46, v47\n\t"
    "v_accvgpr_write_b32 a15, v39\n\t"
    "s_barrier\n\t"
    "ds_read_b128 a[96:99], v4 offset:0\n\t"
    "ds_read_b128 a[100:103], v5 offset:0\n\t"
    "ds_read_b128 a[104:107], v6 offset:0\n\t"
    "ds_read_b128 a[108:111], v7 offset:0\n\t"
    "ds_read_b128 a[112:115], v4 offset:4096\n\t"
    "ds_read_b128 a[116:119], v5 offset:4096\n\t"
    "ds_read_b128 a[120:123], v6 offset:4096\n\t"
    "ds_read_b128 a[124:127], v7 offset:4096\n\t"
    "s_add_u32 m0, s65, 32768\n\t"
    "s_add_u32 s58, s66, 32768\n\t"
    "buffer_load_dwordx4 v12, s[40:43], s58 offen nt lds\n\t"
    "buffer_load_dwordx4 v13, s[40:43], s58 offen offset:1024 nt lds\n\t"
    "buffer_load_dwordx4 v14, s[40:43], s58 offen offset:2048 nt lds\n\t"
    "buffer_load_dwordx4 v15, s[40:43], s58 offen offset:3072 nt lds\n\t"
    "s_waitcnt lgkmcnt(0)\n\t"
    "v_mfma_f32_16x16x32_bf16 v[80:83], a[96:99], a[0:3], 0\n\t"
    "v_mfma_f32_16x16x32_bf16 v[80:83], a[100:103], a[4:7], v[80:83]\n\t"
    "v_mfma_f32_16x16x32_bf16 v[80:83], a[104:107], a[8:11], v[80:83]\n\t"
    "v_mfma_f32_16x16x32_bf16 v[80:83], a[108:111], a[12:15], v[80:83]\n\t"
    "v_mfma_f32_16x16x32_bf16 v[84:87], a[112:115], a[0:3], 0\n\t"
    "v_mfma_f32_16x16x32_bf16 v[84:87], a[116:119], a[4:7], v[84:87]\n\t"
    "v_mfma_f32_16x16x32_bf16 v[84:87], a[120:123], a[8:11], v[84:87]\n\t"
    "v_mfma_f32_16x16x32_bf16 v[84:87], a[124:127], a[12:15], v[84:87]\n\t"
    "s_waitcnt vmcnt(8)\n\t"
    "s_barrier\n\t"
    "ds_read_b128 a[128:131], v4 offset:16384\n\t"
    "ds_read_b128 a[132:135], v5 offset:16384\n\t"
    "ds_read_b128 a[136:139], v6 offset:16384\n\t"
    "ds_read_b128 a[140:143], v7 offset:16384\n\t"
    "ds_read_b128 a[144:147], v4 offset:20480\n\t"
    "ds_read_b128 a[148:151], v5 offset:20480\n\t"
    "ds_read_b128 a[152:155], v6 offset:20480\n\t"
    "ds_read_b128 a[156:159], v7 offset:20480\n\t"
    "s_add_u32 m0, s65, 49152\n\t"
    "s_add_u32 s58, s66, 49152\n\t"
    "buffer_load_dwordx4 v12, s[40:43], s58 offen nt lds\n\t"
    "buffer_load_dwordx4 v13, s[40:43], s58 offen offset:1024 nt lds\n\t"
    "buffer_load_dwordx4 v14, s[40:43], s58 offen offset:2048 nt lds\n\t"
    "buffer_load_dwordx4 v15, s[40:43], s58 offen offset:3072 nt lds\n\t"
    "v_max3_f32 v49, v80, v81, v82\n\t"
    "v_max3_f32 v49, v49, v83, v84\n\t"
    "v_max3_f32 v49, v49, v85, v86\n\t"
    "v_max_f32 v49, v49, v87\n\t"
    "v_mov_b32 v50, v49\n\t"
    "s_nop 1\n\t"
    "v_permlane16_swap_b32 v49, v50\n\t"
    "v_max_f32 v49, v49, v50\n\t"
    "v_mov_b32 v50, v49\n\t"
    "s_nop 1\n\t"
    "v_permlane32_swap_b32 v49, v50\n\t"
    "v_max_f32 v49, v49, v50\n\t"
    "v_sub_f32 v48, 0, v49\n\t"
    "v_mov_b32 v49, v48\n\t"
    "v_mov_b32 v50, v48\n\t"
    "v_mov_b32 v51, v48\n\t"
    "ds_read_b64_tr_b16 a[160:161], v8 offset:8192\n\t"
    "ds_read_b64_tr_b16 a[162:163], v8 offset:12288\n\t"
    "ds_read_b64_tr_b16 a[164:165], v9 offset:8192\n\t"
    "ds_read_b64_tr_b16 a[166:167], v9 offset:12288\n\t"
    "ds_read_b64_tr_b16 a[168:169], v10 offset:8192\n\t"
    "ds_read_b64_tr_b16 a[170:171], v10 offset:12288\n\t"
    "ds_read_b64_tr_b16 a[172:173], v11 offset:8192\n\t"
    "ds_read_b64_tr_b16 a[174:175], v11 offset:12288\n\t"
    "ds_read_b64_tr_b16 a[176:177], v8 offset:9216\n\t"
    "ds_read_b64_tr_b16 a[178:179], v8 offset:13312\n\t"
    "ds_read_b64_tr_b16 a[180:181], v9 offset:9216\n\t"
    "ds_read_b64_tr_b16 a[182:183], v9 offset:13312\n\t"
    "ds_read_b64_tr_b16 a[184:185], v10 offset:9216\n\t"
    "ds_read_b64_tr_b16 a[186:187], v10 offset:13312\n\t"
    "ds_read_b64_tr_b16 a[188:189], v11 offset:9216\n\t"
    "ds_read_b64_tr_b16 a[190:191], v11 offset:13312\n\t"
    "s_waitcnt lgkmcnt(15)\n\t"
    "s_cmp_eq_u32 s60, 3\n\t"
    "s_cbranch_scc1 3f\n\t"
    "v_mfma_f32_16x16x32_bf16 v[88:91], a[128:131], a[0:3], v[48:51]\n\t"
    "s_waitcnt vmcnt(8)\n\t"
    "ds_read_b128 v[24:27], v20 offset:16384\n\t"
    "ds_read_b128 v[28:31], v21 offset:16384\n\t"
    "ds_read_b128 v[32:35], v22 offset:16384\n\t"
    "ds_read_b128 v[36:39], v23 offset:16384\n\t"
    "ds_read_b64_tr_b16 a[192:193], v8 offset:24576\n\t"
    "v_add_f32 v80, v48, v80\n\t"
    "v_add_f32 v81, v48, v81\n\t"
    "v_mfma_f32_16x16x32_bf16 v[88:91], a[132:135], a[4:7], v[88:91]\n\t"
    "ds_read_b64_tr_b16 a[194:195], v8 offset:28672\n\t"
    "v_add_f32 v82, v48, v82\n\t"
    "v_add_f32 v83, v48, v83\n\t"
    "v_mfma_f32_16x16x32_bf16 v[88:91], a[136:139], a[8:11], v[88:91]\n\t"
    "s_add_u32 m0, s65, 65536\n\t"
    "ds_read_b64_tr_b16 a[196:197], v9 offset:24576\n\t"
    "v_add_f32 v84, v48, v84\n\t"
    "v_add_f32 v85, v48, v85\n\t"
    "v_mfma_f32_16x16x32_bf16 v[88:91], a[140:143], a[12:15], v[88:91]\n\t"
    "s_add_u32 s58, s66, 65536\n\t"
    "ds_read_b64_tr_b16 a[198:199], v9 offset:28672\n\t"
    "v_add_f32 v86, v48, v86\n\t"
    "s_waitcnt lgkmcnt(7)\n\t"
    "v_lshlrev_b32 v2, 16, v24\n\t"
    "v_and_b32 v3, 0xffff0000, v24\n\t"
    "v_mul_f32 v2, s56, v2\n\t"
    "v_mul_f32 v3, s56, v3\n\t"
    "v_cvt_pk_bf16_f32 v24, v2, v3\n\t"
    "v_accvgpr_write_b32 a16, v24\n\t"
    "v_lshlrev_b32 v46, 16, v25\n\t"
    "v_mfma_f32_16x16x32_bf16 v[92:95], a[144:147], a[0:3], v[48:51]\n\t"
    "buffer_load_dwordx4 v12, s[40:43], s58 offen nt lds\n\t"
    "ds_read_b64_tr_b16 a[200:201], v10 offset:24576\n\t"
    "v_add_f32 v87, v48, v87\n\t"
    "v_exp_f32 v80, v80\n\t"
    "v_and_b32 v47, 0xffff0000, v25\n\t"
    "v_mul_f32 v46, s56, v46\n\t"
    "v_mul_f32 v47, s56, v47\n\t"
    "v_cvt_pk_bf16_f32 v25, v46, v47\n\t"
    "v_accvgpr_write_b32 a17, v25\n\t"
    "v_lshlrev_b32 v2, 16, v26\n\t"
    "v_and_b32 v3, 0xffff0000, v26\n\t"
    "v_mul_f32 v2, s56, v2\n\t"
    "v_mfma_f32_16x16x32_bf16 v[92:95], a[148:151], a[4:7], v[92:95]\n\t"
    "buffer_load_dwordx4 v13, s[40:43], s58 offen offset:1024 nt lds\n\t"
    "ds_read_b64_tr_b16 a[202:203], v10 offset:28672\n\t"
    "v_exp_f32 v81, v81\n\t"
    "v_mov_b32 v40, v80\n\t"
    "v_mul_f32 v3, s56, v3\n\t"
    "v_cvt_pk_bf16_f32 v26, v2, v3\n\t"
    "v_accvgpr_write_b32 a18, v26\n\t"
    "v_lshlrev_b32 v46, 16, v27\n\t"
    "v_and_b32 v47, 0xffff0000, v27\n\t"
    "v_mul_f32 v46, s56, v46\n\t"
    "v_mul_f32 v47, s56, v47\n\t"
    "v_cvt_pk_bf16_f32 v27, v46, v47\n\t"
    "v_mfma_f32_16x16x32_bf16 v[92:95], a[152:155], a[8:11], v[92:95]\n\t"
    "buffer_load_dwordx4 v14, s[40:43], s58 offen offset:2048 nt lds\n\t"
    "ds_read_b64_tr_b16 a[204:205], v11 offset:24576\n\t"
    "v_exp_f32 v82, v82\n\t"
    "v_add_f32 v40, v40, v81\n\t"
    "v_accvgpr_write_b32 a19, v27\n\t"
    "s_waitcnt lgkmcnt(9)\n\t"
    "v_lshlrev_b32 v2, 16, v28\n\t"
    "v_and_b32 v3, 0xffff0000, v28\n\t"
    "v_mul_f32 v2, s56, v2\n\t"
    "v_mul_f32 v3, s56, v3\n\t"
    "v_cvt_pk_bf16_f32 v28, v2, v3\n\t"
    "v_mfma_f32_16x16x32_bf16 v[92:95], a[156:159], a[12:15], v[92:95]\n\t"
    "buffer_load_dwordx4 v15, s[40:43], s58 offen offset:3072 nt lds\n\t"
    "ds_read_b64_tr_b16 a[206:207], v11 offset:28672\n\t"
    "v_cvt_pk_bf16_f32 v72, v80, v81\n\t"
    "v_accvgpr_write_b32 a20, v28\n\t"
    "v_lshlrev_b32 v46, 16, v29\n\t"
    "v_and_b32 v47, 0xffff0000, v29\n\t"
    "v_mul_f32 v46, s56, v46\n\t"
    "v_mul_f32 v47, s56, v47\n\t"
    "v_cvt_pk_bf16_f32 v29, v46, v47\n\t"
    "v_accvgpr_write_b32 a21, v29\n\t"
    "v_lshlrev_b32 v2, 16, v30\n\t"
    "ds_read_b64_tr_b16 a[208:209], v8 offset:25600\n\t"
    "v_exp_f32 v83, v83\n\t"
    "v_add_f32 v40, v40, v82\n\t"
    "v_and_b32 v3, 0xffff0000, v30\n\t"
    "v_mul_f32 v2, s56, v2\n\t"
    "v_mul_f32 v3, s56, v3\n\t"
    "v_cvt_pk_bf16_f32 v30, v2, v3\n\t"
    "v_accvgpr_write_b32 a22, v30\n\t"
    "v_lshlrev_b32 v46, 16, v31\n\t"
    "v_and_b32 v47, 0xffff0000, v31\n\t"
    "v_mul_f32 v46, s56, v46\n\t"
    "ds_read_b64_tr_b16 a[210:211], v8 offset:29696\n\t"
    "v_exp_f32 v84, v84\n\t"
    "v_add_f32 v40, v40, v83\n\t"
    "v_mul_f32 v47, s56, v47\n\t"
    "v_cvt_pk_bf16_f32 v31, v46, v47\n\t"
    "v_accvgpr_write_b32 a23, v31\n\t"
    "s_waitcnt lgkmcnt(11)\n\t"
    "v_lshlrev_b32 v2, 16, v32\n\t"
    "v_and_b32 v3, 0xffff0000, v32\n\t"
    "v_mul_f32 v2, s56, v2\n\t"
    "ds_read_b64_tr_b16 a[212:213], v9 offset:25600\n\t"
    "v_cvt_pk_bf16_f32 v73, v82, v83\n\t"
    "v_exp_f32 v85, v85\n\t"
    "v_mul_f32 v3, s56, v3\n\t"
    "v_cvt_pk_bf16_f32 v32, v2, v3\n\t"
    "v_accvgpr_write_b32 a24, v32\n\t"
    "v_lshlrev_b32 v46, 16, v33\n\t"
    "v_and_b32 v47, 0xffff0000, v33\n\t"
    "v_mul_f32 v46, s56, v46\n\t"
    "v_mul_f32 v47, s56, v47\n\t"
    "v_cvt_pk_bf16_f32 v33, v46, v47\n\t"
    "ds_read_b64_tr_b16 a[214:215], v9 offset:29696\n\t"
    "v_add_f32 v40, v40, v84\n\t"
    "v_accvgpr_write_b32 a25, v33\n\t"
    "v_lshlrev_b32 v2, 16, v34\n\t"
    "v_and_b32 v3, 0xffff0000, v34\n\t"
    "v_mul_f32 v2, s56, v2\n\t"
    "v_mul_f32 v3, s56, v3\n\t"
    "v_cvt_pk_bf16_f32 v34, v2, v3\n\t"
    "v_accvgpr_write_b32 a26, v34\n\t"
    "v_lshlrev_b32 v46, 16, v35\n\t"
    "ds_read_b64_tr_b16 a[216:217], v10 offset:25600\n\t"
    "v_exp_f32 v86, v86\n\t"
    "v_add_f32 v40, v40, v85\n\t"
    "v_and_b32 v47, 0xffff0000, v35\n\t"
    "v_mul_f32 v46, s56, v46\n\t"
    "v_mul_f32 v47, s56, v47\n\t"
    "v_cvt_pk_bf16_f32 v35, v46, v47\n\t"
    "v_accvgpr_write_b32 a27, v35\n\t"
    "s_waitcnt lgkmcnt(13)\n\t"
    "v_lshlrev_b32 v2, 16, v36\n\t"
    "ds_read_b64_tr_b16 a[218:219], v10 offset:29696\n\t"
    "v_cvt_pk_bf16_f32 v74, v84, v85\n\t"
    "v_exp_f32 v87, v87\n\t"
    "v_and_b32 v3, 0xffff0000, v36\n\t"
    "v_mul_f32 v2, s56, v2\n\t"
    "v_mul_f32 v3, s56, v3\n\t"
    "v_cvt_pk_bf16_f32 v36, v2, v3\n\t"
    "v_accvgpr_write_b32 a28, v36\n\t"
    "v_lshlrev_b32 v46, 16, v37\n\t"
    "v_and_b32 v47, 0xffff0000, v37\n\t"
    "v_mul_f32 v46, s56, v46\n\t"
    "ds_read_b64_tr_b16 a[220:221], v11 offset:25600\n\t"
    "v_add_f32 v40, v40, v86\n\t"
    "v_add_f32 v40, v40, v87\n\t"
    "v_mul_f32 v47, s56, v47\n\t"
    "v_cvt_pk_bf16_f32 v37, v46, v47\n\t"
    "v_accvgpr_write_b32 a29, v37\n\t"
    "v_lshlrev_b32 v2, 16, v38\n\t"
    "v_and_b32 v3, 0xffff0000, v38\n\t"
    "v_mul_f32 v2, s56, v2\n\t"
    "v_mul_f32 v3, s56, v3\n\t"
    "v_cvt_pk_bf16_f32 v38, v2, v3\n\t"
    "ds_read_b64_tr_b16 a[222:223], v11 offset:29696\n\t"
    "v_cvt_pk_bf16_f32 v75, v86, v87\n\t"
    "v_accvgpr_write_b32 a30, v38\n\t"
    "v_lshlrev_b32 v46, 16, v39\n\t"
    "v_and_b32 v47, 0xffff0000, v39\n\t"
    "v_mul_f32 v46, s56, v46\n\t"
    "v_mul_f32 v47, s56, v47\n\t"
    "v_cvt_pk_bf16_f32 v39, v46, v47\n\t"
    "v_accvgpr_write_b32 a31, v39\n\t"
    "v_mfma_f32_16x16x32_bf16 v[80:83], a[96:99], a[16:19], 0\n\t"
    "v_exp_f32 v88, v88\n\t"
    "v_exp_f32 v89, v89\n\t"
    "v_mfma_f32_16x16x32_bf16 v[80:83], a[100:103], a[20:23], v[80:83]\n\t"
    "v_add_f32 v40, v40, v88\n\t"
    "v_mfma_f32_16x16x32_bf16 v[80:83], a[104:107], a[24:27], v[80:83]\n\t"
    "v_exp_f32 v90, v90\n\t"
    "v_mfma_f32_16x16x32_bf16 v[80:83], a[108:111], a[28:31], v[80:83]\n\t"
    "v_add_f32 v40, v40, v89\n\t"
    "v_mfma_f32_16x16x32_bf16 v[84:87], a[112:115], a[16:19], 0\n\t"
    "v_cvt_pk_bf16_f32 v76, v88, v89\n\t"
    "v_exp_f32 v91, v91\n\t"
    "v_mfma_f32_16x16x32_bf16 v[84:87], a[116:119], a[20:23], v[84:87]\n\t"
    "v_add_f32 v40, v40, v90\n\t"
    "v_mfma_f32_16x16x32_bf16 v[84:87], a[120:123], a[24:27], v[84:87]\n\t"
    "v_exp_f32 v92, v92\n\t"
    "v_mfma_f32_16x16x32_bf16 v[84:87], a[124:127], a[28:31], v[84:87]\n\t"
    "v_add_f32 v40, v40, v91\n\t"
    "v_mfma_f32_16x16x32_bf16 a[224:227], a[160:163], v[72:75], 0\n\t"
    "v_cvt_pk_bf16_f32 v77, v90, v91\n\t"
    "v_exp_f32 v93, v93\n\t"
    "v_mfma_f32_16x16x32_bf16 a[228:231], a[164:167], v[72:75], 0\n\t"
    "v_add_f32 v40, v40, v92\n\t"
    "v_mfma_f32_16x16x32_bf16 a[232:235], a[168:171], v[72:75], 0\n\t"
    "v_exp_f32 v94, v94\n\t"
    "v_max3_f32 v53, v80, v81, v82\n\t"
    "v_max3_f32 v53, v53, v83, v84\n\t"
    "v_max3_f32 v53, v53, v85, v86\n\t"
    "v_mfma_f32_16x16x32_bf16 a[236:239], a[172:175], v[72:75], 0\n\t"
    "v_add_f32 v40, v40, v93\n\t"
    "v_max_f32 v53, v53, v87\n\t"
    "v_mov_b32 v54, v53\n\t"
    "v_mfma_f32_16x16x32_bf16 a[240:243], a[176:179], v[72:75], 0\n\t"
    "v_cvt_pk_bf16_f32 v78, v92, v93\n\t"
    "v_exp_f32 v95, v95\n\t"
    "v_permlane16_swap_b32 v53, v54\n\t"
    "v_max_f32 v53, v53, v54\n\t"
    "v_mfma_f32_16x16x32_bf16 a[244:247], a[180:183], v[72:75], 0\n\t"
    "v_add_f32 v40, v40, v94\n\t"
    "v_mov_b32 v54, v53\n\t"
    "s_nop 1\n\t"
    "v_permlane32_swap_b32 v53, v54\n\t"
    "v_max_f32 v53, v53, v54\n\t"
    "v_mfma_f32_16x16x32_bf16 a[248:251], a[184:187], v[72:75], 0\n\t"
    "v_add_f32 v40, v40, v95\n\t"
    "v_sub_f32 v52, 0, v53\n\t"
    "v_mov_b32 v53, v52\n\t"
    "v_mfma_f32_16x16x32_bf16 a[252:255], a[188:191], v[72:75], 0\n\t"
    "v_cvt_pk_bf16_f32 v79, v94, v95\n\t"
    "v_mov_b32 v54, v52\n\t"
    "v_mov_b32 v55, v52\n\t"
    "s_nop 1\n\t"
    "v_mfma_f32_16x16x32_bf16 v[88:91], a[128:131], a[16:19], v[52:55]\n\t"
    "s_waitcnt vmcnt(8)\n\t"
    "ds_read_b128 v[24:27], v20 offset:32768\n\t"
    "ds_read_b128 v[28:31], v21 offset:32768\n\t"
    "ds_read_b128 v[32:35], v22 offset:32768\n\t"
    "ds_read_b128 v[36:39], v23 offset:32768\n\t"
    "v_add_f32 v80, v52, v80\n\t"
    "v_add_f32 v81, v52, v81\n\t"
    "v_mfma_f32_16x16x32_bf16 v[88:91], a[132:135], a[20:23], v[88:91]\n\t"
    "v_add_f32 v82, v52, v82\n\t"
    "v_add_f32 v83, v52, v83\n\t"
    "v_mfma_f32_16x16x32_bf16 v[88:91], a[136:139], a[24:27], v[88:91]\n\t"
    "s_add_u32 m0, s65, 81920\n\t"
    "v_add_f32 v84, v52, v84\n\t"
    "v_add_f32 v85, v52, v85\n\t"
    "v_mfma_f32_16x16x32_bf16 v[88:91], a[140:143], a[28:31], v[88:91]\n\t"
    "s_add_u32 s58, s66, 81920\n\t"
    "v_add_f32 v86, v52, v86\n\t"
    "s_waitcnt lgkmcnt(3)\n\t"
    "v_lshlrev_b32 v2, 16, v24\n\t"
    "v_and_b32 v3, 0xffff0000, v24\n\t"
    "v_mul_f32 v2, s56, v2\n\t"
    "v_mul_f32 v3, s56, v3\n\t"
    "v_cvt_pk_bf16_f32 v24, v2, v3\n\t"
    "v_accvgpr_write_b32 a32, v24\n\t"
    "v_lshlrev_b32 v46, 16, v25\n\t"
    "v_mfma_f32_16x16x32_bf16 v[92:95], a[144:147], a[16:19], v[52:55]\n\t"
    "buffer_load_dwordx4 v12, s[40:43], s58 offen nt lds\n\t"
    "v_add_f32 v87, v52, v87\n\t"
    "v_exp_f32 v80, v80\n\t"
    "v_and_b32 v47, 0xffff0000, v25\n\t"
    "v_mul_f32 v46, s56, v46\n\t"
    "v_mul_f32 v47, s56, v47\n\t"
    "v_cvt_pk_bf16_f32 v25, v46, v47\n\t"
    "v_accvgpr_write_b32 a33, v25\n\t"
    "v_lshlrev_b32 v2, 16, v26\n\t"
    "v_and_b32 v3, 0xffff0000, v26\n\t"
    "v_mul_f32 v2, s56, v2\n\t"
    "v_mfma_f32_16x16x32_bf16 v[92:95], a[148:151], a[20:23], v[92:95]\n\t"
    "buffer_load_dwordx4 v13, s[40:43], s58 offen offset:1024 nt lds\n\t"
    "v_exp_f32 v81, v81\n\t"
    "v_mov_b32 v41, v80\n\t"
    "v_mul_f32 v3, s56, v3\n\t"
    "v_cvt_pk_bf16_f32 v26, v2, v3\n\t"
    "v_accvgpr_write_b32 a34, v26\n\t"
    "v_lshlrev_b32 v46, 16, v27\n\t"
    "v_and_b32 v47, 0xffff0000, v27\n\t"
    "v_mul_f32 v46, s56, v46\n\t"
    "v_mul_f32 v47, s56, v47\n\t"
    "v_cvt_pk_bf16_f32 v27, v46, v47\n\t"
    "v_mfma_f32_16x16x32_bf16 v[92:95], a[152:155], a[24:27], v[92:95]\n\t"
    "buffer_load_dwordx4 v14, s[40:43], s58 offen offset:2048 nt lds\n\t"
    "v_exp_f32 v82, v82\n\t"
    "v_add_f32 v41, v41, v81\n\t"
    "v_accvgpr_write_b32 a35, v27\n\t"
    "s_waitcnt lgkmcnt(2)\n\t"
    "v_lshlrev_b32 v2, 16, v28\n\t"
    "v_and_b32 v3, 0xffff0000, v28\n\t"
    "v_mul_f32 v2, s56, v2\n\t"
    "v_mul_f32 v3, s56, v3\n\t"
    "v_cvt_pk_bf16_f32 v28, v2, v3\n\t"
    "v_mfma_f32_16x16x32_bf16 v[92:95], a[156:159], a[28:31], v[92:95]\n\t"
    "buffer_load_dwordx4 v15, s[40:43], s58 offen offset:3072 nt lds\n\t"
    "v_cvt_pk_bf16_f32 v72, v80, v81\n\t"
    "v_accvgpr_write_b32 a36, v28\n\t"
    "v_lshlrev_b32 v46, 16, v29\n\t"
    "v_and_b32 v47, 0xffff0000, v29\n\t"
    "v_mul_f32 v46, s56, v46\n\t"
    "v_mul_f32 v47, s56, v47\n\t"
    "v_cvt_pk_bf16_f32 v29, v46, v47\n\t"
    "v_accvgpr_write_b32 a37, v29\n\t"
    "v_lshlrev_b32 v2, 16, v30\n\t"
    "v_mfma_f32_16x16x32_bf16 a[224:227], a[192:195], v[76:79], a[224:227]\n\t"
    "v_exp_f32 v83, v83\n\t"
    "v_add_f32 v41, v41, v82\n\t"
    "v_and_b32 v3, 0xffff0000, v30\n\t"
    "v_mul_f32 v2, s56, v2\n\t"
    "v_mul_f32 v3, s56, v3\n\t"
    "v_cvt_pk_bf16_f32 v30, v2, v3\n\t"
    "v_accvgpr_write_b32 a38, v30\n\t"
    "v_lshlrev_b32 v46, 16, v31\n\t"
    "v_and_b32 v47, 0xffff0000, v31\n\t"
    "v_mul_f32 v46, s56, v46\n\t"
    "v_mfma_f32_16x16x32_bf16 a[228:231], a[196:199], v[76:79], a[228:231]\n\t"
    "v_exp_f32 v84, v84\n\t"
    "v_add_f32 v41, v41, v83\n\t"
    "v_mul_f32 v47, s56, v47\n\t"
    "v_cvt_pk_bf16_f32 v31, v46, v47\n\t"
    "v_accvgpr_write_b32 a39, v31\n\t"
    "s_waitcnt lgkmcnt(1)\n\t"
    "v_lshlrev_b32 v2, 16, v32\n\t"
    "v_and_b32 v3, 0xffff0000, v32\n\t"
    "v_mul_f32 v2, s56, v2\n\t"
    "v_mfma_f32_16x16x32_bf16 a[232:235], a[200:203], v[76:79], a[232:235]\n\t"
    "v_cvt_pk_bf16_f32 v73, v82, v83\n\t"
    "v_exp_f32 v85, v85\n\t"
    "v_mul_f32 v3, s56, v3\n\t"
    "v_cvt_pk_bf16_f32 v32, v2, v3\n\t"
    "v_accvgpr_write_b32 a40, v32\n\t"
    "v_lshlrev_b32 v46, 16, v33\n\t"
    "v_and_b32 v47, 0xffff0000, v33\n\t"
    "v_mul_f32 v46, s56, v46\n\t"
    "v_mul_f32 v47, s56, v47\n\t"
    "v_cvt_pk_bf16_f32 v33, v46, v47\n\t"
    "v_mfma_f32_16x16x32_bf16 a[236:239], a[204:207], v[76:79], a[236:239]\n\t"
    "v_add_f32 v41, v41, v84\n\t"
    "v_accvgpr_write_b32 a41, v33\n\t"
    "v_lshlrev_b32 v2, 16, v34\n\t"
    "v_and_b32 v3, 0xffff0000, v34\n\t"
    "v_mul_f32 v2, s56, v2\n\t"
    "v_mul_f32 v3, s56, v3\n\t"
    "v_cvt_pk_bf16_f32 v34, v2, v3\n\t"
    "v_accvgpr_write_b32 a42, v34\n\t"
    "v_lshlrev_b32 v46, 16, v35\n\t"
    "v_mfma_f32_16x16x32_bf16 a[240:243], a[208:211], v[76:79], a[240:243]\n\t"
    "v_exp_f32 v86, v86\n\t"
    "v_add_f32 v41, v41, v85\n\t"
    "v_and_b32 v47, 0xffff0000, v35\n\t"
    "v_mul_f32 v46, s56, v46\n\t"
    "v_mul_f32 v47, s56, v47\n\t"
    "v_cvt_pk_bf16_f32 v35, v46, v47\n\t"
    "v_accvgpr_write_b32 a43, v35\n\t"
    "s_waitcnt lgkmcnt(0)\n\t"
    "v_lshlrev_b32 v2, 16, v36\n\t"
    "v_mfma_f32_16x16x32_bf16 a[244:247], a[212:215], v[76:79], a[244:247]\n\t"
    "v_cvt_pk_bf16_f32 v74, v84, v85\n\t"
    "v_exp_f32 v87, v87\n\t"
    "v_and_b32 v3, 0xffff0000, v36\n\t"
    "v_mul_f32 v2, s56, v2\n\t"
    "v_mul_f32 v3, s56, v3\n\t"
    "v_cvt_pk_bf16_f32 v36, v2, v3\n\t"
    "v_accvgpr_write_b32 a44, v36\n\t"
    "v_lshlrev_b32 v46, 16, v37\n\t"
    "v_and_b32 v47, 0xffff0000, v37\n\t"
    "v_mul_f32 v46, s56, v46\n\t"
    "v_mfma_f32_16x16x32_bf16 a[248:251], a[216:219], v[76:79], a[248:251]\n\t"
    "v_add_f32 v41, v41, v86\n\t"
    "v_add_f32 v41, v41, v87\n\t"
    "v_mul_f32 v47, s56, v47\n\t"
    "v_cvt_pk_bf16_f32 v37, v46, v47\n\t"
    "v_accvgpr_write_b32 a45, v37\n\t"
    "v_lshlrev_b32 v2, 16, v38\n\t"
    "v_and_b32 v3, 0xffff0000, v38\n\t"
    "v_mul_f32 v2, s56, v2\n\t"
    "v_mul_f32 v3, s56, v3\n\t"
    "v_cvt_pk_bf16_f32 v38, v2, v3\n\t"
    "v_mfma_f32_16x16x32_bf16 a[252:255], a[220:223], v[76:79], a[252:255]\n\t"
    "v_cvt_pk_bf16_f32 v75, v86, v87\n\t"
    "v_accvgpr_write_b32 a46, v38\n\t"
    "v_lshlrev_b32 v46, 16, v39\n\t"
    "v_and_b32 v47, 0xffff0000, v39\n\t"
    "v_mul_f32 v46, s56, v46\n\t"
    "v_mul_f32 v47, s56, v47\n\t"
    "v_cvt_pk_bf16_f32 v39, v46, v47\n\t"
    "v_accvgpr_write_b32 a47, v39\n\t"
    "v_mfma_f32_16x16x32_bf16 v[80:83], a[96:99], a[32:35], 0\n\t"
    "v_exp_f32 v88, v88\n\t"
    "v_exp_f32 v89, v89\n\t"
    "v_mfma_f32_16x16x32_bf16 v[80:83], a[100:103], a[36:39], v[80:83]\n\t"
    "v_add_f32 v41, v41, v88\n\t"
    "v_mfma_f32_16x16x32_bf16 v[80:83], a[104:107], a[40:43], v[80:83]\n\t"
    "v_exp_f32 v90, v90\n\t"
    "v_mfma_f32_16x16x32_bf16 v[80:83], a[108:111], a[44:47], v[80:83]\n\t"
    "v_add_f32 v41, v41, v89\n\t"
    "v_mfma_f32_16x16x32_bf16 v[84:87], a[112:115], a[32:35], 0\n\t"
    "v_cvt_pk_bf16_f32 v76, v88, v89\n\t"
    "v_exp_f32 v91, v91\n\t"
    "v_mfma_f32_16x16x32_bf16 v[84:87], a[116:119], a[36:39], v[84:87]\n\t"
    "v_add_f32 v41, v41, v90\n\t"
    "v_mfma_f32_16x16x32_bf16 v[84:87], a[120:123], a[40:43], v[84:87]\n\t"
    "v_exp_f32 v92, v92\n\t"
    "v_mfma_f32_16x16x32_bf16 v[84:87], a[124:127], a[44:47], v[84:87]\n\t"
    "v_add_f32 v41, v41, v91\n\t"
    "v_mfma_f32_16x16x32_bf16 v[96:99], a[160:163], v[72:75], 0\n\t"
    "v_cvt_pk_bf16_f32 v77, v90, v91\n\t"
    "v_exp_f32 v93, v93\n\t"
    "v_mfma_f32_16x16x32_bf16 v[100:103], a[164:167], v[72:75], 0\n\t"
    "v_add_f32 v41, v41, v92\n\t"
    "v_mfma_f32_16x16x32_bf16 v[104:107], a[168:171], v[72:75], 0\n\t"
    "v_exp_f32 v94, v94\n\t"
    "v_max3_f32 v57, v80, v81, v82\n\t"
    "v_max3_f32 v57, v57, v83, v84\n\t"
    "v_max3_f32 v57, v57, v85, v86\n\t"
    "v_mfma_f32_16x16x32_bf16 v[108:111], a[172:175], v[72:75], 0\n\t"
    "v_add_f32 v41, v41, v93\n\t"
    "v_max_f32 v57, v57, v87\n\t"
    "v_mov_b32 v58, v57\n\t"
    "v_mfma_f32_16x16x32_bf16 v[112:115], a[176:179], v[72:75], 0\n\t"
    "v_cvt_pk_bf16_f32 v78, v92, v93\n\t"
    "v_exp_f32 v95, v95\n\t"
    "v_permlane16_swap_b32 v57, v58\n\t"
    "v_max_f32 v57, v57, v58\n\t"
    "v_mfma_f32_16x16x32_bf16 v[116:119], a[180:183], v[72:75], 0\n\t"
    "v_add_f32 v41, v41, v94\n\t"
    "v_mov_b32 v58, v57\n\t"
    "s_nop 1\n\t"
    "v_permlane32_swap_b32 v57, v58\n\t"
    "v_max_f32 v57, v57, v58\n\t"
    "v_mfma_f32_16x16x32_bf16 v[120:123], a[184:187], v[72:75], 0\n\t"
    "v_add_f32 v41, v41, v95\n\t"
    "v_sub_f32 v56, 0, v57\n\t"
    "v_mov_b32 v57, v56\n\t"
    "v_mfma_f32_16x16x32_bf16 v[124:127], a[188:191], v[72:75], 0\n\t"
    "v_cvt_pk_bf16_f32 v79, v94, v95\n\t"
    "v_mov_b32 v58, v56\n\t"
    "v_mov_b32 v59, v56\n\t"
    "s_nop 1\n\t"
    "v_mfma_f32_16x16x32_bf16 v[88:91], a[128:131], a[32:35], v[56:59]\n\t"
    "s_waitcnt vmcnt(8)\n\t"
    "ds_read_b128 v[24:27], v20 offset:49152\n\t"
    "ds_read_b128 v[28:31], v21 offset:49152\n\t"
    "ds_read_b128 v[32:35], v22 offset:49152\n\t"
    "ds_read_b128 v[36:39], v23 offset:49152\n\t"
    "v_add_f32 v80, v56, v80\n\t"
    "v_add_f32 v81, v56, v81\n\t"
    "v_mfma_f32_16x16x32_bf16 v[88:91], a[132:135], a[36:39], v[88:91]\n\t"
    "v_add_f32 v82, v56, v82\n\t"
    "v_add_f32 v83, v56, v83\n\t"
    "v_mfma_f32_16x16x32_bf16 v[88:91], a[136:139], a[40:43], v[88:91]\n\t"
    "s_add_u32 m0, s61, 32768\n\t"
    "v_add_f32 v84, v56, v84\n\t"
    "v_add_f32 v85, v56, v85\n\t"
    "v_mfma_f32_16x16x32_bf16 v[88:91], a[140:143], a[44:47], v[88:91]\n\t"
    "s_add_u32 s58, s62, 16384\n\t"
    "v_add_f32 v86, v56, v86\n\t"
    "s_waitcnt lgkmcnt(3)\n\t"
    "v_lshlrev_b32 v2, 16, v24\n\t"
    "v_and_b32 v3, 0xffff0000, v24\n\t"
    "v_mul_f32 v2, s56, v2\n\t"
    "v_mul_f32 v3, s56, v3\n\t"
    "v_cvt_pk_bf16_f32 v24, v2, v3\n\t"
    "v_accvgpr_write_b32 a48, v24\n\t"
    "v_lshlrev_b32 v46, 16, v25\n\t"
    "v_mfma_f32_16x16x32_bf16 v[92:95], a[144:147], a[32:35], v[56:59]\n\t"
    "buffer_load_dwordx4 v16, s[44:47], s58 offen nt lds\n\t"
    "v_add_f32 v87, v56, v87\n\t"
    "v_exp_f32 v80, v80\n\t"
    "v_and_b32 v47, 0xffff0000, v25\n\t"
    "v_mul_f32 v46, s56, v46\n\t"
    "v_mul_f32 v47, s56, v47\n\t"
    "v_cvt_pk_bf16_f32 v25, v46, v47\n\t"
    "v_accvgpr_write_b32 a49, v25\n\t"
    "v_lshlrev_b32 v2, 16, v26\n\t"
    "v_and_b32 v3, 0xffff0000, v26\n\t"
    "v_mul_f32 v2, s56, v2\n\t"
    "v_mfma_f32_16x16x32_bf16 v[92:95], a[148:151], a[36:39], v[92:95]\n\t"
    "buffer_load_dwordx4 v17, s[44:47], s58 offen offset:1024 nt lds\n\t"
    "v_exp_f32 v81, v81\n\t"
    "v_mov_b32 v42, v80\n\t"
    "v_mul_f32 v3, s56, v3\n\t"
    "v_cvt_pk_bf16_f32 v26, v2, v3\n\t"
    "v_accvgpr_write_b32 a50, v26\n\t"
    "v_lshlrev_b32 v46, 16, v27\n\t"
    "v_and_b32 v47, 0xffff0000, v27\n\t"
    "v_mul_f32 v46, s56, v46\n\t"
    "v_mul_f32 v47, s56, v47\n\t"
    "v_cvt_pk_bf16_f32 v27, v46, v47\n\t"
    "v_mfma_f32_16x16x32_bf16 v[92:95], a[152:155], a[40:43], v[92:95]\n\t"
    "s_add_u32 m0, s63, 32768\n\t"
    "v_exp_f32 v82, v82\n\t"
    "v_add_f32 v42, v42, v81\n\t"
    "v_accvgpr_write_b32 a51, v27\n\t"
    "s_waitcnt lgkmcnt(2)\n\t"
    "v_lshlrev_b32 v2, 16, v28\n\t"
    "v_and_b32 v3, 0xffff0000, v28\n\t"
    "v_mul_f32 v2, s56, v2\n\t"
    "v_mul_f32 v3, s56, v3\n\t"
    "v_cvt_pk_bf16_f32 v28, v2, v3\n\t"
    "v_mfma_f32_16x16x32_bf16 v[92:95], a[156:159], a[44:47], v[92:95]\n\t"
    "s_add_u32 s59, s64, 16384\n\t"
    "v_cvt_pk_bf16_f32 v72, v80, v81\n\t"
    "v_accvgpr_write_b32 a52, v28\n\t"
    "v_lshlrev_b32 v46, 16, v29\n\t"
    "v_and_b32 v47, 0xffff0000, v29\n\t"
    "v_mul_f32 v46, s56, v46\n\t"
    "v_mul_f32 v47, s56, v47\n\t"
    "v_cvt_pk_bf16_f32 v29, v46, v47\n\t"
    "v_accvgpr_write_b32 a53, v29\n\t"
    "v_lshlrev_b32 v2, 16, v30\n\t"
    "v_mfma_f32_16x16x32_bf16 v[96:99], a[192:195], v[76:79], v[96:99]\n\t"
    "buffer_load_dwordx4 v18, s[48:51], s59 offen nt lds\n\t"
    "v_exp_f32 v83, v83\n\t"
    "v_add_f32 v42, v42, v82\n\t"
    "v_and_b32 v3, 0xffff0000, v30\n\t"
    "v_mul_f32 v2, s56, v2\n\t"
    "v_mul_f32 v3, s56, v3\n\t"
    "v_cvt_pk_bf16_f32 v30, v2, v3\n\t"
    "v_accvgpr_write_b32 a54, v30\n\t"
    "v_lshlrev_b32 v46, 16, v31\n\t"
    "v_and_b32 v47, 0xffff0000, v31\n\t"
    "v_mul_f32 v46, s56, v46\n\t"
    "v_mfma_f32_16x16x32_bf16 v[100:103], a[196:199], v[76:79], v[100:103]\n\t"
    "buffer_load_dwordx4 v18, s[48:51], s59 offen offset:2048 nt lds\n\t"
    "v_exp_f32 v84, v84\n\t"
    "v_add_f32 v42, v42, v83\n\t"
    "v_mul_f32 v47, s56, v47\n\t"
    "v_cvt_pk_bf16_f32 v31, v46, v47\n\t"
    "v_accvgpr_write_b32 a55, v31\n\t"
    "s_waitcnt lgkmcnt(1)\n\t"
    "v_lshlrev_b32 v2, 16, v32\n\t"
    "v_and_b32 v3, 0xffff0000, v32\n\t"
    "v_mul_f32 v2, s56, v2\n\t"
    "v_mfma_f32_16x16x32_bf16 v[104:107], a[200:203], v[76:79], v[104:107]\n\t"
    "v_cvt_pk_bf16_f32 v73, v82, v83\n\t"
    "v_exp_f32 v85, v85\n\t"
    "v_mul_f32 v3, s56, v3\n\t"
    "v_cvt_pk_bf16_f32 v32, v2, v3\n\t"
    "v_accvgpr_write_b32 a56, v32\n\t"
    "v_lshlrev_b32 v46, 16, v33\n\t"
    "v_and_b32 v47, 0xffff0000, v33\n\t"
    "v_mul_f32 v46, s56, v46\n\t"
    "v_mul_f32 v47, s56, v47\n\t"
    "v_cvt_pk_bf16_f32 v33, v46, v47\n\t"
    "v_mfma_f32_16x16x32_bf16 v[108:111], a[204:207], v[76:79], v[108:111]\n\t"
    "v_add_f32 v42, v42, v84\n\t"
    "v_accvgpr_write_b32 a57, v33\n\t"
    "v_lshlrev_b32 v2, 16, v34\n\t"
    "v_and_b32 v3, 0xffff0000, v34\n\t"
    "v_mul_f32 v2, s56, v2\n\t"
    "v_mul_f32 v3, s56, v3\n\t"
    "v_cvt_pk_bf16_f32 v34, v2, v3\n\t"
    "v_accvgpr_write_b32 a58, v34\n\t"
    "v_lshlrev_b32 v46, 16, v35\n\t"
    "v_mfma_f32_16x16x32_bf16 v[112:115], a[208:211], v[76:79], v[112:115]\n\t"
    "v_exp_f32 v86, v86\n\t"
    "v_add_f32 v42, v42, v85\n\t"
    "v_and_b32 v47, 0xffff0000, v35\n\t"
    "v_mul_f32 v46, s56, v46\n\t"
    "v_mul_f32 v47, s56, v47\n\t"
    "v_cvt_pk_bf16_f32 v35, v46, v47\n\t"
    "v_accvgpr_write_b32 a59, v35\n\t"
    "s_waitcnt lgkmcnt(0)\n\t"
    "v_lshlrev_b32 v2, 16, v36\n\t"
    "v_mfma_f32_16x16x32_bf16 v[116:119], a[212:215], v[76:79], v[116:119]\n\t"
    "v_cvt_pk_bf16_f32 v74, v84, v85\n\t"
    "v_exp_f32 v87, v87\n\t"
    "v_and_b32 v3, 0xffff0000, v36\n\t"
    "v_mul_f32 v2, s56, v2\n\t"
    "v_mul_f32 v3, s56, v3\n\t"
    "v_cvt_pk_bf16_f32 v36, v2, v3\n\t"
    "v_accvgpr_write_b32 a60, v36\n\t"
    "v_lshlrev_b32 v46, 16, v37\n\t"
    "v_and_b32 v47, 0xffff0000, v37\n\t"
    "v_mul_f32 v46, s56, v46\n\t"
    "v_mfma_f32_16x16x32_bf16 v[120:123], a[216:219], v[76:79], v[120:123]\n\t"
    "v_add_f32 v42, v42, v86\n\t"
    "v_add_f32 v42, v42, v87\n\t"
    "v_mul_f32 v47, s56, v47\n\t"
    "v_cvt_pk_bf16_f32 v37, v46, v47\n\t"
    "v_accvgpr_write_b32 a61, v37\n\t"
    "v_lshlrev_b32 v2, 16, v38\n\t"
    "v_and_b32 v3, 0xffff0000, v38\n\t"
    "v_mul_f32 v2, s56, v2\n\t"
    "v_mul_f32 v3, s56, v3\n\t"
    "v_cvt_pk_bf16_f32 v38, v2, v3\n\t"
    "v_mfma_f32_16x16x32_bf16 v[124:127], a[220:223], v[76:79], v[124:127]\n\t"
    "v_cvt_pk_bf16_f32 v75, v86, v87\n\t"
    "v_accvgpr_write_b32 a62, v38\n\t"
    "v_lshlrev_b32 v46, 16, v39\n\t"
    "v_and_b32 v47, 0xffff0000, v39\n\t"
    "v_mul_f32 v46, s56, v46\n\t"
    "v_mul_f32 v47, s56, v47\n\t"
    "v_cvt_pk_bf16_f32 v39, v46, v47\n\t"
    "v_accvgpr_write_b32 a63, v39\n\t"
    "v_mfma_f32_16x16x32_bf16 v[80:83], a[96:99], a[48:51], 0\n\t"
    "v_exp_f32 v88, v88\n\t"
    "v_exp_f32 v89, v89\n\t"
    "v_mfma_f32_16x16x32_bf16 v[80:83], a[100:103], a[52:55], v[80:83]\n\t"
    "v_add_f32 v42, v42, v88\n\t"
    "v_mfma_f32_16x16x32_bf16 v[80:83], a[104:107], a[56:59], v[80:83]\n\t"
    "v_exp_f32 v90, v90\n\t"
    "v_mfma_f32_16x16x32_bf16 v[80:83], a[108:111], a[60:63], v[80:83]\n\t"
    "v_add_f32 v42, v42, v89\n\t"
    "v_mfma_f32_16x16x32_bf16 v[84:87], a[112:115], a[48:51], 0\n\t"
    "v_cvt_pk_bf16_f32 v76, v88, v89\n\t"
    "v_exp_f32 v91, v91\n\t"
    "v_mfma_f32_16x16x32_bf16 v[84:87], a[116:119], a[52:55], v[84:87]\n\t"
    "v_add_f32 v42, v42, v90\n\t"
    "v_mfma_f32_16x16x32_bf16 v[84:87], a[120:123], a[56:59], v[84:87]\n\t"
    "v_exp_f32 v92, v92\n\t"
    "v_mfma_f32_16x16x32_bf16 v[84:87], a[124:127], a[60:63], v[84:87]\n\t"
    "v_add_f32 v42, v42, v91\n\t"
    "v_mfma_f32_16x16x32_bf16 v[128:131], a[160:163], v[72:75], 0\n\t"
    "v_cvt_pk_bf16_f32 v77, v90, v91\n\t"
    "v_exp_f32 v93, v93\n\t"
    "v_mfma_f32_16x16x32_bf16 v[132:135], a[164:167], v[72:75], 0\n\t"
    "v_add_f32 v42, v42, v92\n\t"
    "v_mfma_f32_16x16x32_bf16 v[136:139], a[168:171], v[72:75], 0\n\t"
    "v_exp_f32 v94, v94\n\t"
    "v_max3_f32 v61, v80, v81, v82\n\t"
    "v_max3_f32 v61, v61, v83, v84\n\t"
    "v_max3_f32 v61, v61, v85, v86\n\t"
    "v_mfma_f32_16x16x32_bf16 v[140:143], a[172:175], v[72:75], 0\n\t"
    "v_add_f32 v42, v42, v93\n\t"
    "v_max_f32 v61, v61, v87\n\t"
    "v_mov_b32 v62, v61\n\t"
    "v_mfma_f32_16x16x32_bf16 v[144:147], a[176:179], v[72:75], 0\n\t"
    "v_cvt_pk_bf16_f32 v78, v92, v93\n\t"
    "v_exp_f32 v95, v95\n\t"
    "v_permlane16_swap_b32 v61, v62\n\t"
    "v_max_f32 v61, v61, v62\n\t"
    "v_mfma_f32_16x16x32_bf16 v[148:151], a[180:183], v[72:75], 0\n\t"
    "v_add_f32 v42, v42, v94\n\t"
    "v_mov_b32 v62, v61\n\t"
    "s_nop 1\n\t"
    "v_permlane32_swap_b32 v61, v62\n\t"
    "v_max_f32 v61, v61, v62\n\t"
    "v_mfma_f32_16x16x32_bf16 v[152:155], a[184:187], v[72:75], 0\n\t"
    "v_add_f32 v42, v42, v95\n\t"
    "v_sub_f32 v60, 0, v61\n\t"
    "v_mov_b32 v61, v60\n\t"
    "v_mfma_f32_16x16x32_bf16 v[156:159], a[188:191], v[72:75], 0\n\t"
    "v_cvt_pk_bf16_f32 v79, v94, v95\n\t"
    "v_mov_b32 v62, v60\n\t"
    "v_mov_b32 v63, v60\n\t"
    "s_nop 1\n\t"
    "v_mfma_f32_16x16x32_bf16 v[88:91], a[128:131], a[48:51], v[60:63]\n\t"
    "s_waitcnt vmcnt(8)\n\t"
    "v_add_u32 v20, 0x10000, v20\n\t"
    "v_add_u32 v21, 0x10000, v21\n\t"
    "v_add_u32 v22, 0x10000, v22\n\t"
    "v_add_u32 v23, 0x10000, v23\n\t"
    "ds_read_b128 v[24:27], v20 offset:0\n\t"
    "v_add_f32 v80, v60, v80\n\t"
    "v_add_f32 v81, v60, v81\n\t"
    "v_mfma_f32_16x16x32_bf16 v[88:91], a[132:135], a[52:55], v[88:91]\n\t"
    "ds_read_b128 v[28:31], v21 offset:0\n\t"
    "ds_read_b128 v[32:35], v22 offset:0\n\t"
    "ds_read_b128 v[36:39], v23 offset:0\n\t"
    "v_add_f32 v82, v60, v82\n\t"
    "v_add_f32 v83, v60, v83\n\t"
    "v_mfma_f32_16x16x32_bf16 v[88:91], a[136:139], a[56:59], v[88:91]\n\t"
    "v_add_f32 v84, v60, v84\n\t"
    "v_add_f32 v85, v60, v85\n\t"
    "v_mfma_f32_16x16x32_bf16 v[88:91], a[140:143], a[60:63], v[88:91]\n\t"
    "v_add_f32 v86, v60, v86\n\t"
    "s_waitcnt lgkmcnt(3)\n\t"
    "v_lshlrev_b32 v2, 16, v24\n\t"
    "v_and_b32 v3, 0xffff0000, v24\n\t"
    "v_mul_f32 v2, s56, v2\n\t"
    "v_mul_f32 v3, s56, v3\n\t"
    "v_cvt_pk_bf16_f32 v24, v2, v3\n\t"
    "v_accvgpr_write_b32 a64, v24\n\t"
    "v_lshlrev_b32 v46, 16, v25\n\t"
    "v_mfma_f32_16x16x32_bf16 v[92:95], a[144:147], a[48:51], v[60:63]\n\t"
    "v_add_f32 v87, v60, v87\n\t"
    "v_exp_f32 v80, v80\n\t"
    "v_and_b32 v47, 0xffff0000, v25\n\t"
    "v_mul_f32 v46, s56, v46\n\t"
    "v_mul_f32 v47, s56, v47\n\t"
    "v_cvt_pk_bf16_f32 v25, v46, v47\n\t"
    "v_accvgpr_write_b32 a65, v25\n\t"
    "v_lshlrev_b32 v2, 16, v26\n\t"
    "v_and_b32 v3, 0xffff0000, v26\n\t"
    "v_mul_f32 v2, s56, v2\n\t"
    "v_mfma_f32_16x16x32_bf16 v[92:95], a[148:151], a[52:55], v[92:95]\n\t"
    "v_exp_f32 v81, v81\n\t"
    "v_mov_b32 v43, v80\n\t"
    "v_mul_f32 v3, s56, v3\n\t"
    "v_cvt_pk_bf16_f32 v26, v2, v3\n\t"
    "v_accvgpr_write_b32 a66, v26\n\t"
    "v_lshlrev_b32 v46, 16, v27\n\t"
    "v_and_b32 v47, 0xffff0000, v27\n\t"
    "v_mul_f32 v46, s56, v46\n\t"
    "v_mul_f32 v47, s56, v47\n\t"
    "v_cvt_pk_bf16_f32 v27, v46, v47\n\t"
    "v_mfma_f32_16x16x32_bf16 v[92:95], a[152:155], a[56:59], v[92:95]\n\t"
    "v_exp_f32 v82, v82\n\t"
    "v_add_f32 v43, v43, v81\n\t"
    "v_accvgpr_write_b32 a67, v27\n\t"
    "s_waitcnt lgkmcnt(2)\n\t"
    "v_lshlrev_b32 v2, 16, v28\n\t"
    "v_and_b32 v3, 0xffff0000, v28\n\t"
    "v_mul_f32 v2, s56, v2\n\t"
    "v_mul_f32 v3, s56, v3\n\t"
    "v_cvt_pk_bf16_f32 v28, v2, v3\n\t"
    "v_mfma_f32_16x16x32_bf16 v[92:95], a[156:159], a[60:63], v[92:95]\n\t"
    "v_cvt_pk_bf16_f32 v72, v80, v81\n\t"
    "v_accvgpr_write_b32 a68, v28\n\t"
    "v_lshlrev_b32 v46, 16, v29\n\t"
    "v_and_b32 v47, 0xffff0000, v29\n\t"
    "v_mul_f32 v46, s56, v46\n\t"
    "v_mul_f32 v47, s56, v47\n\t"
    "v_cvt_pk_bf16_f32 v29, v46, v47\n\t"
    "v_accvgpr_write_b32 a69, v29\n\t"
    "v_lshlrev_b32 v2, 16, v30\n\t"
    "v_mfma_f32_16x16x32_bf16 v[128:131], a[192:195], v[76:79], v[128:131]\n\t"
    "v_exp_f32 v83, v83\n\t"
    "v_add_f32 v43, v43, v82\n\t"
    "v_and_b32 v3, 0xffff0000, v30\n\t"
    "v_mul_f32 v2, s56, v2\n\t"
    "v_mul_f32 v3, s56, v3\n\t"
    "v_cvt_pk_bf16_f32 v30, v2, v3\n\t"
    "v_accvgpr_write_b32 a70, v30\n\t"
    "v_lshlrev_b32 v46, 16, v31\n\t"
    "v_and_b32 v47, 0xffff0000, v31\n\t"
    "v_mul_f32 v46, s56, v46\n\t"
    "v_mfma_f32_16x16x32_bf16 v[132:135], a[196:199], v[76:79], v[132:135]\n\t"
    "v_exp_f32 v84, v84\n\t"
    "v_add_f32 v43, v43, v83\n\t"
    "v_mul_f32 v47, s56, v47\n\t"
    "v_cvt_pk_bf16_f32 v31, v46, v47\n\t"
    "v_accvgpr_write_b32 a71, v31\n\t"
    "s_waitcnt lgkmcnt(1)\n\t"
    "v_lshlrev_b32 v2, 16, v32\n\t"
    "v_and_b32 v3, 0xffff0000, v32\n\t"
    "v_mul_f32 v2, s56, v2\n\t"
    "v_mfma_f32_16x16x32_bf16 v[136:139], a[200:203], v[76:79], v[136:139]\n\t"
    "v_cvt_pk_bf16_f32 v73, v82, v83\n\t"
    "v_exp_f32 v85, v85\n\t"
    "v_mul_f32 v3, s56, v3\n\t"
    "v_cvt_pk_bf16_f32 v32, v2, v3\n\t"
    "v_accvgpr_write_b32 a72, v32\n\t"
    "v_lshlrev_b32 v46, 16, v33\n\t"
    "v_and_b32 v47, 0xffff0000, v33\n\t"
    "v_mul_f32 v46, s56, v46\n\t"
    "v_mul_f32 v47, s56, v47\n\t"
    "v_cvt_pk_bf16_f32 v33, v46, v47\n\t"
    "v_mfma_f32_16x16x32_bf16 v[140:143], a[204:207], v[76:79], v[140:143]\n\t"
    "v_add_f32 v43, v43, v84\n\t"
    "v_accvgpr_write_b32 a73, v33\n\t"
    "v_lshlrev_b32 v2, 16, v34\n\t"
    "v_and_b32 v3, 0xffff0000, v34\n\t"
    "v_mul_f32 v2, s56, v2\n\t"
    "v_mul_f32 v3, s56, v3\n\t"
    "v_cvt_pk_bf16_f32 v34, v2, v3\n\t"
    "v_accvgpr_write_b32 a74, v34\n\t"
    "v_lshlrev_b32 v46, 16, v35\n\t"
    "v_mfma_f32_16x16x32_bf16 v[144:147], a[208:211], v[76:79], v[144:147]\n\t"
    "v_exp_f32 v86, v86\n\t"
    "v_add_f32 v43, v43, v85\n\t"
    "v_and_b32 v47, 0xffff0000, v35\n\t"
    "v_mul_f32 v46, s56, v46\n\t"
    "v_mul_f32 v47, s56, v47\n\t"
    "v_cvt_pk_bf16_f32 v35, v46, v47\n\t"
    "v_accvgpr_write_b32 a75, v35\n\t"
    "s_waitcnt lgkmcnt(0)\n\t"
    "v_lshlrev_b32 v2, 16, v36\n\t"
    "v_mfma_f32_16x16x32_bf16 v[148:151], a[212:215], v[76:79], v[148:151]\n\t"
    "v_cvt_pk_bf16_f32 v74, v84, v85\n\t"
    "v_exp_f32 v87, v87\n\t"
    "v_and_b32 v3, 0xffff0000, v36\n\t"
    "v_mul_f32 v2, s56, v2\n\t"
    "v_mul_f32 v3, s56, v3\n\t"
    "v_cvt_pk_bf16_f32 v36, v2, v3\n\t"
    "v_accvgpr_write_b32 a76, v36\n\t"
    "v_lshlrev_b32 v46, 16, v37\n\t"
    "v_and_b32 v47, 0xffff0000, v37\n\t"
    "v_mul_f32 v46, s56, v46\n\t"
    "v_mfma_f32_16x16x32_bf16 v[152:155], a[216:219], v[76:79], v[152:155]\n\t"
    "v_add_f32 v43, v43, v86\n\t"
    "v_add_f32 v43, v43, v87\n\t"
    "v_mul_f32 v47, s56, v47\n\t"
    "v_cvt_pk_bf16_f32 v37, v46, v47\n\t"
    "v_accvgpr_write_b32 a77, v37\n\t"
    "v_lshlrev_b32 v2, 16, v38\n\t"
    "v_and_b32 v3, 0xffff0000, v38\n\t"
    "v_mul_f32 v2, s56, v2\n\t"
    "v_mul_f32 v3, s56, v3\n\t"
    "v_cvt_pk_bf16_f32 v38, v2, v3\n\t"
    "v_mfma_f32_16x16x32_bf16 v[156:159], a[220:223], v[76:79], v[156:159]\n\t"
    "v_cvt_pk_bf16_f32 v75, v86, v87\n\t"
    "v_accvgpr_write_b32 a78, v38\n\t"
    "v_lshlrev_b32 v46, 16, v39\n\t"
    "v_and_b32 v47, 0xffff0000, v39\n\t"
    "v_mul_f32 v46, s56, v46\n\t"
    "v_mul_f32 v47, s56, v47\n\t"
    "v_cvt_pk_bf16_f32 v39, v46, v47\n\t"
    "v_accvgpr_write_b32 a79, v39\n\t"
    "v_mfma_f32_16x16x32_bf16 v[80:83], a[96:99], a[64:67], 0\n\t"
    "v_exp_f32 v88, v88\n\t"
    "v_exp_f32 v89, v89\n\t"
    "v_mfma_f32_16x16x32_bf16 v[80:83], a[100:103], a[68:71], v[80:83]\n\t"
    "v_add_f32 v43, v43, v88\n\t"
    "v_mfma_f32_16x16x32_bf16 v[80:83], a[104:107], a[72:75], v[80:83]\n\t"
    "v_exp_f32 v90, v90\n\t"
    "v_mfma_f32_16x16x32_bf16 v[80:83], a[108:111], a[76:79], v[80:83]\n\t"
    "v_add_f32 v43, v43, v89\n\t"
    "v_mfma_f32_16x16x32_bf16 v[84:87], a[112:115], a[64:67], 0\n\t"
    "v_cvt_pk_bf16_f32 v76, v88, v89\n\t"
    "v_exp_f32 v91, v91\n\t"
    "v_mfma_f32_16x16x32_bf16 v[84:87], a[116:119], a[68:71], v[84:87]\n\t"
    "v_add_f32 v43, v43, v90\n\t"
    "v_mfma_f32_16x16x32_bf16 v[84:87], a[120:123], a[72:75], v[84:87]\n\t"
    "v_exp_f32 v92, v92\n\t"
    "v_mfma_f32_16x16x32_bf16 v[84:87], a[124:127], a[76:79], v[84:87]\n\t"
    "v_add_f32 v43, v43, v91\n\t"
    "v_mfma_f32_16x16x32_bf16 v[160:163], a[160:163], v[72:75], 0\n\t"
    "v_cvt_pk_bf16_f32 v77, v90, v91\n\t"
    "v_exp_f32 v93, v93\n\t"
    "v_mfma_f32_16x16x32_bf16 v[164:167], a[164:167], v[72:75], 0\n\t"
    "v_add_f32 v43, v43, v92\n\t"
    "v_mfma_f32_16x16x32_bf16 v[168:171], a[168:171], v[72:75], 0\n\t"
    "v_exp_f32 v94, v94\n\t"
    "v_max3_f32 v65, v80, v81, v82\n\t"
    "v_max3_f32 v65, v65, v83, v84\n\t"
    "v_max3_f32 v65, v65, v85, v86\n\t"
    "v_mfma_f32_16x16x32_bf16 v[172:175], a[172:175], v[72:75], 0\n\t"
    "v_add_f32 v43, v43, v93\n\t"
    "v_max_f32 v65, v65, v87\n\t"
    "v_mov_b32 v66, v65\n\t"
    "v_mfma_f32_16x16x32_bf16 v[176:179], a[176:179], v[72:75], 0\n\t"
    "v_cvt_pk_bf16_f32 v78, v92, v93\n\t"
    "v_exp_f32 v95, v95\n\t"
    "v_permlane16_swap_b32 v65, v66\n\t"
    "v_max_f32 v65, v65, v66\n\t"
    "v_mfma_f32_16x16x32_bf16 v[180:183], a[180:183], v[72:75], 0\n\t"
    "v_add_f32 v43, v43, v94\n\t"
    "v_mov_b32 v66, v65\n\t"
    "s_nop 1\n\t"
    "v_permlane32_swap_b32 v65, v66\n\t"
    "v_max_f32 v65, v65, v66\n\t"
    "v_mfma_f32_16x16x32_bf16 v[184:187], a[184:187], v[72:75], 0\n\t"
    "v_add_f32 v43, v43, v95\n\t"
    "v_sub_f32 v64, 0, v65\n\t"
    "v_mov_b32 v65, v64\n\t"
    "v_mfma_f32_16x16x32_bf16 v[188:191], a[188:191], v[72:75], 0\n\t"
    "v_cvt_pk_bf16_f32 v79, v94, v95\n\t"
    "v_mov_b32 v66, v64\n\t"
    "v_mov_b32 v67, v64\n\t"
    "s_nop 1\n\t"
    "v_mfma_f32_16x16x32_bf16 v[88:91], a[128:131], a[64:67], v[64:67]\n\t"
    "s_waitcnt vmcnt(4)\n\t"
    "ds_read_b128 v[24:27], v20 offset:16384\n\t"
    "ds_read_b128 v[28:31], v21 offset:16384\n\t"
    "ds_read_b128 v[32:35], v22 offset:16384\n\t"
    "ds_read_b128 v[36:39], v23 offset:16384\n\t"
    "s_add_u32 m0, s61, 49152\n\t"
    "v_add_f32 v80, v64, v80\n\t"
    "v_add_f32 v81, v64, v81\n\t"
    "v_mfma_f32_16x16x32_bf16 v[88:91], a[132:135], a[68:71], v[88:91]\n\t"
    "s_add_u32 s58, s62, 24576\n\t"
    "v_add_f32 v82, v64, v82\n\t"
    "v_add_f32 v83, v64, v83\n\t"
    "v_mfma_f32_16x16x32_bf16 v[88:91], a[136:139], a[72:75], v[88:91]\n\t"
    "buffer_load_dwordx4 v16, s[44:47], s58 offen nt lds\n\t"
    "v_add_f32 v84, v64, v84\n\t"
    "v_add_f32 v85, v64, v85\n\t"
    "v_mfma_f32_16x16x32_bf16 v[88:91], a[140:143], a[76:79], v[88:91]\n\t"
    "buffer_load_dwordx4 v17, s[44:47], s58 offen offset:1024 nt lds\n\t"
    "v_add_f32 v86, v64, v86\n\t"
    "s_waitcnt lgkmcnt(3)\n\t"
    "v_lshlrev_b32 v2, 16, v24\n\t"
    "v_and_b32 v3, 0xffff0000, v24\n\t"
    "v_mul_f32 v2, s56, v2\n\t"
    "v_mul_f32 v3, s56, v3\n\t"
    "v_cvt_pk_bf16_f32 v24, v2, v3\n\t"
    "v_accvgpr_write_b32 a80, v24\n\t"
    "v_lshlrev_b32 v46, 16, v25\n\t"
    "v_mfma_f32_16x16x32_bf16 v[92:95], a[144:147], a[64:67], v[64:67]\n\t"
    "s_add_u32 m0, s63, 49152\n\t"
    "v_add_f32 v87, v64, v87\n\t"
    "v_exp_f32 v80, v80\n\t"
    "v_and_b32 v47, 0xffff0000, v25\n\t"
    "v_mul_f32 v46, s56, v46\n\t"
    "v_mul_f32 v47, s56, v47\n\t"
    "v_cvt_pk_bf16_f32 v25, v46, v47\n\t"
    "v_accvgpr_write_b32 a81, v25\n\t"
    "v_lshlrev_b32 v2, 16, v26\n\t"
    "v_and_b32 v3, 0xffff0000, v26\n\t"
    "v_mul_f32 v2, s56, v2\n\t"
    "v_mfma_f32_16x16x32_bf16 v[92:95], a[148:151], a[68:71], v[92:95]\n\t"
    "s_add_u32 s59, s64, 24576\n\t"
    "v_exp_f32 v81, v81\n\t"
    "v_mov_b32 v44, v80\n\t"
    "v_mul_f32 v3, s56, v3\n\t"
    "v_cvt_pk_bf16_f32 v26, v2, v3\n\t"
    "v_accvgpr_write_b32 a82, v26\n\t"
    "v_lshlrev_b32 v46, 16, v27\n\t"
    "v_and_b32 v47, 0xffff0000, v27\n\t"
    "v_mul_f32 v46, s56, v46\n\t"
    "v_mul_f32 v47, s56, v47\n\t"
    "v_cvt_pk_bf16_f32 v27, v46, v47\n\t"
    "v_mfma_f32_16x16x32_bf16 v[92:95], a[152:155], a[72:75], v[92:95]\n\t"
    "buffer_load_dwordx4 v18, s[48:51], s59 offen nt lds\n\t"
    "v_exp_f32 v82, v82\n\t"
    "v_add_f32 v44, v44, v81\n\t"
    "v_accvgpr_write_b32 a83, v27\n\t"
    "s_waitcnt lgkmcnt(2)\n\t"
    "v_lshlrev_b32 v2, 16, v28\n\t"
    "v_and_b32 v3, 0xffff0000, v28\n\t"
    "v_mul_f32 v2, s56, v2\n\t"
    "v_mul_f32 v3, s56, v3\n\t"
    "v_cvt_pk_bf16_f32 v28, v2, v3\n\t"
    "v_mfma_f32_16x16x32_bf16 v[92:95], a[156:159], a[76:79], v[92:95]\n\t"
    "buffer_load_dwordx4 v18, s[48:51], s59 offen offset:2048 nt lds\n\t"
    "v_cvt_pk_bf16_f32 v72, v80, v81\n\t"
    "v_accvgpr_write_b32 a84, v28\n\t"
    "v_lshlrev_b32 v46, 16, v29\n\t"
    "v_and_b32 v47, 0xffff0000, v29\n\t"
    "v_mul_f32 v46, s56, v46\n\t"
    "v_mul_f32 v47, s56, v47\n\t"
    "v_cvt_pk_bf16_f32 v29, v46, v47\n\t"
    "v_accvgpr_write_b32 a85, v29\n\t"
    "v_lshlrev_b32 v2, 16, v30\n\t"
    "v_mfma_f32_16x16x32_bf16 v[160:163], a[192:195], v[76:79], v[160:163]\n\t"
    "v_exp_f32 v83, v83\n\t"
    "v_add_f32 v44, v44, v82\n\t"
    "v_and_b32 v3, 0xffff0000, v30\n\t"
    "v_mul_f32 v2, s56, v2\n\t"
    "v_mul_f32 v3, s56, v3\n\t"
    "v_cvt_pk_bf16_f32 v30, v2, v3\n\t"
    "v_accvgpr_write_b32 a86, v30\n\t"
    "v_lshlrev_b32 v46, 16, v31\n\t"
    "v_and_b32 v47, 0xffff0000, v31\n\t"
    "v_mul_f32 v46, s56, v46\n\t"
    "v_mfma_f32_16x16x32_bf16 v[164:167], a[196:199], v[76:79], v[164:167]\n\t"
    "v_exp_f32 v84, v84\n\t"
    "v_add_f32 v44, v44, v83\n\t"
    "v_mul_f32 v47, s56, v47\n\t"
    "v_cvt_pk_bf16_f32 v31, v46, v47\n\t"
    "v_accvgpr_write_b32 a87, v31\n\t"
    "s_waitcnt lgkmcnt(1)\n\t"
    "v_lshlrev_b32 v2, 16, v32\n\t"
    "v_and_b32 v3, 0xffff0000, v32\n\t"
    "v_mul_f32 v2, s56, v2\n\t"
    "v_mfma_f32_16x16x32_bf16 v[168:171], a[200:203], v[76:79], v[168:171]\n\t"
    "v_cvt_pk_bf16_f32 v73, v82, v83\n\t"
    "v_exp_f32 v85, v85\n\t"
    "v_mul_f32 v3, s56, v3\n\t"
    "v_cvt_pk_bf16_f32 v32, v2, v3\n\t"
    "v_accvgpr_write_b32 a88, v32\n\t"
    "v_lshlrev_b32 v46, 16, v33\n\t"
    "v_and_b32 v47, 0xffff0000, v33\n\t"
    "v_mul_f32 v46, s56, v46\n\t"
    "v_mul_f32 v47, s56, v47\n\t"
    "v_cvt_pk_bf16_f32 v33, v46, v47\n\t"
    "v_mfma_f32_16x16x32_bf16 v[172:175], a[204:207], v[76:79], v[172:175]\n\t"
    "v_add_f32 v44, v44, v84\n\t"
    "v_accvgpr_write_b32 a89, v33\n\t"
    "v_lshlrev_b32 v2, 16, v34\n\t"
    "v_and_b32 v3, 0xffff0000, v34\n\t"
    "v_mul_f32 v2, s56, v2\n\t"
    "v_mul_f32 v3, s56, v3\n\t"
    "v_cvt_pk_bf16_f32 v34, v2, v3\n\t"
    "v_accvgpr_write_b32 a90, v34\n\t"
    "v_lshlrev_b32 v46, 16, v35\n\t"
    "v_mfma_f32_16x16x32_bf16 v[176:179], a[208:211], v[76:79], v[176:179]\n\t"
    "v_exp_f32 v86, v86\n\t"
    "v_add_f32 v44, v44, v85\n\t"
    "v_and_b32 v47, 0xffff0000, v35\n\t"
    "v_mul_f32 v46, s56, v46\n\t"
    "v_mul_f32 v47, s56, v47\n\t"
    "v_cvt_pk_bf16_f32 v35, v46, v47\n\t"
    "v_accvgpr_write_b32 a91, v35\n\t"
    "s_waitcnt lgkmcnt(0)\n\t"
    "v_lshlrev_b32 v2, 16, v36\n\t"
    "v_mfma_f32_16x16x32_bf16 v[180:183], a[212:215], v[76:79], v[180:183]\n\t"
    "v_cvt_pk_bf16_f32 v74, v84, v85\n\t"
    "v_exp_f32 v87, v87\n\t"
    "v_and_b32 v3, 0xffff0000, v36\n\t"
    "v_mul_f32 v2, s56, v2\n\t"
    "v_mul_f32 v3, s56, v3\n\t"
    "v_cvt_pk_bf16_f32 v36, v2, v3\n\t"
    "v_accvgpr_write_b32 a92, v36\n\t"
    "v_lshlrev_b32 v46, 16, v37\n\t"
    "v_and_b32 v47, 0xffff0000, v37\n\t"
    "v_mul_f32 v46, s56, v46\n\t"
    "v_mfma_f32_16x16x32_bf16 v[184:187], a[216:219], v[76:79], v[184:187]\n\t"
    "v_add_f32 v44, v44, v86\n\t"
    "v_add_f32 v44, v44, v87\n\t"
    "v_mul_f32 v47, s56, v47\n\t"
    "v_cvt_pk_bf16_f32 v37, v46, v47\n\t"
    "v_accvgpr_write_b32 a93, v37\n\t"
    "v_lshlrev_b32 v2, 16, v38\n\t"
    "v_and_b32 v3, 0xffff0000, v38\n\t"
    "v_mul_f32 v2, s56, v2\n\t"
    "v_mul_f32 v3, s56, v3\n\t"
    "v_cvt_pk_bf16_f32 v38, v2, v3\n\t"
    "v_mfma_f32_16x16x32_bf16 v[188:191], a[220:223], v[76:79], v[188:191]\n\t"
    "v_cvt_pk_bf16_f32 v75, v86, v87\n\t"
    "v_accvgpr_write_b32 a94, v38\n\t"
    "v_lshlrev_b32 v46, 16, v39\n\t"
    "v_and_b32 v47, 0xffff0000, v39\n\t"
    "v_mul_f32 v46, s56, v46\n\t"
    "v_mul_f32 v47, s56, v47\n\t"
    "v_cvt_pk_bf16_f32 v39, v46, v47\n\t"
    "v_accvgpr_write_b32 a95, v39\n\t"
    "v_mfma_f32_16x16x32_bf16 v[80:83], a[96:99], a[80:83], 0\n\t"
    "v_exp_f32 v88, v88\n\t"
    "v_exp_f32 v89, v89\n\t"
    "v_mfma_f32_16x16x32_bf16 v[80:83], a[100:103], a[84:87], v[80:83]\n\t"
    "v_add_f32 v44, v44, v88\n\t"
    "v_mfma_f32_16x16x32_bf16 v[80:83], a[104:107], a[88:91], v[80:83]\n\t"
    "v_exp_f32 v90, v90\n\t"
    "v_mfma_f32_16x16x32_bf16 v[80:83], a[108:111], a[92:95], v[80:83]\n\t"
    "v_add_f32 v44, v44, v89\n\t"
    "v_mfma_f32_16x16x32_bf16 v[84:87], a[112:115], a[80:83], 0\n\t"
    "v_cvt_pk_bf16_f32 v76, v88, v89\n\t"
    "v_exp_f32 v91, v91\n\t"
    "v_mfma_f32_16x16x32_bf16 v[84:87], a[116:119], a[84:87], v[84:87]\n\t"
    "v_add_f32 v44, v44, v90\n\t"
    "v_mfma_f32_16x16x32_bf16 v[84:87], a[120:123], a[88:91], v[84:87]\n\t"
    "v_exp_f32 v92, v92\n\t"
    "v_mfma_f32_16x16x32_bf16 v[84:87], a[124:127], a[92:95], v[84:87]\n\t"
    "s_waitcnt vmcnt(4)\n\t"
    "s_barrier\n\t"
    "v_add_f32 v44, v44, v91\n\t"
    "v_mfma_f32_16x16x32_bf16 v[192:195], a[160:163], v[72:75], 0\n\t"
    "ds_read_b128 a[96:99], v4 offset:32768\n\t"
    "v_cvt_pk_bf16_f32 v77, v90, v91\n\t"
    "v_exp_f32 v93, v93\n\t"
    "v_mfma_f32_16x16x32_bf16 v[196:199], a[164:167], v[72:75], 0\n\t"
    "ds_read_b128 a[100:103], v5 offset:32768\n\t"
    "v_add_f32 v44, v44, v92\n\t"
    "v_mfma_f32_16x16x32_bf16 v[200:203], a[168:171], v[72:75], 0\n\t"
    "ds_read_b128 a[104:107], v6 offset:32768\n\t"
    "v_exp_f32 v94, v94\n\t"
    "v_max3_f32 v69, v80, v81, v82\n\t"
    "v_max3_f32 v69, v69, v83, v84\n\t"
    "v_max3_f32 v69, v69, v85, v86\n\t"
    "v_mfma_f32_16x16x32_bf16 v[204:207], a[172:175], v[72:75], 0\n\t"
    "ds_read_b128 a[108:111], v7 offset:32768\n\t"
    "v_add_f32 v44, v44, v93\n\t"
    "v_max_f32 v69, v69, v87\n\t"
    "v_mov_b32 v70, v69\n\t"
    "v_mfma_f32_16x16x32_bf16 v[208:211], a[176:179], v[72:75], 0\n\t"
    "ds_read_b128 a[112:115], v4 offset:36864\n\t"
    "v_cvt_pk_bf16_f32 v78, v92, v93\n\t"
    "v_exp_f32 v95, v95\n\t"
    "v_permlane16_swap_b32 v69, v70\n\t"
    "v_max_f32 v69, v69, v70\n\t"
    "v_mfma_f32_16x16x32_bf16 v[212:215], a[180:183], v[72:75], 0\n\t"
    "ds_read_b128 a[116:119], v5 offset:36864\n\t"
    "v_add_f32 v44, v44, v94\n\t"
    "v_mov_b32 v70, v69\n\t"
    "s_nop 1\n\t"
    "v_permlane32_swap_b32 v69, v70\n\t"
    "v_max_f32 v69, v69, v70\n\t"
    "v_mfma_f32_16x16x32_bf16 v[216:219], a[184:187], v[72:75], 0\n\t"
    "ds_read_b128 a[120:123], v6 offset:36864\n\t"
    "v_add_f32 v44, v44, v95\n\t"
    "v_sub_f32 v68, 0, v69\n\t"
    "v_mov_b32 v69, v68\n\t"
    "v_mfma_f32_16x16x32_bf16 v[220:223], a[188:191], v[72:75], 0\n\t"
    "ds_read_b128 a[124:127], v7 offset:36864\n\t"
    "v_cvt_pk_bf16_f32 v79, v94, v95\n\t"
    "v_mov_b32 v70, v68\n\t"
    "v_mov_b32 v71, v68\n\t"
    "s_nop 1\n\t"
    "v_mfma_f32_16x16x32_bf16 v[88:91], a[128:131], a[80:83], v[68:71]\n\t"
    "v_add_f32 v80, v68, v80\n\t"
    "v_add_f32 v81, v68, v81\n\t"
    "v_mfma_f32_16x16x32_bf16 v[88:91], a[132:135], a[84:87], v[88:91]\n\t"
    "v_add_f32 v82, v68, v82\n\t"
    "v_add_f32 v83, v68, v83\n\t"
    "v_mfma_f32_16x16x32_bf16 v[88:91], a[136:139], a[88:91], v[88:91]\n\t"
    "v_add_f32 v84, v68, v84\n\t"
    "v_add_f32 v85, v68, v85\n\t"
    "v_mfma_f32_16x16x32_bf16 v[88:91], a[140:143], a[92:95], v[88:91]\n\t"
    "v_add_f32 v86, v68, v86\n\t"
    "v_mfma_f32_16x16x32_bf16 v[92:95], a[144:147], a[80:83], v[68:71]\n\t"
    "v_add_f32 v87, v68, v87\n\t"
    "v_exp_f32 v80, v80\n\t"
    "v_mfma_f32_16x16x32_bf16 v[92:95], a[148:151], a[84:87], v[92:95]\n\t"
    "v_exp_f32 v81, v81\n\t"
    "v_mov_b32 v45, v80\n\t"
    "v_mfma_f32_16x16x32_bf16 v[92:95], a[152:155], a[88:91], v[92:95]\n\t"
    "v_exp_f32 v82, v82\n\t"
    "v_add_f32 v45, v45, v81\n\t"
    "v_mfma_f32_16x16x32_bf16 v[92:95], a[156:159], a[92:95], v[92:95]\n\t"
    "v_cvt_pk_bf16_f32 v72, v80, v81\n\t"
    "v_mfma_f32_16x16x32_bf16 v[192:195], a[192:195], v[76:79], v[192:195]\n\t"
    "v_exp_f32 v83, v83\n\t"
    "v_add_f32 v45, v45, v82\n\t"
    "v_mfma_f32_16x16x32_bf16 v[196:199], a[196:199], v[76:79], v[196:199]\n\t"
    "v_exp_f32 v84, v84\n\t"
    "v_add_f32 v45, v45, v83\n\t"
    "v_mfma_f32_16x16x32_bf16 v[200:203], a[200:203], v[76:79], v[200:203]\n\t"
    "v_cvt_pk_bf16_f32 v73, v82, v83\n\t"
    "v_exp_f32 v85, v85\n\t"
    "v_mfma_f32_16x16x32_bf16 v[204:207], a[204:207], v[76:79], v[204:207]\n\t"
    "v_add_f32 v45, v45, v84\n\t"
    "v_mfma_f32_16x16x32_bf16 v[208:211], a[208:211], v[76:79], v[208:211]\n\t"
    "v_exp_f32 v86, v86\n\t"
    "v_add_f32 v45, v45, v85\n\t"
    "v_mfma_f32_16x16x32_bf16 v[212:215], a[212:215], v[76:79], v[212:215]\n\t"
    "v_cvt_pk_bf16_f32 v74, v84, v85\n\t"
    "v_exp_f32 v87, v87\n\t"
    "v_mfma_f32_16x16x32_bf16 v[216:219], a[216:219], v[76:79], v[216:219]\n\t"
    "v_add_f32 v45, v45, v86\n\t"
    "v_add_f32 v45, v45, v87\n\t"
    "v_mfma_f32_16x16x32_bf16 v[220:223], a[220:223], v[76:79], v[220:223]\n\t"
    "s_waitcnt lgkmcnt(0)\n\t"
    "v_cvt_pk_bf16_f32 v75, v86, v87\n\t"
    "v_mfma_f32_16x16x32_bf16 v[80:83], a[96:99], a[0:3], v[48:51]\n\t"
    "s_add_u32 m0, s61, 65536\n\t"
    "v_exp_f32 v88, v88\n\t"
    "v_exp_f32 v89, v89\n\t"
    "v_mfma_f32_16x16x32_bf16 v[80:83], a[100:103], a[4:7], v[80:83]\n\t"
    "s_add_u32 s58, s62, 32768\n\t"
    "v_add_f32 v45, v45, v88\n\t"
    "v_mfma_f32_16x16x32_bf16 v[80:83], a[104:107], a[8:11], v[80:83]\n\t"
    "buffer_load_dwordx4 v16, s[44:47], s58 offen nt lds\n\t"
    "v_exp_f32 v90, v90\n\t"
    "v_mfma_f32_16x16x32_bf16 v[80:83], a[108:111], a[12:15], v[80:83]\n\t"
    "buffer_load_dwordx4 v17, s[44:47], s58 offen offset:1024 nt lds\n\t"
    "v_add_f32 v45, v45, v89\n\t"
    "v_mfma_f32_16x16x32_bf16 v[84:87], a[112:115], a[0:3], v[48:51]\n\t"
    "s_add_u32 m0, s63, 65536\n\t"
    "v_cvt_pk_bf16_f32 v76, v88, v89\n\t"
    "v_exp_f32 v91, v91\n\t"
    "v_mfma_f32_16x16x32_bf16 v[84:87], a[116:119], a[4:7], v[84:87]\n\t"
    "s_add_u32 s59, s64, 32768\n\t"
    "v_add_f32 v45, v45, v90\n\t"
    "v_mfma_f32_16x16x32_bf16 v[84:87], a[120:123], a[8:11], v[84:87]\n\t"
    "buffer_load_dwordx4 v18, s[48:51], s59 offen nt lds\n\t"
    "v_exp_f32 v92, v92\n\t"
    "v_mfma_f32_16x16x32_bf16 v[84:87], a[124:127], a[12:15], v[84:87]\n\t"
    "buffer_load_dwordx4 v18, s[48:51], s59 offen offset:2048 nt lds\n\t"
    "v_add_f32 v45, v45, v91\n\t"
    "v_mfma_f32_16x16x32_bf16 v[224:227], a[160:163], v[72:75], 0\n\t"
    "v_cvt_pk_bf16_f32 v77, v90, v91\n\t"
    "v_exp_f32 v93, v93\n\t"
    "v_mfma_f32_16x16x32_bf16 v[228:231], a[164:167], v[72:75], 0\n\t"
    "v_add_f32 v45, v45, v92\n\t"
    "v_mfma_f32_16x16x32_bf16 v[232:235], a[168:171], v[72:75], 0\n\t"
    "v_exp_f32 v94, v94\n\t"
    "v_mfma_f32_16x16x32_bf16 v[236:239], a[172:175], v[72:75], 0\n\t"
    "v_add_f32 v45, v45, v93\n\t"
    "v_mfma_f32_16x16x32_bf16 v[240:243], a[176:179], v[72:75], 0\n\t"
    "v_cvt_pk_bf16_f32 v78, v92, v93\n\t"
    "v_exp_f32 v95, v95\n\t"
    "v_mfma_f32_16x16x32_bf16 v[244:247], a[180:183], v[72:75], 0\n\t"
    "v_add_f32 v45, v45, v94\n\t"
    "v_mfma_f32_16x16x32_bf16 v[248:251], a[184:187], v[72:75], 0\n\t"
    "v_add_f32 v45, v45, v95\n\t"
    "v_mfma_f32_16x16x32_bf16 v[252:255], a[188:191], v[72:75], 0\n\t"
    "v_cvt_pk_bf16_f32 v79, v94, v95\n\t"
    "v_mfma_f32_16x16x32_bf16 v[88:91], a[96:99], a[16:19], v[52:55]\n\t"
    "ds_read_b64_tr_b16 a[160:161], v8 offset:40960\n\t"
    "v_exp_f32 v80, v80\n\t"
    "v_mfma_f32_16x16x32_bf16 v[88:91], a[100:103], a[20:23], v[88:91]\n\t"
    "ds_read_b64_tr_b16 a[162:163], v8 offset:45056\n\t"
    "v_add_f32 v40, v40, v80\n\t"
    "v_mfma_f32_16x16x32_bf16 v[88:91], a[104:107], a[24:27], v[88:91]\n\t"
    "ds_read_b64_tr_b16 a[164:165], v9 offset:40960\n\t"
    "v_exp_f32 v81, v81\n\t"
    "v_mfma_f32_16x16x32_bf16 v[88:91], a[108:111], a[28:31], v[88:91]\n\t"
    "ds_read_b64_tr_b16 a[166:167], v9 offset:45056\n\t"
    "v_add_f32 v40, v40, v81\n\t"
    "v_cvt_pk_bf16_f32 v72, v80, v81\n\t"
    "v_mfma_f32_16x16x32_bf16 v[92:95], a[112:115], a[16:19], v[52:55]\n\t"
    "ds_read_b64_tr_b16 a[168:169], v10 offset:40960\n\t"
    "v_exp_f32 v82, v82\n\t"
    "v_mfma_f32_16x16x32_bf16 v[92:95], a[116:119], a[20:23], v[92:95]\n\t"
    "ds_read_b64_tr_b16 a[170:171], v10 offset:45056\n\t"
    "v_add_f32 v40, v40, v82\n\t"
    "v_mfma_f32_16x16x32_bf16 v[92:95], a[120:123], a[24:27], v[92:95]\n\t"
    "ds_read_b64_tr_b16 a[172:173], v11 offset:40960\n\t"
    "v_exp_f32 v83, v83\n\t"
    "v_mfma_f32_16x16x32_bf16 v[92:95], a[124:127], a[28:31], v[92:95]\n\t"
    "ds_read_b64_tr_b16 a[174:175], v11 offset:45056\n\t"
    "v_add_f32 v40, v40, v83\n\t"
    "v_cvt_pk_bf16_f32 v73, v82, v83\n\t"
    "v_mfma_f32_16x16x32_bf16 v[224:227], a[192:195], v[76:79], v[224:227]\n\t"
    "ds_read_b64_tr_b16 a[176:177], v8 offset:41984\n\t"
    "v_exp_f32 v84, v84\n\t"
    "v_mfma_f32_16x16x32_bf16 v[228:231], a[196:199], v[76:79], v[228:231]\n\t"
    "ds_read_b64_tr_b16 a[178:179], v8 offset:46080\n\t"
    "v_add_f32 v40, v40, v84\n\t"
    "v_mfma_f32_16x16x32_bf16 v[232:235], a[200:203], v[76:79], v[232:235]\n\t"
    "ds_read_b64_tr_b16 a[180:181], v9 offset:41984\n\t"
    "v_exp_f32 v85, v85\n\t"
    "v_mfma_f32_16x16x32_bf16 v[236:239], a[204:207], v[76:79], v[236:239]\n\t"
    "ds_read_b64_tr_b16 a[182:183], v9 offset:46080\n\t"
    "v_add_f32 v40, v40, v85\n\t"
    "v_cvt_pk_bf16_f32 v74, v84, v85\n\t"
    "v_mfma_f32_16x16x32_bf16 v[240:243], a[208:211], v[76:79], v[240:243]\n\t"
    "ds_read_b64_tr_b16 a[184:185], v10 offset:41984\n\t"
    "v_exp_f32 v86, v86\n\t"
    "v_mfma_f32_16x16x32_bf16 v[244:247], a[212:215], v[76:79], v[244:247]\n\t"
    "ds_read_b64_tr_b16 a[186:187], v10 offset:46080\n\t"
    "v_add_f32 v40, v40, v86\n\t"
    "v_mfma_f32_16x16x32_bf16 v[248:251], a[216:219], v[76:79], v[248:251]\n\t"
    "ds_read_b64_tr_b16 a[188:189], v11 offset:41984\n\t"
    "v_exp_f32 v87, v87\n\t"
    "v_mfma_f32_16x16x32_bf16 v[252:255], a[220:223], v[76:79], v[252:255]\n\t"
    "ds_read_b64_tr_b16 a[190:191], v11 offset:46080\n\t"
    "s_waitcnt vmcnt(4)\n\t"
    "s_barrier\n\t"
    "v_add_f32 v40, v40, v87\n\t"
    "v_cvt_pk_bf16_f32 v75, v86, v87\n\t"
    "v_mfma_f32_16x16x32_bf16 v[80:83], a[96:99], a[32:35], v[56:59]\n\t"
    "v_exp_f32 v88, v88\n\t"
    "v_mfma_f32_16x16x32_bf16 v[80:83], a[100:103], a[36:39], v[80:83]\n\t"
    "ds_read_b128 a[128:131], v4 offset:49152\n\t"
    "v_add_f32 v41, v41, v88\n\t"
    "v_mfma_f32_16x16x32_bf16 v[80:83], a[104:107], a[40:43], v[80:83]\n\t"
    "v_exp_f32 v89, v89\n\t"
    "v_mfma_f32_16x16x32_bf16 v[80:83], a[108:111], a[44:47], v[80:83]\n\t"
    "v_add_f32 v41, v41, v89\n\t"
    "v_cvt_pk_bf16_f32 v76, v88, v89\n\t"
    "v_mfma_f32_16x16x32_bf16 v[84:87], a[112:115], a[32:35], v[56:59]\n\t"
    "v_exp_f32 v90, v90\n\t"
    "v_mfma_f32_16x16x32_bf16 v[84:87], a[116:119], a[36:39], v[84:87]\n\t"
    "ds_read_b128 a[132:135], v5 offset:49152\n\t"
    "v_add_f32 v41, v41, v90\n\t"
    "v_mfma_f32_16x16x32_bf16 v[84:87], a[120:123], a[40:43], v[84:87]\n\t"
    "v_exp_f32 v91, v91\n\t"
    "v_mfma_f32_16x16x32_bf16 v[84:87], a[124:127], a[44:47], v[84:87]\n\t"
    "s_waitcnt lgkmcnt(2)\n\t"
    "v_add_f32 v41, v41, v91\n\t"
    "v_cvt_pk_bf16_f32 v77, v90, v91\n\t"
    "v_mfma_f32_16x16x32_bf16 a[224:227], a[160:163], v[72:75], a[224:227]\n\t"
    "v_exp_f32 v92, v92\n\t"
    "v_mfma_f32_16x16x32_bf16 a[228:231], a[164:167], v[72:75], a[228:231]\n\t"
    "ds_read_b128 a[136:139], v6 offset:49152\n\t"
    "v_add_f32 v41, v41, v92\n\t"
    "v_mfma_f32_16x16x32_bf16 a[232:235], a[168:171], v[72:75], a[232:235]\n\t"
    "v_exp_f32 v93, v93\n\t"
    "v_mfma_f32_16x16x32_bf16 a[236:239], a[172:175], v[72:75], a[236:239]\n\t"
    "v_add_f32 v41, v41, v93\n\t"
    "v_cvt_pk_bf16_f32 v78, v92, v93\n\t"
    "v_mfma_f32_16x16x32_bf16 a[240:243], a[176:179], v[72:75], a[240:243]\n\t"
    "v_exp_f32 v94, v94\n\t"
    "v_mfma_f32_16x16x32_bf16 a[244:247], a[180:183], v[72:75], a[244:247]\n\t"
    "ds_read_b128 a[140:143], v7 offset:49152\n\t"
    "v_add_f32 v41, v41, v94\n\t"
    "v_mfma_f32_16x16x32_bf16 a[248:251], a[184:187], v[72:75], a[248:251]\n\t"
    "v_exp_f32 v95, v95\n\t"
    "v_mfma_f32_16x16x32_bf16 a[252:255], a[188:191], v[72:75], a[252:255]\n\t"
    "v_add_f32 v41, v41, v95\n\t"
    "v_cvt_pk_bf16_f32 v79, v94, v95\n\t"
    "v_mfma_f32_16x16x32_bf16 v[88:91], a[96:99], a[48:51], v[60:63]\n\t"
    "v_exp_f32 v80, v80\n\t"
    "v_mfma_f32_16x16x32_bf16 v[88:91], a[100:103], a[52:55], v[88:91]\n\t"
    "ds_read_b128 a[144:147], v4 offset:53248\n\t"
    "v_add_f32 v42, v42, v80\n\t"
    "v_mfma_f32_16x16x32_bf16 v[88:91], a[104:107], a[56:59], v[88:91]\n\t"
    "v_exp_f32 v81, v81\n\t"
    "v_mfma_f32_16x16x32_bf16 v[88:91], a[108:111], a[60:63], v[88:91]\n\t"
    "v_add_f32 v42, v42, v81\n\t"
    "v_cvt_pk_bf16_f32 v72, v80, v81\n\t"
    "v_mfma_f32_16x16x32_bf16 v[92:95], a[112:115], a[48:51], v[60:63]\n\t"
    "v_exp_f32 v82, v82\n\t"
    "v_mfma_f32_16x16x32_bf16 v[92:95], a[116:119], a[52:55], v[92:95]\n\t"
    "ds_read_b128 a[148:151], v5 offset:53248\n\t"
    "v_add_f32 v42, v42, v82\n\t"
    "v_mfma_f32_16x16x32_bf16 v[92:95], a[120:123], a[56:59], v[92:95]\n\t"
    "v_exp_f32 v83, v83\n\t"
    "v_mfma_f32_16x16x32_bf16 v[92:95], a[124:127], a[60:63], v[92:95]\n\t"
    "v_add_f32 v42, v42, v83\n\t"
    "v_cvt_pk_bf16_f32 v73, v82, v83\n\t"
    "v_mfma_f32_16x16x32_bf16 v[96:99], a[160:163], v[76:79], v[96:99]\n\t"
    "v_exp_f32 v84, v84\n\t"
    "v_mfma_f32_16x16x32_bf16 v[100:103], a[164:167], v[76:79], v[100:103]\n\t"
    "ds_read_b128 a[152:155], v6 offset:53248\n\t"
    "v_add_f32 v42, v42, v84\n\t"
    "v_mfma_f32_16x16x32_bf16 v[104:107], a[168:171], v[76:79], v[104:107]\n\t"
    "v_exp_f32 v85, v85\n\t"
    "v_mfma_f32_16x16x32_bf16 v[108:111], a[172:175], v[76:79], v[108:111]\n\t"
    "v_add_f32 v42, v42, v85\n\t"
    "v_cvt_pk_bf16_f32 v74, v84, v85\n\t"
    "v_mfma_f32_16x16x32_bf16 v[112:115], a[176:179], v[76:79], v[112:115]\n\t"
    "v_exp_f32 v86, v86\n\t"
    "v_mfma_f32_16x16x32_bf16 v[116:119], a[180:183], v[76:79], v[116:119]\n\t"
    "ds_read_b128 a[156:159], v7 offset:53248\n\t"
    "v_add_f32 v42, v42, v86\n\t"
    "v_mfma_f32_16x16x32_bf16 v[120:123], a[184:187], v[76:79], v[120:123]\n\t"
    "v_exp_f32 v87, v87\n\t"
    "v_mfma_f32_16x16x32_bf16 v[124:127], a[188:191], v[76:79], v[124:127]\n\t"
    "v_add_f32 v42, v42, v87\n\t"
    "v_cvt_pk_bf16_f32 v75, v86, v87\n\t"
    "v_mfma_f32_16x16x32_bf16 v[80:83], a[96:99], a[64:67], v[64:67]\n\t"
    "v_exp_f32 v88, v88\n\t"
    "v_mfma_f32_16x16x32_bf16 v[80:83], a[100:103], a[68:71], v[80:83]\n\t"
    "ds_read_b64_tr_b16 a[192:193], v8 offset:57344\n\t"
    "v_add_f32 v43, v43, v88\n\t"
    "v_mfma_f32_16x16x32_bf16 v[80:83], a[104:107], a[72:75], v[80:83]\n\t"
    "v_exp_f32 v89, v89\n\t"
    "v_mfma_f32_16x16x32_bf16 v[80:83], a[108:111], a[76:79], v[80:83]\n\t"
    "v_add_f32 v43, v43, v89\n\t"
    "v_cvt_pk_bf16_f32 v76, v88, v89\n\t"
    "v_mfma_f32_16x16x32_bf16 v[84:87], a[112:115], a[64:67], v[64:67]\n\t"
    "v_exp_f32 v90, v90\n\t"
    "v_mfma_f32_16x16x32_bf16 v[84:87], a[116:119], a[68:71], v[84:87]\n\t"
    "ds_read_b64_tr_b16 a[194:195], v8 offset:61440\n\t"
    "v_add_f32 v43, v43, v90\n\t"
    "v_mfma_f32_16x16x32_bf16 v[84:87], a[120:123], a[72:75], v[84:87]\n\t"
    "v_exp_f32 v91, v91\n\t"
    "v_mfma_f32_16x16x32_bf16 v[84:87], a[124:127], a[76:79], v[84:87]\n\t"
    "v_add_f32 v43, v43, v91\n\t"
    "v_cvt_pk_bf16_f32 v77, v90, v91\n\t"
    "v_mfma_f32_16x16x32_bf16 v[128:131], a[160:163], v[72:75], v[128:131]\n\t"
    "v_exp_f32 v92, v92\n\t"
    "v_mfma_f32_16x16x32_bf16 v[132:135], a[164:167], v[72:75], v[132:135]\n\t"
    "ds_read_b64_tr_b16 a[196:197], v9 offset:57344\n\t"
    "v_add_f32 v43, v43, v92\n\t"
    "v_mfma_f32_16x16x32_bf16 v[136:139], a[168:171], v[72:75], v[136:139]\n\t"
    "v_exp_f32 v93, v93\n\t"
    "v_mfma_f32_16x16x32_bf16 v[140:143], a[172:175], v[72:75], v[140:143]\n\t"
    "v_add_f32 v43, v43, v93\n\t"
    "v_cvt_pk_bf16_f32 v78, v92, v93\n\t"
    "v_mfma_f32_16x16x32_bf16 v[144:147], a[176:179], v[72:75], v[144:147]\n\t"
    "v_exp_f32 v94, v94\n\t"
    "v_mfma_f32_16x16x32_bf16 v[148:151], a[180:183], v[72:75], v[148:151]\n\t"
    "ds_read_b64_tr_b16 a[198:199], v9 offset:61440\n\t"
    "v_add_f32 v43, v43, v94\n\t"
    "v_mfma_f32_16x16x32_bf16 v[152:155], a[184:187], v[72:75], v[152:155]\n\t"
    "v_exp_f32 v95, v95\n\t"
    "v_mfma_f32_16x16x32_bf16 v[156:159], a[188:191], v[72:75], v[156:159]\n\t"
    "v_add_f32 v43, v43, v95\n\t"
    "v_cvt_pk_bf16_f32 v79, v94, v95\n\t"
    "v_mfma_f32_16x16x32_bf16 v[88:91], a[96:99], a[80:83], v[68:71]\n\t"
    "v_exp_f32 v80, v80\n\t"
    "v_mfma_f32_16x16x32_bf16 v[88:91], a[100:103], a[84:87], v[88:91]\n\t"
    "ds_read_b64_tr_b16 a[200:201], v10 offset:57344\n\t"
    "v_add_f32 v44, v44, v80\n\t"
    "v_mfma_f32_16x16x32_bf16 v[88:91], a[104:107], a[88:91], v[88:91]\n\t"
    "v_exp_f32 v81, v81\n\t"
    "v_mfma_f32_16x16x32_bf16 v[88:91], a[108:111], a[92:95], v[88:91]\n\t"
    "v_add_f32 v44, v44, v81\n\t"
    "v_cvt_pk_bf16_f32 v72, v80, v81\n\t"
    "v_mfma_f32_16x16x32_bf16 v[92:95], a[112:115], a[80:83], v[68:71]\n\t"
    "v_exp_f32 v82, v82\n\t"
    "v_mfma_f32_16x16x32_bf16 v[92:95], a[116:119], a[84:87], v[92:95]\n\t"
    "ds_read_b64_tr_b16 a[202:203], v10 offset:61440\n\t"
    "v_add_f32 v44, v44, v82\n\t"
    "v_mfma_f32_16x16x32_bf16 v[92:95], a[120:123], a[88:91], v[92:95]\n\t"
    "v_exp_f32 v83, v83\n\t"
    "v_mfma_f32_16x16x32_bf16 v[92:95], a[124:127], a[92:95], v[92:95]\n\t"
    "v_add_f32 v44, v44, v83\n\t"
    "v_cvt_pk_bf16_f32 v73, v82, v83\n\t"
    "v_mfma_f32_16x16x32_bf16 v[160:163], a[160:163], v[76:79], v[160:163]\n\t"
    "v_exp_f32 v84, v84\n\t"
    "v_mfma_f32_16x16x32_bf16 v[164:167], a[164:167], v[76:79], v[164:167]\n\t"
    "ds_read_b64_tr_b16 a[204:205], v11 offset:57344\n\t"
    "v_add_f32 v44, v44, v84\n\t"
    "v_mfma_f32_16x16x32_bf16 v[168:171], a[168:171], v[76:79], v[168:171]\n\t"
    "v_exp_f32 v85, v85\n\t"
    "v_mfma_f32_16x16x32_bf16 v[172:175], a[172:175], v[76:79], v[172:175]\n\t"
    "v_add_f32 v44, v44, v85\n\t"
    "v_cvt_pk_bf16_f32 v74, v84, v85\n\t"
    "v_mfma_f32_16x16x32_bf16 v[176:179], a[176:179], v[76:79], v[176:179]\n\t"
    "v_exp_f32 v86, v86\n\t"
    "v_mfma_f32_16x16x32_bf16 v[180:183], a[180:183], v[76:79], v[180:183]\n\t"
    "ds_read_b64_tr_b16 a[206:207], v11 offset:61440\n\t"
    "v_add_f32 v44, v44, v86\n\t"
    "v_mfma_f32_16x16x32_bf16 v[184:187], a[184:187], v[76:79], v[184:187]\n\t"
    "v_exp_f32 v87, v87\n\t"
    "v_mfma_f32_16x16x32_bf16 v[188:191], a[188:191], v[76:79], v[188:191]\n\t"
    "s_waitcnt lgkmcnt(8)\n\t"
    "v_add_f32 v44, v44, v87\n\t"
    "v_cvt_pk_bf16_f32 v75, v86, v87\n\t"
    "v_mfma_f32_16x16x32_bf16 v[80:83], a[128:131], a[0:3], v[48:51]\n\t"
    "v_exp_f32 v88, v88\n\t"
    "v_mfma_f32_16x16x32_bf16 v[80:83], a[132:135], a[4:7], v[80:83]\n\t"
    "ds_read_b64_tr_b16 a[208:209], v8 offset:58368\n\t"
    "v_add_f32 v45, v45, v88\n\t"
    "v_mfma_f32_16x16x32_bf16 v[80:83], a[136:139], a[8:11], v[80:83]\n\t"
    "v_exp_f32 v89, v89\n\t"
    "v_mfma_f32_16x16x32_bf16 v[80:83], a[140:143], a[12:15], v[80:83]\n\t"
    "v_add_f32 v45, v45, v89\n\t"
    "v_cvt_pk_bf16_f32 v76, v88, v89\n\t"
    "v_mfma_f32_16x16x32_bf16 v[84:87], a[144:147], a[0:3], v[48:51]\n\t"
    "v_exp_f32 v90, v90\n\t"
    "v_mfma_f32_16x16x32_bf16 v[84:87], a[148:151], a[4:7], v[84:87]\n\t"
    "ds_read_b64_tr_b16 a[210:211], v8 offset:62464\n\t"
    "v_add_f32 v45, v45, v90\n\t"
    "v_mfma_f32_16x16x32_bf16 v[84:87], a[152:155], a[8:11], v[84:87]\n\t"
    "v_exp_f32 v91, v91\n\t"
    "v_mfma_f32_16x16x32_bf16 v[84:87], a[156:159], a[12:15], v[84:87]\n\t"
    "v_add_f32 v45, v45, v91\n\t"
    "v_cvt_pk_bf16_f32 v77, v90, v91\n\t"
    "v_mfma_f32_16x16x32_bf16 v[192:195], a[160:163], v[72:75], v[192:195]\n\t"
    "v_exp_f32 v92, v92\n\t"
    "v_mfma_f32_16x16x32_bf16 v[196:199], a[164:167], v[72:75], v[196:199]\n\t"
    "ds_read_b64_tr_b16 a[212:213], v9 offset:58368\n\t"
    "v_add_f32 v45, v45, v92\n\t"
    "v_mfma_f32_16x16x32_bf16 v[200:203], a[168:171], v[72:75], v[200:203]\n\t"
    "v_exp_f32 v93, v93\n\t"
    "v_mfma_f32_16x16x32_bf16 v[204:207], a[172:175], v[72:75], v[204:207]\n\t"
    "v_add_f32 v45, v45, v93\n\t"
    "v_cvt_pk_bf16_f32 v78, v92, v93\n\t"
    "v_mfma_f32_16x16x32_bf16 v[208:211], a[176:179], v[72:75], v[208:211]\n\t"
    "v_exp_f32 v94, v94\n\t"
    "v_mfma_f32_16x16x32_bf16 v[212:215], a[180:183], v[72:75], v[212:215]\n\t"
    "ds_read_b64_tr_b16 a[214:215], v9 offset:62464\n\t"
    "v_add_f32 v45, v45, v94\n\t"
    "v_mfma_f32_16x16x32_bf16 v[216:219], a[184:187], v[72:75], v[216:219]\n\t"
    "v_exp_f32 v95, v95\n\t"
    "v_mfma_f32_16x16x32_bf16 v[220:223], a[188:191], v[72:75], v[220:223]\n\t"
    "v_add_f32 v45, v45, v95\n\t"
    "v_cvt_pk_bf16_f32 v79, v94, v95\n\t"
    "v_mfma_f32_16x16x32_bf16 v[88:91], a[128:131], a[16:19], v[52:55]\n\t"
    "v_exp_f32 v80, v80\n\t"
    "v_mfma_f32_16x16x32_bf16 v[88:91], a[132:135], a[20:23], v[88:91]\n\t"
    "ds_read_b64_tr_b16 a[216:217], v10 offset:58368\n\t"
    "v_add_f32 v40, v40, v80\n\t"
    "v_xor_b32 v4, 0x10000, v4\n\t"
    "v_mfma_f32_16x16x32_bf16 v[88:91], a[136:139], a[24:27], v[88:91]\n\t"
    "v_exp_f32 v81, v81\n\t"
    "v_mfma_f32_16x16x32_bf16 v[88:91], a[140:143], a[28:31], v[88:91]\n\t"
    "v_add_f32 v40, v40, v81\n\t"
    "v_cvt_pk_bf16_f32 v72, v80, v81\n\t"
    "v_mfma_f32_16x16x32_bf16 v[92:95], a[144:147], a[16:19], v[52:55]\n\t"
    "v_exp_f32 v82, v82\n\t"
    "v_mfma_f32_16x16x32_bf16 v[92:95], a[148:151], a[20:23], v[92:95]\n\t"
    "ds_read_b64_tr_b16 a[218:219], v10 offset:62464\n\t"
    "v_add_f32 v40, v40, v82\n\t"
    "v_xor_b32 v5, 0x10000, v5\n\t"
    "v_mfma_f32_16x16x32_bf16 v[92:95], a[152:155], a[24:27], v[92:95]\n\t"
    "v_exp_f32 v83, v83\n\t"
    "v_mfma_f32_16x16x32_bf16 v[92:95], a[156:159], a[28:31], v[92:95]\n\t"
    "v_add_f32 v40, v40, v83\n\t"
    "v_cvt_pk_bf16_f32 v73, v82, v83\n\t"
    "v_mfma_f32_16x16x32_bf16 v[224:227], a[160:163], v[76:79], v[224:227]\n\t"
    "v_exp_f32 v84, v84\n\t"
    "v_mfma_f32_16x16x32_bf16 v[228:231], a[164:167], v[76:79], v[228:231]\n\t"
    "ds_read_b64_tr_b16 a[220:221], v11 offset:58368\n\t"
    "v_add_f32 v40, v40, v84\n\t"
    "v_xor_b32 v6, 0x10000, v6\n\t"
    "v_mfma_f32_16x16x32_bf16 v[232:235], a[168:171], v[76:79], v[232:235]\n\t"
    "v_exp_f32 v85, v85\n\t"
    "v_mfma_f32_16x16x32_bf16 v[236:239], a[172:175], v[76:79], v[236:239]\n\t"
    "v_add_f32 v40, v40, v85\n\t"
    "v_cvt_pk_bf16_f32 v74, v84, v85\n\t"
    "v_mfma_f32_16x16x32_bf16 v[240:243], a[176:179], v[76:79], v[240:243]\n\t"
    "v_exp_f32 v86, v86\n\t"
    "v_mfma_f32_16x16x32_bf16 v[244:247], a[180:183], v[76:79], v[244:247]\n\t"
    "ds_read_b64_tr_b16 a[222:223], v11 offset:62464\n\t"
    "v_add_f32 v40, v40, v86\n\t"
    "v_xor_b32 v7, 0x10000, v7\n\t"
    "v_mfma_f32_16x16x32_bf16 v[248:251], a[184:187], v[76:79], v[248:251]\n\t"
    "v_exp_f32 v87, v87\n\t"
    "v_mfma_f32_16x16x32_bf16 v[252:255], a[188:191], v[76:79], v[252:255]\n\t"
    "s_waitcnt vmcnt(0)\n\t"
    "s_barrier\n\t"
    "v_add_f32 v40, v40, v87\n\t"
    "v_cvt_pk_bf16_f32 v75, v86, v87\n\t"
    "v_mfma_f32_16x16x32_bf16 v[80:83], a[128:131], a[32:35], v[56:59]\n\t"
    "v_exp_f32 v88, v88\n\t"
    "v_mfma_f32_16x16x32_bf16 v[80:83], a[132:135], a[36:39], v[80:83]\n\t"
    "ds_read_b128 a[96:99], v4 offset:0\n\t"
    "v_add_f32 v41, v41, v88\n\t"
    "v_mfma_f32_16x16x32_bf16 v[80:83], a[136:139], a[40:43], v[80:83]\n\t"
    "v_exp_f32 v89, v89\n\t"
    "v_mfma_f32_16x16x32_bf16 v[80:83], a[140:143], a[44:47], v[80:83]\n\t"
    "v_add_f32 v41, v41, v89\n\t"
    "v_cvt_pk_bf16_f32 v76, v88, v89\n\t"
    "v_mfma_f32_16x16x32_bf16 v[84:87], a[144:147], a[32:35], v[56:59]\n\t"
    "v_exp_f32 v90, v90\n\t"
    "v_mfma_f32_16x16x32_bf16 v[84:87], a[148:151], a[36:39], v[84:87]\n\t"
    "ds_read_b128 a[100:103], v5 offset:0\n\t"
    "v_add_f32 v41, v41, v90\n\t"
    "v_mfma_f32_16x16x32_bf16 v[84:87], a[152:155], a[40:43], v[84:87]\n\t"
    "v_exp_f32 v91, v91\n\t"
    "v_mfma_f32_16x16x32_bf16 v[84:87], a[156:159], a[44:47], v[84:87]\n\t"
    "s_waitcnt lgkmcnt(2)\n\t"
    "v_add_f32 v41, v41, v91\n\t"
    "v_cvt_pk_bf16_f32 v77, v90, v91\n\t"
    "v_mfma_f32_16x16x32_bf16 a[224:227], a[192:195], v[72:75], a[224:227]\n\t"
    "v_exp_f32 v92, v92\n\t"
    "v_mfma_f32_16x16x32_bf16 a[228:231], a[196:199], v[72:75], a[228:231]\n\t"
    "ds_read_b128 a[104:107], v6 offset:0\n\t"
    "v_add_f32 v41, v41, v92\n\t"
    "v_mfma_f32_16x16x32_bf16 a[232:235], a[200:203], v[72:75], a[232:235]\n\t"
    "v_exp_f32 v93, v93\n\t"
    "v_mfma_f32_16x16x32_bf16 a[236:239], a[204:207], v[72:75], a[236:239]\n\t"
    "v_add_f32 v41, v41, v93\n\t"
    "v_cvt_pk_bf16_f32 v78, v92, v93\n\t"
    "v_mfma_f32_16x16x32_bf16 a[240:243], a[208:211], v[72:75], a[240:243]\n\t"
    "v_exp_f32 v94, v94\n\t"
    "v_mfma_f32_16x16x32_bf16 a[244:247], a[212:215], v[72:75], a[244:247]\n\t"
    "ds_read_b128 a[108:111], v7 offset:0\n\t"
    "v_add_f32 v41, v41, v94\n\t"
    "v_mfma_f32_16x16x32_bf16 a[248:251], a[216:219], v[72:75], a[248:251]\n\t"
    "v_exp_f32 v95, v95\n\t"
    "v_mfma_f32_16x16x32_bf16 a[252:255], a[220:223], v[72:75], a[252:255]\n\t"
    "v_add_f32 v41, v41, v95\n\t"
    "v_cvt_pk_bf16_f32 v79, v94, v95\n\t"
    "v_mfma_f32_16x16x32_bf16 v[88:91], a[128:131], a[48:51], v[60:63]\n\t"
    "v_exp_f32 v80, v80\n\t"
    "v_mfma_f32_16x16x32_bf16 v[88:91], a[132:135], a[52:55], v[88:91]\n\t"
    "ds_read_b128 a[112:115], v4 offset:4096\n\t"
    "v_add_f32 v42, v42, v80\n\t"
    "v_xor_b32 v8, 0x10000, v8\n\t"
    "v_mfma_f32_16x16x32_bf16 v[88:91], a[136:139], a[56:59], v[88:91]\n\t"
    "v_exp_f32 v81, v81\n\t"
    "v_mfma_f32_16x16x32_bf16 v[88:91], a[140:143], a[60:63], v[88:91]\n\t"
    "v_add_f32 v42, v42, v81\n\t"
    "v_cvt_pk_bf16_f32 v72, v80, v81\n\t"
    "v_mfma_f32_16x16x32_bf16 v[92:95], a[144:147], a[48:51], v[60:63]\n\t"
    "v_exp_f32 v82, v82\n\t"
    "v_mfma_f32_16x16x32_bf16 v[92:95], a[148:151], a[52:55], v[92:95]\n\t"
    "ds_read_b128 a[116:119], v5 offset:4096\n\t"
    "v_add_f32 v42, v42, v82\n\t"
    "v_xor_b32 v9, 0x10000, v9\n\t"
    "v_mfma_f32_16x16x32_bf16 v[92:95], a[152:155], a[56:59], v[92:95]\n\t"
    "v_exp_f32 v83, v83\n\t"
    "v_mfma_f32_16x16x32_bf16 v[92:95], a[156:159], a[60:63], v[92:95]\n\t"
    "v_add_f32 v42, v42, v83\n\t"
    "v_cvt_pk_bf16_f32 v73, v82, v83\n\t"
    "v_mfma_f32_16x16x32_bf16 v[96:99], a[192:195], v[76:79], v[96:99]\n\t"
    "v_exp_f32 v84, v84\n\t"
    "v_mfma_f32_16x16x32_bf16 v[100:103], a[196:199], v[76:79], v[100:103]\n\t"
    "ds_read_b128 a[120:123], v6 offset:4096\n\t"
    "v_add_f32 v42, v42, v84\n\t"
    "v_xor_b32 v10, 0x10000, v10\n\t"
    "v_mfma_f32_16x16x32_bf16 v[104:107], a[200:203], v[76:79], v[104:107]\n\t"
    "v_exp_f32 v85, v85\n\t"
    "v_mfma_f32_16x16x32_bf16 v[108:111], a[204:207], v[76:79], v[108:111]\n\t"
    "v_add_f32 v42, v42, v85\n\t"
    "v_cvt_pk_bf16_f32 v74, v84, v85\n\t"
    "v_mfma_f32_16x16x32_bf16 v[112:115], a[208:211], v[76:79], v[112:115]\n\t"
    "v_exp_f32 v86, v86\n\t"
    "v_mfma_f32_16x16x32_bf16 v[116:119], a[212:215], v[76:79], v[116:119]\n\t"
    "ds_read_b128 a[124:127], v7 offset:4096\n\t"
    "v_add_f32 v42, v42, v86\n\t"
    "v_xor_b32 v11, 0x10000, v11\n\t"
    "v_mfma_f32_16x16x32_bf16 v[120:123], a[216:219], v[76:79], v[120:123]\n\t"
    "v_exp_f32 v87, v87\n\t"
    "v_mfma_f32_16x16x32_bf16 v[124:127], a[220:223], v[76:79], v[124:127]\n\t"
    "v_add_f32 v42, v42, v87\n\t"
    "v_cvt_pk_bf16_f32 v75, v86, v87\n\t"
    "v_mfma_f32_16x16x32_bf16 v[80:83], a[128:131], a[64:67], v[64:67]\n\t"
    "v_exp_f32 v88, v88\n\t"
    "v_mfma_f32_16x16x32_bf16 v[80:83], a[132:135], a[68:71], v[80:83]\n\t"
    "ds_read_b64_tr_b16 a[160:161], v8 offset:8192\n\t"
    "v_add_f32 v43, v43, v88\n\t"
    "v_mfma_f32_16x16x32_bf16 v[80:83], a[136:139], a[72:75], v[80:83]\n\t"
    "v_exp_f32 v89, v89\n\t"
    "v_mfma_f32_16x16x32_bf16 v[80:83], a[140:143], a[76:79], v[80:83]\n\t"
    "v_add_f32 v43, v43, v89\n\t"
    "v_cvt_pk_bf16_f32 v76, v88, v89\n\t"
    "v_mfma_f32_16x16x32_bf16 v[84:87], a[144:147], a[64:67], v[64:67]\n\t"
    "v_exp_f32 v90, v90\n\t"
    "v_mfma_f32_16x16x32_bf16 v[84:87], a[148:151], a[68:71], v[84:87]\n\t"
    "ds_read_b64_tr_b16 a[162:163], v8 offset:12288\n\t"
    "v_add_f32 v43, v43, v90\n\t"
    "v_mfma_f32_16x16x32_bf16 v[84:87], a[152:155], a[72:75], v[84:87]\n\t"
    "v_exp_f32 v91, v91\n\t"
    "v_mfma_f32_16x16x32_bf16 v[84:87], a[156:159], a[76:79], v[84:87]\n\t"
    "v_add_f32 v43, v43, v91\n\t"
    "v_cvt_pk_bf16_f32 v77, v90, v91\n\t"
    "v_mfma_f32_16x16x32_bf16 v[128:131], a[192:195], v[72:75], v[128:131]\n\t"
    "v_exp_f32 v92, v92\n\t"
    "v_mfma_f32_16x16x32_bf16 v[132:135], a[196:199], v[72:75], v[132:135]\n\t"
    "ds_read_b64_tr_b16 a[164:165], v9 offset:8192\n\t"
    "v_add_f32 v43, v43, v92\n\t"
    "v_mfma_f32_16x16x32_bf16 v[136:139], a[200:203], v[72:75], v[136:139]\n\t"
    "v_exp_f32 v93, v93\n\t"
    "v_mfma_f32_16x16x32_bf16 v[140:143], a[204:207], v[72:75], v[140:143]\n\t"
    "v_add_f32 v43, v43, v93\n\t"
    "v_cvt_pk_bf16_f32 v78, v92, v93\n\t"
    "v_mfma_f32_16x16x32_bf16 v[144:147], a[208:211], v[72:75], v[144:147]\n\t"
    "v_exp_f32 v94, v94\n\t"
    "v_mfma_f32_16x16x32_bf16 v[148:151], a[212:215], v[72:75], v[148:151]\n\t"
    "ds_read_b64_tr_b16 a[166:167], v9 offset:12288\n\t"
    "v_add_f32 v43, v43, v94\n\t"
    "v_mfma_f32_16x16x32_bf16 v[152:155], a[216:219], v[72:75], v[152:155]\n\t"
    "v_exp_f32 v95, v95\n\t"
    "v_mfma_f32_16x16x32_bf16 v[156:159], a[220:223], v[72:75], v[156:159]\n\t"
    "v_add_f32 v43, v43, v95\n\t"
    "v_cvt_pk_bf16_f32 v79, v94, v95\n\t"
    "v_mfma_f32_16x16x32_bf16 v[88:91], a[128:131], a[80:83], v[68:71]\n\t"
    "v_exp_f32 v80, v80\n\t"
    "v_mfma_f32_16x16x32_bf16 v[88:91], a[132:135], a[84:87], v[88:91]\n\t"
    "ds_read_b64_tr_b16 a[168:169], v10 offset:8192\n\t"
    "v_add_f32 v44, v44, v80\n\t"
    "v_mfma_f32_16x16x32_bf16 v[88:91], a[136:139], a[88:91], v[88:91]\n\t"
    "v_exp_f32 v81, v81\n\t"
    "v_mfma_f32_16x16x32_bf16 v[88:91], a[140:143], a[92:95], v[88:91]\n\t"
    "v_add_f32 v44, v44, v81\n\t"
    "v_cvt_pk_bf16_f32 v72, v80, v81\n\t"
    "v_mfma_f32_16x16x32_bf16 v[92:95], a[144:147], a[80:83], v[68:71]\n\t"
    "v_exp_f32 v82, v82\n\t"
    "v_mfma_f32_16x16x32_bf16 v[92:95], a[148:151], a[84:87], v[92:95]\n\t"
    "ds_read_b64_tr_b16 a[170:171], v10 offset:12288\n\t"
    "v_add_f32 v44, v44, v82\n\t"
    "v_mfma_f32_16x16x32_bf16 v[92:95], a[152:155], a[88:91], v[92:95]\n\t"
    "v_exp_f32 v83, v83\n\t"
    "v_mfma_f32_16x16x32_bf16 v[92:95], a[156:159], a[92:95], v[92:95]\n\t"
    "v_add_f32 v44, v44, v83\n\t"
    "v_cvt_pk_bf16_f32 v73, v82, v83\n\t"
    "v_mfma_f32_16x16x32_bf16 v[160:163], a[192:195], v[76:79], v[160:163]\n\t"
    "v_exp_f32 v84, v84\n\t"
    "v_mfma_f32_16x16x32_bf16 v[164:167], a[196:199], v[76:79], v[164:167]\n\t"
    "ds_read_b64_tr_b16 a[172:173], v11 offset:8192\n\t"
    "v_add_f32 v44, v44, v84\n\t"
    "v_mfma_f32_16x16x32_bf16 v[168:171], a[200:203], v[76:79], v[168:171]\n\t"
    "v_exp_f32 v85, v85\n\t"
    "v_mfma_f32_16x16x32_bf16 v[172:175], a[204:207], v[76:79], v[172:175]\n\t"
    "v_add_f32 v44, v44, v85\n\t"
    "v_cvt_pk_bf16_f32 v74, v84, v85\n\t"
    "v_mfma_f32_16x16x32_bf16 v[176:179], a[208:211], v[76:79], v[176:179]\n\t"
    "v_exp_f32 v86, v86\n\t"
    "v_mfma_f32_16x16x32_bf16 v[180:183], a[212:215], v[76:79], v[180:183]\n\t"
    "ds_read_b64_tr_b16 a[174:175], v11 offset:12288\n\t"
    "v_add_f32 v44, v44, v86\n\t"
    "v_mfma_f32_16x16x32_bf16 v[184:187], a[216:219], v[76:79], v[184:187]\n\t"
    "v_exp_f32 v87, v87\n\t"
    "v_mfma_f32_16x16x32_bf16 v[188:191], a[220:223], v[76:79], v[188:191]\n\t"
    "s_waitcnt lgkmcnt(8)\n\t"
    "v_add_f32 v44, v44, v87\n\t"
    "v_cvt_pk_bf16_f32 v75, v86, v87\n\t"
    "v_mfma_f32_16x16x32_bf16 v[80:83], a[96:99], a[0:3], v[48:51]\n\t"
    "v_exp_f32 v88, v88\n\t"
    "v_mfma_f32_16x16x32_bf16 v[80:83], a[100:103], a[4:7], v[80:83]\n\t"
    "ds_read_b64_tr_b16 a[176:177], v8 offset:9216\n\t"
    "v_add_f32 v45, v45, v88\n\t"
    "v_mfma_f32_16x16x32_bf16 v[80:83], a[104:107], a[8:11], v[80:83]\n\t"
    "v_exp_f32 v89, v89\n\t"
    "v_mfma_f32_16x16x32_bf16 v[80:83], a[108:111], a[12:15], v[80:83]\n\t"
    "v_add_f32 v45, v45, v89\n\t"
    "v_cvt_pk_bf16_f32 v76, v88, v89\n\t"
    "v_mfma_f32_16x16x32_bf16 v[84:87], a[112:115], a[0:3], v[48:51]\n\t"
    "v_exp_f32 v90, v90\n\t"
    "v_mfma_f32_16x16x32_bf16 v[84:87], a[116:119], a[4:7], v[84:87]\n\t"
    "ds_read_b64_tr_b16 a[178:179], v8 offset:13312\n\t"
    "v_add_f32 v45, v45, v90\n\t"
    "v_mfma_f32_16x16x32_bf16 v[84:87], a[120:123], a[8:11], v[84:87]\n\t"
    "v_exp_f32 v91, v91\n\t"
    "v_mfma_f32_16x16x32_bf16 v[84:87], a[124:127], a[12:15], v[84:87]\n\t"
    "v_add_f32 v45, v45, v91\n\t"
    "v_cvt_pk_bf16_f32 v77, v90, v91\n\t"
    "v_mfma_f32_16x16x32_bf16 v[192:195], a[192:195], v[72:75], v[192:195]\n\t"
    "v_exp_f32 v92, v92\n\t"
    "v_mfma_f32_16x16x32_bf16 v[196:199], a[196:199], v[72:75], v[196:199]\n\t"
    "ds_read_b64_tr_b16 a[180:181], v9 offset:9216\n\t"
    "v_add_f32 v45, v45, v92\n\t"
    "v_mfma_f32_16x16x32_bf16 v[200:203], a[200:203], v[72:75], v[200:203]\n\t"
    "v_exp_f32 v93, v93\n\t"
    "v_mfma_f32_16x16x32_bf16 v[204:207], a[204:207], v[72:75], v[204:207]\n\t"
    "v_add_f32 v45, v45, v93\n\t"
    "v_cvt_pk_bf16_f32 v78, v92, v93\n\t"
    "v_mfma_f32_16x16x32_bf16 v[208:211], a[208:211], v[72:75], v[208:211]\n\t"
    "v_exp_f32 v94, v94\n\t"
    "v_mfma_f32_16x16x32_bf16 v[212:215], a[212:215], v[72:75], v[212:215]\n\t"
    "ds_read_b64_tr_b16 a[182:183], v9 offset:13312\n\t"
    "v_add_f32 v45, v45, v94\n\t"
    "v_mfma_f32_16x16x32_bf16 v[216:219], a[216:219], v[72:75], v[216:219]\n\t"
    "v_exp_f32 v95, v95\n\t"
    "v_mfma_f32_16x16x32_bf16 v[220:223], a[220:223], v[72:75], v[220:223]\n\t"
    "v_add_f32 v45, v45, v95\n\t"
    "v_cvt_pk_bf16_f32 v79, v94, v95\n\t"
    "v_mfma_f32_16x16x32_bf16 v[88:91], a[96:99], a[16:19], v[52:55]\n\t"
    "v_exp_f32 v80, v80\n\t"
    "v_mfma_f32_16x16x32_bf16 v[88:91], a[100:103], a[20:23], v[88:91]\n\t"
    "ds_read_b64_tr_b16 a[184:185], v10 offset:9216\n\t"
    "v_add_f32 v40, v40, v80\n\t"
    "v_mfma_f32_16x16x32_bf16 v[88:91], a[104:107], a[24:27], v[88:91]\n\t"
    "v_exp_f32 v81, v81\n\t"
    "v_mfma_f32_16x16x32_bf16 v[88:91], a[108:111], a[28:31], v[88:91]\n\t"
    "v_add_f32 v40, v40, v81\n\t"
    "v_cvt_pk_bf16_f32 v72, v80, v81\n\t"
    "v_mfma_f32_16x16x32_bf16 v[92:95], a[112:115], a[16:19], v[52:55]\n\t"
    "v_exp_f32 v82, v82\n\t"
    "v_mfma_f32_16x16x32_bf16 v[92:95], a[116:119], a[20:23], v[92:95]\n\t"
    "ds_read_b64_tr_b16 a[186:187], v10 offset:13312\n\t"
    "v_add_f32 v40, v40, v82\n\t"
    "v_mfma_f32_16x16x32_bf16 v[92:95], a[120:123], a[24:27], v[92:95]\n\t"
    "v_exp_f32 v83, v83\n\t"
    "v_mfma_f32_16x16x32_bf16 v[92:95], a[124:127], a[28:31], v[92:95]\n\t"
    "v_add_f32 v40, v40, v83\n\t"
    "v_cvt_pk_bf16_f32 v73, v82, v83\n\t"
    "v_mfma_f32_16x16x32_bf16 v[224:227], a[192:195], v[76:79], v[224:227]\n\t"
    "v_exp_f32 v84, v84\n\t"
    "v_mfma_f32_16x16x32_bf16 v[228:231], a[196:199], v[76:79], v[228:231]\n\t"
    "ds_read_b64_tr_b16 a[188:189], v11 offset:9216\n\t"
    "v_add_f32 v40, v40, v84\n\t"
    "v_mfma_f32_16x16x32_bf16 v[232:235], a[200:203], v[76:79], v[232:235]\n\t"
    "v_exp_f32 v85, v85\n\t"
    "v_mfma_f32_16x16x32_bf16 v[236:239], a[204:207], v[76:79], v[236:239]\n\t"
    "v_add_f32 v40, v40, v85\n\t"
    "v_cvt_pk_bf16_f32 v74, v84, v85\n\t"
    "v_mfma_f32_16x16x32_bf16 v[240:243], a[208:211], v[76:79], v[240:243]\n\t"
    "v_exp_f32 v86, v86\n\t"
    "v_mfma_f32_16x16x32_bf16 v[244:247], a[212:215], v[76:79], v[244:247]\n\t"
    "ds_read_b64_tr_b16 a[190:191], v11 offset:13312\n\t"
    "v_add_f32 v40, v40, v86\n\t"
    "v_mfma_f32_16x16x32_bf16 v[248:251], a[216:219], v[76:79], v[248:251]\n\t"
    "v_exp_f32 v87, v87\n\t"
    "v_mfma_f32_16x16x32_bf16 v[252:255], a[220:223], v[76:79], v[252:255]\n\t"
    "s_barrier\n\t"
    "v_add_f32 v40, v40, v87\n\t"
    "v_cvt_pk_bf16_f32 v75, v86, v87\n\t"
    "v_mfma_f32_16x16x32_bf16 v[80:83], a[96:99], a[32:35], v[56:59]\n\t"
    "v_exp_f32 v88, v88\n\t"
    "v_mfma_f32_16x16x32_bf16 v[80:83], a[100:103], a[36:39], v[80:83]\n\t"
    "ds_read_b128 a[128:131], v4 offset:16384\n\t"
    "v_add_f32 v41, v41, v88\n\t"
    "v_mfma_f32_16x16x32_bf16 v[80:83], a[104:107], a[40:43], v[80:83]\n\t"
    "v_exp_f32 v89, v89\n\t"
    "v_mfma_f32_16x16x32_bf16 v[80:83], a[108:111], a[44:47], v[80:83]\n\t"
    "v_add_f32 v41, v41, v89\n\t"
    "v_cvt_pk_bf16_f32 v76, v88, v89\n\t"
    "v_mfma_f32_16x16x32_bf16 v[84:87], a[112:115], a[32:35], v[56:59]\n\t"
    "v_exp_f32 v90, v90\n\t"
    "v_mfma_f32_16x16x32_bf16 v[84:87], a[116:119], a[36:39], v[84:87]\n\t"
    "ds_read_b128 a[132:135], v5 offset:16384\n\t"
    "v_add_f32 v41, v41, v90\n\t"
    "v_mfma_f32_16x16x32_bf16 v[84:87], a[120:123], a[40:43], v[84:87]\n\t"
    "v_exp_f32 v91, v91\n\t"
    "v_mfma_f32_16x16x32_bf16 v[84:87], a[124:127], a[44:47], v[84:87]\n\t"
    "s_waitcnt lgkmcnt(2)\n\t"
    "v_add_f32 v41, v41, v91\n\t"
    "v_cvt_pk_bf16_f32 v77, v90, v91\n\t"
    "v_mfma_f32_16x16x32_bf16 a[224:227], a[160:163], v[72:75], a[224:227]\n\t"
    "v_exp_f32 v92, v92\n\t"
    "v_mfma_f32_16x16x32_bf16 a[228:231], a[164:167], v[72:75], a[228:231]\n\t"
    "ds_read_b128 a[136:139], v6 offset:16384\n\t"
    "v_add_f32 v41, v41, v92\n\t"
    "v_mfma_f32_16x16x32_bf16 a[232:235], a[168:171], v[72:75], a[232:235]\n\t"
    "v_exp_f32 v93, v93\n\t"
    "v_mfma_f32_16x16x32_bf16 a[236:239], a[172:175], v[72:75], a[236:239]\n\t"
    "v_add_f32 v41, v41, v93\n\t"
    "v_cvt_pk_bf16_f32 v78, v92, v93\n\t"
    "v_mfma_f32_16x16x32_bf16 a[240:243], a[176:179], v[72:75], a[240:243]\n\t"
    "v_exp_f32 v94, v94\n\t"
    "v_mfma_f32_16x16x32_bf16 a[244:247], a[180:183], v[72:75], a[244:247]\n\t"
    "ds_read_b128 a[140:143], v7 offset:16384\n\t"
    "v_add_f32 v41, v41, v94\n\t"
    "v_mfma_f32_16x16x32_bf16 a[248:251], a[184:187], v[72:75], a[248:251]\n\t"
    "v_exp_f32 v95, v95\n\t"
    "v_mfma_f32_16x16x32_bf16 a[252:255], a[188:191], v[72:75], a[252:255]\n\t"
    "v_add_f32 v41, v41, v95\n\t"
    "v_cvt_pk_bf16_f32 v79, v94, v95\n\t"
    "v_mfma_f32_16x16x32_bf16 v[88:91], a[96:99], a[48:51], v[60:63]\n\t"
    "v_exp_f32 v80, v80\n\t"
    "v_mfma_f32_16x16x32_bf16 v[88:91], a[100:103], a[52:55], v[88:91]\n\t"
    "ds_read_b128 a[144:147], v4 offset:20480\n\t"
    "v_add_f32 v42, v42, v80\n\t"
    "v_mfma_f32_16x16x32_bf16 v[88:91], a[104:107], a[56:59], v[88:91]\n\t"
    "v_exp_f32 v81, v81\n\t"
    "v_mfma_f32_16x16x32_bf16 v[88:91], a[108:111], a[60:63], v[88:91]\n\t"
    "v_add_f32 v42, v42, v81\n\t"
    "v_cvt_pk_bf16_f32 v72, v80, v81\n\t"
    "v_mfma_f32_16x16x32_bf16 v[92:95], a[112:115], a[48:51], v[60:63]\n\t"
    "v_exp_f32 v82, v82\n\t"
    "v_mfma_f32_16x16x32_bf16 v[92:95], a[116:119], a[52:55], v[92:95]\n\t"
    "ds_read_b128 a[148:151], v5 offset:20480\n\t"
    "v_add_f32 v42, v42, v82\n\t"
    "v_mfma_f32_16x16x32_bf16 v[92:95], a[120:123], a[56:59], v[92:95]\n\t"
    "v_exp_f32 v83, v83\n\t"
    "v_mfma_f32_16x16x32_bf16 v[92:95], a[124:127], a[60:63], v[92:95]\n\t"
    "v_add_f32 v42, v42, v83\n\t"
    "v_cvt_pk_bf16_f32 v73, v82, v83\n\t"
    "v_mfma_f32_16x16x32_bf16 v[96:99], a[160:163], v[76:79], v[96:99]\n\t"
    "v_exp_f32 v84, v84\n\t"
    "v_mfma_f32_16x16x32_bf16 v[100:103], a[164:167], v[76:79], v[100:103]\n\t"
    "ds_read_b128 a[152:155], v6 offset:20480\n\t"
    "v_add_f32 v42, v42, v84\n\t"
    "v_mfma_f32_16x16x32_bf16 v[104:107], a[168:171], v[76:79], v[104:107]\n\t"
    "v_exp_f32 v85, v85\n\t"
    "v_mfma_f32_16x16x32_bf16 v[108:111], a[172:175], v[76:79], v[108:111]\n\t"
    "v_add_f32 v42, v42, v85\n\t"
    "v_cvt_pk_bf16_f32 v74, v84, v85\n\t"
    "v_mfma_f32_16x16x32_bf16 v[112:115], a[176:179], v[76:79], v[112:115]\n\t"
    "v_exp_f32 v86, v86\n\t"
    "v_mfma_f32_16x16x32_bf16 v[116:119], a[180:183], v[76:79], v[116:119]\n\t"
    "ds_read_b128 a[156:159], v7 offset:20480\n\t"
    "v_add_f32 v42, v42, v86\n\t"
    "v_mfma_f32_16x16x32_bf16 v[120:123], a[184:187], v[76:79], v[120:123]\n\t"
    "v_exp_f32 v87, v87\n\t"
    "v_mfma_f32_16x16x32_bf16 v[124:127], a[188:191], v[76:79], v[124:127]\n\t"
    "v_add_f32 v42, v42, v87\n\t"
    "v_cvt_pk_bf16_f32 v75, v86, v87\n\t"
    "v_mfma_f32_16x16x32_bf16 v[80:83], a[96:99], a[64:67], v[64:67]\n\t"
    "v_exp_f32 v88, v88\n\t"
    "v_mfma_f32_16x16x32_bf16 v[80:83], a[100:103], a[68:71], v[80:83]\n\t"
    "ds_read_b64_tr_b16 a[192:193], v8 offset:24576\n\t"
    "v_add_f32 v43, v43, v88\n\t"
    "v_mfma_f32_16x16x32_bf16 v[80:83], a[104:107], a[72:75], v[80:83]\n\t"
    "v_exp_f32 v89, v89\n\t"
    "v_mfma_f32_16x16x32_bf16 v[80:83], a[108:111], a[76:79], v[80:83]\n\t"
    "v_add_f32 v43, v43, v89\n\t"
    "v_cvt_pk_bf16_f32 v76, v88, v89\n\t"
    "v_mfma_f32_16x16x32_bf16 v[84:87], a[112:115], a[64:67], v[64:67]\n\t"
    "v_exp_f32 v90, v90\n\t"
    "v_mfma_f32_16x16x32_bf16 v[84:87], a[116:119], a[68:71], v[84:87]\n\t"
    "ds_read_b64_tr_b16 a[194:195], v8 offset:28672\n\t"
    "v_add_f32 v43, v43, v90\n\t"
    "v_mfma_f32_16x16x32_bf16 v[84:87], a[120:123], a[72:75], v[84:87]\n\t"
    "v_exp_f32 v91, v91\n\t"
    "v_mfma_f32_16x16x32_bf16 v[84:87], a[124:127], a[76:79], v[84:87]\n\t"
    "v_add_f32 v43, v43, v91\n\t"
    "v_cvt_pk_bf16_f32 v77, v90, v91\n\t"
    "v_mfma_f32_16x16x32_bf16 v[128:131], a[160:163], v[72:75], v[128:131]\n\t"
    "v_exp_f32 v92, v92\n\t"
    "v_mfma_f32_16x16x32_bf16 v[132:135], a[164:167], v[72:75], v[132:135]\n\t"
    "ds_read_b64_tr_b16 a[196:197], v9 offset:24576\n\t"
    "v_add_f32 v43, v43, v92\n\t"
    "v_mfma_f32_16x16x32_bf16 v[136:139], a[168:171], v[72:75], v[136:139]\n\t"
    "v_exp_f32 v93, v93\n\t"
    "v_mfma_f32_16x16x32_bf16 v[140:143], a[172:175], v[72:75], v[140:143]\n\t"
    "v_add_f32 v43, v43, v93\n\t"
    "v_cvt_pk_bf16_f32 v78, v92, v93\n\t"
    "v_mfma_f32_16x16x32_bf16 v[144:147], a[176:179], v[72:75], v[144:147]\n\t"
    "v_exp_f32 v94, v94\n\t"
    "v_mfma_f32_16x16x32_bf16 v[148:151], a[180:183], v[72:75], v[148:151]\n\t"
    "ds_read_b64_tr_b16 a[198:199], v9 offset:28672\n\t"
    "v_add_f32 v43, v43, v94\n\t"
    "v_mfma_f32_16x16x32_bf16 v[152:155], a[184:187], v[72:75], v[152:155]\n\t"
    "v_exp_f32 v95, v95\n\t"
    "v_mfma_f32_16x16x32_bf16 v[156:159], a[188:191], v[72:75], v[156:159]\n\t"
    "v_add_f32 v43, v43, v95\n\t"
    "v_cvt_pk_bf16_f32 v79, v94, v95\n\t"
    "v_mfma_f32_16x16x32_bf16 v[88:91], a[96:99], a[80:83], v[68:71]\n\t"
    "v_exp_f32 v80, v80\n\t"
    "v_mfma_f32_16x16x32_bf16 v[88:91], a[100:103], a[84:87], v[88:91]\n\t"
    "ds_read_b64_tr_b16 a[200:201], v10 offset:24576\n\t"
    "v_add_f32 v44, v44, v80\n\t"
    "v_mfma_f32_16x16x32_bf16 v[88:91], a[104:107], a[88:91], v[88:91]\n\t"
    "v_exp_f32 v81, v81\n\t"
    "v_mfma_f32_16x16x32_bf16 v[88:91], a[108:111], a[92:95], v[88:91]\n\t"
    "v_add_f32 v44, v44, v81\n\t"
    "v_cvt_pk_bf16_f32 v72, v80, v81\n\t"
    "v_mfma_f32_16x16x32_bf16 v[92:95], a[112:115], a[80:83], v[68:71]\n\t"
    "v_exp_f32 v82, v82\n\t"
    "v_mfma_f32_16x16x32_bf16 v[92:95], a[116:119], a[84:87], v[92:95]\n\t"
    "ds_read_b64_tr_b16 a[202:203], v10 offset:28672\n\t"
    "v_add_f32 v44, v44, v82\n\t"
    "v_mfma_f32_16x16x32_bf16 v[92:95], a[120:123], a[88:91], v[92:95]\n\t"
    "v_exp_f32 v83, v83\n\t"
    "v_mfma_f32_16x16x32_bf16 v[92:95], a[124:127], a[92:95], v[92:95]\n\t"
    "v_add_f32 v44, v44, v83\n\t"
    "v_cvt_pk_bf16_f32 v73, v82, v83\n\t"
    "v_mfma_f32_16x16x32_bf16 v[160:163], a[160:163], v[76:79], v[160:163]\n\t"
    "v_exp_f32 v84, v84\n\t"
    "v_mfma_f32_16x16x32_bf16 v[164:167], a[164:167], v[76:79], v[164:167]\n\t"
    "ds_read_b64_tr_b16 a[204:205], v11 offset:24576\n\t"
    "v_add_f32 v44, v44, v84\n\t"
    "v_mfma_f32_16x16x32_bf16 v[168:171], a[168:171], v[76:79], v[168:171]\n\t"
    "v_exp_f32 v85, v85\n\t"
    "v_mfma_f32_16x16x32_bf16 v[172:175], a[172:175], v[76:79], v[172:175]\n\t"
    "v_add_f32 v44, v44, v85\n\t"
    "v_cvt_pk_bf16_f32 v74, v84, v85\n\t"
    "v_mfma_f32_16x16x32_bf16 v[176:179], a[176:179], v[76:79], v[176:179]\n\t"
    "v_exp_f32 v86, v86\n\t"
    "v_mfma_f32_16x16x32_bf16 v[180:183], a[180:183], v[76:79], v[180:183]\n\t"
    "ds_read_b64_tr_b16 a[206:207], v11 offset:28672\n\t"
    "v_add_f32 v44, v44, v86\n\t"
    "v_mfma_f32_16x16x32_bf16 v[184:187], a[184:187], v[76:79], v[184:187]\n\t"
    "v_exp_f32 v87, v87\n\t"
    "v_mfma_f32_16x16x32_bf16 v[188:191], a[188:191], v[76:79], v[188:191]\n\t"
    "s_waitcnt lgkmcnt(8)\n\t"
    "v_add_f32 v44, v44, v87\n\t"
    "v_cvt_pk_bf16_f32 v75, v86, v87\n\t"
    "v_mfma_f32_16x16x32_bf16 v[80:83], a[128:131], a[0:3], v[48:51]\n\t"
    "v_exp_f32 v88, v88\n\t"
    "v_mfma_f32_16x16x32_bf16 v[80:83], a[132:135], a[4:7], v[80:83]\n\t"
    "ds_read_b64_tr_b16 a[208:209], v8 offset:25600\n\t"
    "v_add_f32 v45, v45, v88\n\t"
    "v_mfma_f32_16x16x32_bf16 v[80:83], a[136:139], a[8:11], v[80:83]\n\t"
    "v_exp_f32 v89, v89\n\t"
    "v_mfma_f32_16x16x32_bf16 v[80:83], a[140:143], a[12:15], v[80:83]\n\t"
    "v_add_f32 v45, v45, v89\n\t"
    "v_cvt_pk_bf16_f32 v76, v88, v89\n\t"
    "v_mfma_f32_16x16x32_bf16 v[84:87], a[144:147], a[0:3], v[48:51]\n\t"
    "v_exp_f32 v90, v90\n\t"
    "v_mfma_f32_16x16x32_bf16 v[84:87], a[148:151], a[4:7], v[84:87]\n\t"
    "ds_read_b64_tr_b16 a[210:211], v8 offset:29696\n\t"
    "v_add_f32 v45, v45, v90\n\t"
    "v_mfma_f32_16x16x32_bf16 v[84:87], a[152:155], a[8:11], v[84:87]\n\t"
    "v_exp_f32 v91, v91\n\t"
    "v_mfma_f32_16x16x32_bf16 v[84:87], a[156:159], a[12:15], v[84:87]\n\t"
    "v_add_f32 v45, v45, v91\n\t"
    "v_cvt_pk_bf16_f32 v77, v90, v91\n\t"
    "v_mfma_f32_16x16x32_bf16 v[192:195], a[160:163], v[72:75], v[192:195]\n\t"
    "v_exp_f32 v92, v92\n\t"
    "v_mfma_f32_16x16x32_bf16 v[196:199], a[164:167], v[72:75], v[196:199]\n\t"
    "ds_read_b64_tr_b16 a[212:213], v9 offset:25600\n\t"
    "v_add_f32 v45, v45, v92\n\t"
    "v_mfma_f32_16x16x32_bf16 v[200:203], a[168:171], v[72:75], v[200:203]\n\t"
    "v_exp_f32 v93, v93\n\t"
    "v_mfma_f32_16x16x32_bf16 v[204:207], a[172:175], v[72:75], v[204:207]\n\t"
    "v_add_f32 v45, v45, v93\n\t"
    "v_cvt_pk_bf16_f32 v78, v92, v93\n\t"
    "v_mfma_f32_16x16x32_bf16 v[208:211], a[176:179], v[72:75], v[208:211]\n\t"
    "v_exp_f32 v94, v94\n\t"
    "v_mfma_f32_16x16x32_bf16 v[212:215], a[180:183], v[72:75], v[212:215]\n\t"
    "ds_read_b64_tr_b16 a[214:215], v9 offset:29696\n\t"
    "v_add_f32 v45, v45, v94\n\t"
    "v_mfma_f32_16x16x32_bf16 v[216:219], a[184:187], v[72:75], v[216:219]\n\t"
    "v_exp_f32 v95, v95\n\t"
    "v_mfma_f32_16x16x32_bf16 v[220:223], a[188:191], v[72:75], v[220:223]\n\t"
    "v_add_f32 v45, v45, v95\n\t"
    "v_cvt_pk_bf16_f32 v79, v94, v95\n\t"
    "v_mfma_f32_16x16x32_bf16 v[88:91], a[128:131], a[16:19], v[52:55]\n\t"
    "v_exp_f32 v80, v80\n\t"
    "v_mfma_f32_16x16x32_bf16 v[88:91], a[132:135], a[20:23], v[88:91]\n\t"
    "ds_read_b64_tr_b16 a[216:217], v10 offset:25600\n\t"
    "v_add_f32 v40, v40, v80\n\t"
    "v_mfma_f32_16x16x32_bf16 v[88:91], a[136:139], a[24:27], v[88:91]\n\t"
    "v_exp_f32 v81, v81\n\t"
    "v_mfma_f32_16x16x32_bf16 v[88:91], a[140:143], a[28:31], v[88:91]\n\t"
    "v_add_f32 v40, v40, v81\n\t"
    "v_cvt_pk_bf16_f32 v72, v80, v81\n\t"
    "v_mfma_f32_16x16x32_bf16 v[92:95], a[144:147], a[16:19], v[52:55]\n\t"
    "v_exp_f32 v82, v82\n\t"
    "v_mfma_f32_16x16x32_bf16 v[92:95], a[148:151], a[20:23], v[92:95]\n\t"
    "ds_read_b64_tr_b16 a[218:219], v10 offset:29696\n\t"
    "v_add_f32 v40, v40, v82\n\t"
    "v_mfma_f32_16x16x32_bf16 v[92:95], a[152:155], a[24:27], v[92:95]\n\t"
    "v_exp_f32 v83, v83\n\t"
    "v_mfma_f32_16x16x32_bf16 v[92:95], a[156:159], a[28:31], v[92:95]\n\t"
    "v_add_f32 v40, v40, v83\n\t"
    "v_cvt_pk_bf16_f32 v73, v82, v83\n\t"
    "v_mfma_f32_16x16x32_bf16 v[224:227], a[160:163], v[76:79], v[224:227]\n\t"
    "v_exp_f32 v84, v84\n\t"
    "v_mfma_f32_16x16x32_bf16 v[228:231], a[164:167], v[76:79], v[228:231]\n\t"
    "ds_read_b64_tr_b16 a[220:221], v11 offset:25600\n\t"
    "v_add_f32 v40, v40, v84\n\t"
    "v_mfma_f32_16x16x32_bf16 v[232:235], a[168:171], v[76:79], v[232:235]\n\t"
    "v_exp_f32 v85, v85\n\t"
    "v_mfma_f32_16x16x32_bf16 v[236:239], a[172:175], v[76:79], v[236:239]\n\t"
    "v_add_f32 v40, v40, v85\n\t"
    "v_cvt_pk_bf16_f32 v74, v84, v85\n\t"
    "v_mfma_f32_16x16x32_bf16 v[240:243], a[176:179], v[76:79], v[240:243]\n\t"
    "v_exp_f32 v86, v86\n\t"
    "v_mfma_f32_16x16x32_bf16 v[244:247], a[180:183], v[76:79], v[244:247]\n\t"
    "ds_read_b64_tr_b16 a[222:223], v11 offset:29696\n\t"
    "v_add_f32 v40, v40, v86\n\t"
    "v_mfma_f32_16x16x32_bf16 v[248:251], a[184:187], v[76:79], v[248:251]\n\t"
    "v_exp_f32 v87, v87\n\t"
    "v_mfma_f32_16x16x32_bf16 v[252:255], a[188:191], v[76:79], v[252:255]\n\t"
    "s_barrier\n\t"
    "v_add_f32 v40, v40, v87\n\t"
    "v_cvt_pk_bf16_f32 v75, v86, v87\n\t"
    "v_mfma_f32_16x16x32_bf16 v[80:83], a[128:131], a[32:35], v[56:59]\n\t"
    "v_exp_f32 v88, v88\n\t"
    "v_mfma_f32_16x16x32_bf16 v[80:83], a[132:135], a[36:39], v[80:83]\n\t"
    "ds_read_b128 a[96:99], v4 offset:32768\n\t"
    "v_add_f32 v41, v41, v88\n\t"
    "v_mfma_f32_16x16x32_bf16 v[80:83], a[136:139], a[40:43], v[80:83]\n\t"
    "v_exp_f32 v89, v89\n\t"
    "v_mfma_f32_16x16x32_bf16 v[80:83], a[140:143], a[44:47], v[80:83]\n\t"
    "v_add_f32 v41, v41, v89\n\t"
    "v_cvt_pk_bf16_f32 v76, v88, v89\n\t"
    "v_mfma_f32_16x16x32_bf16 v[84:87], a[144:147], a[32:35], v[56:59]\n\t"
    "v_exp_f32 v90, v90\n\t"
    "v_mfma_f32_16x16x32_bf16 v[84:87], a[148:151], a[36:39], v[84:87]\n\t"
    "ds_read_b128 a[100:103], v5 offset:32768\n\t"
    "v_add_f32 v41, v41, v90\n\t"
    "v_mfma_f32_16x16x32_bf16 v[84:87], a[152:155], a[40:43], v[84:87]\n\t"
    "v_exp_f32 v91, v91\n\t"
    "v_mfma_f32_16x16x32_bf16 v[84:87], a[156:159], a[44:47], v[84:87]\n\t"
    "s_waitcnt lgkmcnt(2)\n\t"
    "v_add_f32 v41, v41, v91\n\t"
    "v_cvt_pk_bf16_f32 v77, v90, v91\n\t"
    "v_mfma_f32_16x16x32_bf16 a[224:227], a[192:195], v[72:75], a[224:227]\n\t"
    "v_exp_f32 v92, v92\n\t"
    "v_mfma_f32_16x16x32_bf16 a[228:231], a[196:199], v[72:75], a[228:231]\n\t"
    "ds_read_b128 a[104:107], v6 offset:32768\n\t"
    "v_add_f32 v41, v41, v92\n\t"
    "v_mfma_f32_16x16x32_bf16 a[232:235], a[200:203], v[72:75], a[232:235]\n\t"
    "v_exp_f32 v93, v93\n\t"
    "v_mfma_f32_16x16x32_bf16 a[236:239], a[204:207], v[72:75], a[236:239]\n\t"
    "v_add_f32 v41, v41, v93\n\t"
    "v_cvt_pk_bf16_f32 v78, v92, v93\n\t"
    "v_mfma_f32_16x16x32_bf16 a[240:243], a[208:211], v[72:75], a[240:243]\n\t"
    "v_exp_f32 v94, v94\n\t"
    "v_mfma_f32_16x16x32_bf16 a[244:247], a[212:215], v[72:75], a[244:247]\n\t"
    "ds_read_b128 a[108:111], v7 offset:32768\n\t"
    "v_add_f32 v41, v41, v94\n\t"
    "v_mfma_f32_16x16x32_bf16 a[248:251], a[216:219], v[72:75], a[248:251]\n\t"
    "v_exp_f32 v95, v95\n\t"
    "v_mfma_f32_16x16x32_bf16 a[252:255], a[220:223], v[72:75], a[252:255]\n\t"
    "v_add_f32 v41, v41, v95\n\t"
    "v_cvt_pk_bf16_f32 v79, v94, v95\n\t"
    "v_mfma_f32_16x16x32_bf16 v[88:91], a[128:131], a[48:51], v[60:63]\n\t"
    "v_exp_f32 v80, v80\n\t"
    "v_mfma_f32_16x16x32_bf16 v[88:91], a[132:135], a[52:55], v[88:91]\n\t"
    "ds_read_b128 a[112:115], v4 offset:36864\n\t"
    "v_add_f32 v42, v42, v80\n\t"
    "v_mfma_f32_16x16x32_bf16 v[88:91], a[136:139], a[56:59], v[88:91]\n\t"
    "v_exp_f32 v81, v81\n\t"
    "v_mfma_f32_16x16x32_bf16 v[88:91], a[140:143], a[60:63], v[88:91]\n\t"
    "v_add_f32 v42, v42, v81\n\t"
    "v_cvt_pk_bf16_f32 v72, v80, v81\n\t"
    "v_mfma_f32_16x16x32_bf16 v[92:95], a[144:147], a[48:51], v[60:63]\n\t"
    "v_exp_f32 v82, v82\n\t"
    "v_mfma_f32_16x16x32_bf16 v[92:95], a[148:151], a[52:55], v[92:95]\n\t"
    "ds_read_b128 a[116:119], v5 offset:36864\n\t"
    "v_add_f32 v42, v42, v82\n\t"
    "v_mfma_f32_16x16x32_bf16 v[92:95], a[152:155], a[56:59], v[92:95]\n\t"
    "v_exp_f32 v83, v83\n\t"
    "v_mfma_f32_16x16x32_bf16 v[92:95], a[156:159], a[60:63], v[92:95]\n\t"
    "v_add_f32 v42, v42, v83\n\t"
    "v_cvt_pk_bf16_f32 v73, v82, v83\n\t"
    "v_mfma_f32_16x16x32_bf16 v[96:99], a[192:195], v[76:79], v[96:99]\n\t"
    "v_exp_f32 v84, v84\n\t"
    "v_mfma_f32_16x16x32_bf16 v[100:103], a[196:199], v[76:79], v[100:103]\n\t"
    "ds_read_b128 a[120:123], v6 offset:36864\n\t"
    "v_add_f32 v42, v42, v84\n\t"
    "v_mfma_f32_16x16x32_bf16 v[104:107], a[200:203], v[76:79], v[104:107]\n\t"
    "v_exp_f32 v85, v85\n\t"
    "v_mfma_f32_16x16x32_bf16 v[108:111], a[204:207], v[76:79], v[108:111]\n\t"
    "v_add_f32 v42, v42, v85\n\t"
    "v_cvt_pk_bf16_f32 v74, v84, v85\n\t"
    "v_mfma_f32_16x16x32_bf16 v[112:115], a[208:211], v[76:79], v[112:115]\n\t"
    "v_exp_f32 v86, v86\n\t"
    "v_mfma_f32_16x16x32_bf16 v[116:119], a[212:215], v[76:79], v[116:119]\n\t"
    "ds_read_b128 a[124:127], v7 offset:36864\n\t"
    "v_add_f32 v42, v42, v86\n\t"
    "v_mfma_f32_16x16x32_bf16 v[120:123], a[216:219], v[76:79], v[120:123]\n\t"
    "v_exp_f32 v87, v87\n\t"
    "v_mfma_f32_16x16x32_bf16 v[124:127], a[220:223], v[76:79], v[124:127]\n\t"
    "v_add_f32 v42, v42, v87\n\t"
    "v_cvt_pk_bf16_f32 v75, v86, v87\n\t"
    "v_mfma_f32_16x16x32_bf16 v[80:83], a[128:131], a[64:67], v[64:67]\n\t"
    "v_exp_f32 v88, v88\n\t"
    "v_mfma_f32_16x16x32_bf16 v[80:83], a[132:135], a[68:71], v[80:83]\n\t"
    "ds_read_b64_tr_b16 a[160:161], v8 offset:40960\n\t"
    "v_add_f32 v43, v43, v88\n\t"
    "v_mfma_f32_16x16x32_bf16 v[80:83], a[136:139], a[72:75], v[80:83]\n\t"
    "v_exp_f32 v89, v89\n\t"
    "v_mfma_f32_16x16x32_bf16 v[80:83], a[140:143], a[76:79], v[80:83]\n\t"
    "v_add_f32 v43, v43, v89\n\t"
    "v_cvt_pk_bf16_f32 v76, v88, v89\n\t"
    "v_mfma_f32_16x16x32_bf16 v[84:87], a[144:147], a[64:67], v[64:67]\n\t"
    "v_exp_f32 v90, v90\n\t"
    "v_mfma_f32_16x16x32_bf16 v[84:87], a[148:151], a[68:71], v[84:87]\n\t"
    "ds_read_b64_tr_b16 a[162:163], v8 offset:45056\n\t"
    "v_add_f32 v43, v43, v90\n\t"
    "v_mfma_f32_16x16x32_bf16 v[84:87], a[152:155], a[72:75], v[84:87]\n\t"
    "v_exp_f32 v91, v91\n\t"
    "v_mfma_f32_16x16x32_bf16 v[84:87], a[156:159], a[76:79], v[84:87]\n\t"
    "v_add_f32 v43, v43, v91\n\t"
    "v_cvt_pk_bf16_f32 v77, v90, v91\n\t"
    "v_mfma_f32_16x16x32_bf16 v[128:131], a[192:195], v[72:75], v[128:131]\n\t"
    "v_exp_f32 v92, v92\n\t"
    "v_mfma_f32_16x16x32_bf16 v[132:135], a[196:199], v[72:75], v[132:135]\n\t"
    "ds_read_b64_tr_b16 a[164:165], v9 offset:40960\n\t"
    "v_add_f32 v43, v43, v92\n\t"
    "v_mfma_f32_16x16x32_bf16 v[136:139], a[200:203], v[72:75], v[136:139]\n\t"
    "v_exp_f32 v93, v93\n\t"
    "v_mfma_f32_16x16x32_bf16 v[140:143], a[204:207], v[72:75], v[140:143]\n\t"
    "v_add_f32 v43, v43, v93\n\t"
    "v_cvt_pk_bf16_f32 v78, v92, v93\n\t"
    "v_mfma_f32_16x16x32_bf16 v[144:147], a[208:211], v[72:75], v[144:147]\n\t"
    "v_exp_f32 v94, v94\n\t"
    "v_mfma_f32_16x16x32_bf16 v[148:151], a[212:215], v[72:75], v[148:151]\n\t"
    "ds_read_b64_tr_b16 a[166:167], v9 offset:45056\n\t"
    "v_add_f32 v43, v43, v94\n\t"
    "v_mfma_f32_16x16x32_bf16 v[152:155], a[216:219], v[72:75], v[152:155]\n\t"
    "v_exp_f32 v95, v95\n\t"
    "v_mfma_f32_16x16x32_bf16 v[156:159], a[220:223], v[72:75], v[156:159]\n\t"
    "v_add_f32 v43, v43, v95\n\t"
    "v_cvt_pk_bf16_f32 v79, v94, v95\n\t"
    "v_mfma_f32_16x16x32_bf16 v[88:91], a[128:131], a[80:83], v[68:71]\n\t"
    "v_exp_f32 v80, v80\n\t"
    "v_mfma_f32_16x16x32_bf16 v[88:91], a[132:135], a[84:87], v[88:91]\n\t"
    "ds_read_b64_tr_b16 a[168:169], v10 offset:40960\n\t"
    "v_add_f32 v44, v44, v80\n\t"
    "v_mfma_f32_16x16x32_bf16 v[88:91], a[136:139], a[88:91], v[88:91]\n\t"
    "v_exp_f32 v81, v81\n\t"
    "v_mfma_f32_16x16x32_bf16 v[88:91], a[140:143], a[92:95], v[88:91]\n\t"
    "v_add_f32 v44, v44, v81\n\t"
    "v_cvt_pk_bf16_f32 v72, v80, v81\n\t"
    "v_mfma_f32_16x16x32_bf16 v[92:95], a[144:147], a[80:83], v[68:71]\n\t"
    "v_exp_f32 v82, v82\n\t"
    "v_mfma_f32_16x16x32_bf16 v[92:95], a[148:151], a[84:87], v[92:95]\n\t"
    "ds_read_b64_tr_b16 a[170:171], v10 offset:45056\n\t"
    "v_add_f32 v44, v44, v82\n\t"
    "v_mfma_f32_16x16x32_bf16 v[92:95], a[152:155], a[88:91], v[92:95]\n\t"
    "v_exp_f32 v83, v83\n\t"
    "v_mfma_f32_16x16x32_bf16 v[92:95], a[156:159], a[92:95], v[92:95]\n\t"
    "v_add_f32 v44, v44, v83\n\t"
    "v_cvt_pk_bf16_f32 v73, v82, v83\n\t"
    "v_mfma_f32_16x16x32_bf16 v[160:163], a[192:195], v[76:79], v[160:163]\n\t"
    "v_exp_f32 v84, v84\n\t"
    "v_mfma_f32_16x16x32_bf16 v[164:167], a[196:199], v[76:79], v[164:167]\n\t"
    "ds_read_b64_tr_b16 a[172:173], v11 offset:40960\n\t"
    "v_add_f32 v44, v44, v84\n\t"
    "v_mfma_f32_16x16x32_bf16 v[168:171], a[200:203], v[76:79], v[168:171]\n\t"
    "v_exp_f32 v85, v85\n\t"
    "v_mfma_f32_16x16x32_bf16 v[172:175], a[204:207], v[76:79], v[172:175]\n\t"
    "v_add_f32 v44, v44, v85\n\t"
    "v_cvt_pk_bf16_f32 v74, v84, v85\n\t"
    "v_mfma_f32_16x16x32_bf16 v[176:179], a[208:211], v[76:79], v[176:179]\n\t"
    "v_exp_f32 v86, v86\n\t"
    "v_mfma_f32_16x16x32_bf16 v[180:183], a[212:215], v[76:79], v[180:183]\n\t"
    "ds_read_b64_tr_b16 a[174:175], v11 offset:45056\n\t"
    "v_add_f32 v44, v44, v86\n\t"
    "v_mfma_f32_16x16x32_bf16 v[184:187], a[216:219], v[76:79], v[184:187]\n\t"
    "v_exp_f32 v87, v87\n\t"
    "v_mfma_f32_16x16x32_bf16 v[188:191], a[220:223], v[76:79], v[188:191]\n\t"
    "s_waitcnt lgkmcnt(8)\n\t"
    "v_add_f32 v44, v44, v87\n\t"
    "v_cvt_pk_bf16_f32 v75, v86, v87\n\t"
    "v_mfma_f32_16x16x32_bf16 v[80:83], a[96:99], a[0:3], v[48:51]\n\t"
    "v_exp_f32 v88, v88\n\t"
    "v_mfma_f32_16x16x32_bf16 v[80:83], a[100:103], a[4:7], v[80:83]\n\t"
    "ds_read_b64_tr_b16 a[176:177], v8 offset:41984\n\t"
    "v_add_f32 v45, v45, v88\n\t"
    "v_mfma_f32_16x16x32_bf16 v[80:83], a[104:107], a[8:11], v[80:83]\n\t"
    "v_exp_f32 v89, v89\n\t"
    "v_mfma_f32_16x16x32_bf16 v[80:83], a[108:111], a[12:15], v[80:83]\n\t"
    "v_add_f32 v45, v45, v89\n\t"
    "v_cvt_pk_bf16_f32 v76, v88, v89\n\t"
    "v_mfma_f32_16x16x32_bf16 v[84:87], a[112:115], a[0:3], v[48:51]\n\t"
    "v_exp_f32 v90, v90\n\t"
    "v_mfma_f32_16x16x32_bf16 v[84:87], a[116:119], a[4:7], v[84:87]\n\t"
    "ds_read_b64_tr_b16 a[178:179], v8 offset:46080\n\t"
    "v_add_f32 v45, v45, v90\n\t"
    "v_mfma_f32_16x16x32_bf16 v[84:87], a[120:123], a[8:11], v[84:87]\n\t"
    "v_exp_f32 v91, v91\n\t"
    "v_mfma_f32_16x16x32_bf16 v[84:87], a[124:127], a[12:15], v[84:87]\n\t"
    "v_add_f32 v45, v45, v91\n\t"
    "v_cvt_pk_bf16_f32 v77, v90, v91\n\t"
    "v_mfma_f32_16x16x32_bf16 v[192:195], a[192:195], v[72:75], v[192:195]\n\t"
    "v_exp_f32 v92, v92\n\t"
    "v_mfma_f32_16x16x32_bf16 v[196:199], a[196:199], v[72:75], v[196:199]\n\t"
    "ds_read_b64_tr_b16 a[180:181], v9 offset:41984\n\t"
    "v_add_f32 v45, v45, v92\n\t"
    "v_mfma_f32_16x16x32_bf16 v[200:203], a[200:203], v[72:75], v[200:203]\n\t"
    "v_exp_f32 v93, v93\n\t"
    "v_mfma_f32_16x16x32_bf16 v[204:207], a[204:207], v[72:75], v[204:207]\n\t"
    "v_add_f32 v45, v45, v93\n\t"
    "v_cvt_pk_bf16_f32 v78, v92, v93\n\t"
    "v_mfma_f32_16x16x32_bf16 v[208:211], a[208:211], v[72:75], v[208:211]\n\t"
    "v_exp_f32 v94, v94\n\t"
    "v_mfma_f32_16x16x32_bf16 v[212:215], a[212:215], v[72:75], v[212:215]\n\t"
    "ds_read_b64_tr_b16 a[182:183], v9 offset:46080\n\t"
    "v_add_f32 v45, v45, v94\n\t"
    "v_mfma_f32_16x16x32_bf16 v[216:219], a[216:219], v[72:75], v[216:219]\n\t"
    "v_exp_f32 v95, v95\n\t"
    "v_mfma_f32_16x16x32_bf16 v[220:223], a[220:223], v[72:75], v[220:223]\n\t"
    "v_add_f32 v45, v45, v95\n\t"
    "v_cvt_pk_bf16_f32 v79, v94, v95\n\t"
    "v_mfma_f32_16x16x32_bf16 v[88:91], a[96:99], a[16:19], v[52:55]\n\t"
    "v_exp_f32 v80, v80\n\t"
    "v_mfma_f32_16x16x32_bf16 v[88:91], a[100:103], a[20:23], v[88:91]\n\t"
    "ds_read_b64_tr_b16 a[184:185], v10 offset:41984\n\t"
    "v_add_f32 v40, v40, v80\n\t"
    "v_mfma_f32_16x16x32_bf16 v[88:91], a[104:107], a[24:27], v[88:91]\n\t"
    "v_exp_f32 v81, v81\n\t"
    "v_mfma_f32_16x16x32_bf16 v[88:91], a[108:111], a[28:31], v[88:91]\n\t"
    "v_add_f32 v40, v40, v81\n\t"
    "v_cvt_pk_bf16_f32 v72, v80, v81\n\t"
    "v_mfma_f32_16x16x32_bf16 v[92:95], a[112:115], a[16:19], v[52:55]\n\t"
    "v_exp_f32 v82, v82\n\t"
    "v_mfma_f32_16x16x32_bf16 v[92:95], a[116:119], a[20:23], v[92:95]\n\t"
    "ds_read_b64_tr_b16 a[186:187], v10 offset:46080\n\t"
    "v_add_f32 v40, v40, v82\n\t"
    "v_mfma_f32_16x16x32_bf16 v[92:95], a[120:123], a[24:27], v[92:95]\n\t"
    "v_exp_f32 v83, v83\n\t"
    "v_mfma_f32_16x16x32_bf16 v[92:95], a[124:127], a[28:31], v[92:95]\n\t"
    "v_add_f32 v40, v40, v83\n\t"
    "v_cvt_pk_bf16_f32 v73, v82, v83\n\t"
    "v_mfma_f32_16x16x32_bf16 v[224:227], a[192:195], v[76:79], v[224:227]\n\t"
    "v_exp_f32 v84, v84\n\t"
    "v_mfma_f32_16x16x32_bf16 v[228:231], a[196:199], v[76:79], v[228:231]\n\t"
    "ds_read_b64_tr_b16 a[188:189], v11 offset:41984\n\t"
    "v_add_f32 v40, v40, v84\n\t"
    "v_mfma_f32_16x16x32_bf16 v[232:235], a[200:203], v[76:79], v[232:235]\n\t"
    "v_exp_f32 v85, v85\n\t"
    "v_mfma_f32_16x16x32_bf16 v[236:239], a[204:207], v[76:79], v[236:239]\n\t"
    "v_add_f32 v40, v40, v85\n\t"
    "v_cvt_pk_bf16_f32 v74, v84, v85\n\t"
    "v_mfma_f32_16x16x32_bf16 v[240:243], a[208:211], v[76:79], v[240:243]\n\t"
    "v_exp_f32 v86, v86\n\t"
    "v_mfma_f32_16x16x32_bf16 v[244:247], a[212:215], v[76:79], v[244:247]\n\t"
    "ds_read_b64_tr_b16 a[190:191], v11 offset:46080\n\t"
    "v_add_f32 v40, v40, v86\n\t"
    "v_mfma_f32_16x16x32_bf16 v[248:251], a[216:219], v[76:79], v[248:251]\n\t"
    "v_exp_f32 v87, v87\n\t"
    "v_mfma_f32_16x16x32_bf16 v[252:255], a[220:223], v[76:79], v[252:255]\n\t"
    "s_barrier\n\t"
    "v_add_f32 v40, v40, v87\n\t"
    "v_cvt_pk_bf16_f32 v75, v86, v87\n\t"
    "v_mfma_f32_16x16x32_bf16 v[80:83], a[96:99], a[32:35], v[56:59]\n\t"
    "v_exp_f32 v88, v88\n\t"
    "v_mfma_f32_16x16x32_bf16 v[80:83], a[100:103], a[36:39], v[80:83]\n\t"
    "ds_read_b128 a[128:131], v4 offset:49152\n\t"
    "v_add_f32 v41, v41, v88\n\t"
    "v_mfma_f32_16x16x32_bf16 v[80:83], a[104:107], a[40:43], v[80:83]\n\t"
    "v_exp_f32 v89, v89\n\t"
    "v_mfma_f32_16x16x32_bf16 v[80:83], a[108:111], a[44:47], v[80:83]\n\t"
    "v_add_f32 v41, v41, v89\n\t"
    "v_cvt_pk_bf16_f32 v76, v88, v89\n\t"
    "v_mfma_f32_16x16x32_bf16 v[84:87], a[112:115], a[32:35], v[56:59]\n\t"
    "v_exp_f32 v90, v90\n\t"
    "v_mfma_f32_16x16x32_bf16 v[84:87], a[116:119], a[36:39], v[84:87]\n\t"
    "ds_read_b128 a[132:135], v5 offset:49152\n\t"
    "v_add_f32 v41, v41, v90\n\t"
    "v_mfma_f32_16x16x32_bf16 v[84:87], a[120:123], a[40:43], v[84:87]\n\t"
    "v_exp_f32 v91, v91\n\t"
    "v_mfma_f32_16x16x32_bf16 v[84:87], a[124:127], a[44:47], v[84:87]\n\t"
    "s_waitcnt lgkmcnt(2)\n\t"
    "v_add_f32 v41, v41, v91\n\t"
    "v_cvt_pk_bf16_f32 v77, v90, v91\n\t"
    "v_mfma_f32_16x16x32_bf16 a[224:227], a[160:163], v[72:75], a[224:227]\n\t"
    "v_exp_f32 v92, v92\n\t"
    "v_mfma_f32_16x16x32_bf16 a[228:231], a[164:167], v[72:75], a[228:231]\n\t"
    "ds_read_b128 a[136:139], v6 offset:49152\n\t"
    "v_add_f32 v41, v41, v92\n\t"
    "v_mfma_f32_16x16x32_bf16 a[232:235], a[168:171], v[72:75], a[232:235]\n\t"
    "v_exp_f32 v93, v93\n\t"
    "v_mfma_f32_16x16x32_bf16 a[236:239], a[172:175], v[72:75], a[236:239]\n\t"
    "v_add_f32 v41, v41, v93\n\t"
    "v_cvt_pk_bf16_f32 v78, v92, v93\n\t"
    "v_mfma_f32_16x16x32_bf16 a[240:243], a[176:179], v[72:75], a[240:243]\n\t"
    "v_exp_f32 v94, v94\n\t"
    "v_mfma_f32_16x16x32_bf16 a[244:247], a[180:183], v[72:75], a[244:247]\n\t"
    "ds_read_b128 a[140:143], v7 offset:49152\n\t"
    "v_add_f32 v41, v41, v94\n\t"
    "v_mfma_f32_16x16x32_bf16 a[248:251], a[184:187], v[72:75], a[248:251]\n\t"
    "v_exp_f32 v95, v95\n\t"
    "v_mfma_f32_16x16x32_bf16 a[252:255], a[188:191], v[72:75], a[252:255]\n\t"
    "v_add_f32 v41, v41, v95\n\t"
    "v_cvt_pk_bf16_f32 v79, v94, v95\n\t"
    "v_mfma_f32_16x16x32_bf16 v[88:91], a[96:99], a[48:51], v[60:63]\n\t"
    "v_exp_f32 v80, v80\n\t"
    "v_mfma_f32_16x16x32_bf16 v[88:91], a[100:103], a[52:55], v[88:91]\n\t"
    "ds_read_b128 a[144:147], v4 offset:53248\n\t"
    "v_add_f32 v42, v42, v80\n\t"
    "v_mfma_f32_16x16x32_bf16 v[88:91], a[104:107], a[56:59], v[88:91]\n\t"
    "v_exp_f32 v81, v81\n\t"
    "v_mfma_f32_16x16x32_bf16 v[88:91], a[108:111], a[60:63], v[88:91]\n\t"
    "v_add_f32 v42, v42, v81\n\t"
    "v_cvt_pk_bf16_f32 v72, v80, v81\n\t"
    "v_mfma_f32_16x16x32_bf16 v[92:95], a[112:115], a[48:51], v[60:63]\n\t"
    "v_exp_f32 v82, v82\n\t"
    "v_mfma_f32_16x16x32_bf16 v[92:95], a[116:119], a[52:55], v[92:95]\n\t"
    "ds_read_b128 a[148:151], v5 offset:53248\n\t"
    "v_add_f32 v42, v42, v82\n\t"
    "v_mfma_f32_16x16x32_bf16 v[92:95], a[120:123], a[56:59], v[92:95]\n\t"
    "v_exp_f32 v83, v83\n\t"
    "v_mfma_f32_16x16x32_bf16 v[92:95], a[124:127], a[60:63], v[92:95]\n\t"
    "v_add_f32 v42, v42, v83\n\t"
    "v_cvt_pk_bf16_f32 v73, v82, v83\n\t"
    "v_mfma_f32_16x16x32_bf16 v[96:99], a[160:163], v[76:79], v[96:99]\n\t"
    "v_exp_f32 v84, v84\n\t"
    "v_mfma_f32_16x16x32_bf16 v[100:103], a[164:167], v[76:79], v[100:103]\n\t"
    "ds_read_b128 a[152:155], v6 offset:53248\n\t"
    "v_add_f32 v42, v42, v84\n\t"
    "v_mfma_f32_16x16x32_bf16 v[104:107], a[168:171], v[76:79], v[104:107]\n\t"
    "v_exp_f32 v85, v85\n\t"
    "v_mfma_f32_16x16x32_bf16 v[108:111], a[172:175], v[76:79], v[108:111]\n\t"
    "v_add_f32 v42, v42, v85\n\t"
    "v_cvt_pk_bf16_f32 v74, v84, v85\n\t"
    "v_mfma_f32_16x16x32_bf16 v[112:115], a[176:179], v[76:79], v[112:115]\n\t"
    "v_exp_f32 v86, v86\n\t"
    "v_mfma_f32_16x16x32_bf16 v[116:119], a[180:183], v[76:79], v[116:119]\n\t"
    "ds_read_b128 a[156:159], v7 offset:53248\n\t"
    "v_add_f32 v42, v42, v86\n\t"
    "v_mfma_f32_16x16x32_bf16 v[120:123], a[184:187], v[76:79], v[120:123]\n\t"
    "v_exp_f32 v87, v87\n\t"
    "v_mfma_f32_16x16x32_bf16 v[124:127], a[188:191], v[76:79], v[124:127]\n\t"
    "v_add_f32 v42, v42, v87\n\t"
    "v_cvt_pk_bf16_f32 v75, v86, v87\n\t"
    "v_mfma_f32_16x16x32_bf16 v[80:83], a[96:99], a[64:67], v[64:67]\n\t"
    "v_exp_f32 v88, v88\n\t"
    "v_mfma_f32_16x16x32_bf16 v[80:83], a[100:103], a[68:71], v[80:83]\n\t"
    "ds_read_b64_tr_b16 a[192:193], v8 offset:57344\n\t"
    "v_add_f32 v43, v43, v88\n\t"
    "v_mfma_f32_16x16x32_bf16 v[80:83], a[104:107], a[72:75], v[80:83]\n\t"
    "v_exp_f32 v89, v89\n\t"
    "v_mfma_f32_16x16x32_bf16 v[80:83], a[108:111], a[76:79], v[80:83]\n\t"
    "v_add_f32 v43, v43, v89\n\t"
    "v_cvt_pk_bf16_f32 v76, v88, v89\n\t"
    "v_mfma_f32_16x16x32_bf16 v[84:87], a[112:115], a[64:67], v[64:67]\n\t"
    "v_exp_f32 v90, v90\n\t"
    "v_mfma_f32_16x16x32_bf16 v[84:87], a[116:119], a[68:71], v[84:87]\n\t"
    "ds_read_b64_tr_b16 a[194:195], v8 offset:61440\n\t"
    "v_add_f32 v43, v43, v90\n\t"
    "v_mfma_f32_16x16x32_bf16 v[84:87], a[120:123], a[72:75], v[84:87]\n\t"
    "v_exp_f32 v91, v91\n\t"
    "v_mfma_f32_16x16x32_bf16 v[84:87], a[124:127], a[76:79], v[84:87]\n\t"
    "v_add_f32 v43, v43, v91\n\t"
    "v_cvt_pk_bf16_f32 v77, v90, v91\n\t"
    "v_mfma_f32_16x16x32_bf16 v[128:131], a[160:163], v[72:75], v[128:131]\n\t"
    "v_exp_f32 v92, v92\n\t"
    "v_mfma_f32_16x16x32_bf16 v[132:135], a[164:167], v[72:75], v[132:135]\n\t"
    "ds_read_b64_tr_b16 a[196:197], v9 offset:57344\n\t"
    "v_add_f32 v43, v43, v92\n\t"
    "v_mfma_f32_16x16x32_bf16 v[136:139], a[168:171], v[72:75], v[136:139]\n\t"
    "v_exp_f32 v93, v93\n\t"
    "v_mfma_f32_16x16x32_bf16 v[140:143], a[172:175], v[72:75], v[140:143]\n\t"
    "v_add_f32 v43, v43, v93\n\t"
    "v_cvt_pk_bf16_f32 v78, v92, v93\n\t"
    "v_mfma_f32_16x16x32_bf16 v[144:147], a[176:179], v[72:75], v[144:147]\n\t"
    "v_exp_f32 v94, v94\n\t"
    "v_mfma_f32_16x16x32_bf16 v[148:151], a[180:183], v[72:75], v[148:151]\n\t"
    "ds_read_b64_tr_b16 a[198:199], v9 offset:61440\n\t"
    "v_add_f32 v43, v43, v94\n\t"
    "v_mfma_f32_16x16x32_bf16 v[152:155], a[184:187], v[72:75], v[152:155]\n\t"
    "v_exp_f32 v95, v95\n\t"
    "v_mfma_f32_16x16x32_bf16 v[156:159], a[188:191], v[72:75], v[156:159]\n\t"
    "v_add_f32 v43, v43, v95\n\t"
    "v_cvt_pk_bf16_f32 v79, v94, v95\n\t"
    "v_mfma_f32_16x16x32_bf16 v[88:91], a[96:99], a[80:83], v[68:71]\n\t"
    "v_exp_f32 v80, v80\n\t"
    "v_mfma_f32_16x16x32_bf16 v[88:91], a[100:103], a[84:87], v[88:91]\n\t"
    "ds_read_b64_tr_b16 a[200:201], v10 offset:57344\n\t"
    "v_add_f32 v44, v44, v80\n\t"
    "v_mfma_f32_16x16x32_bf16 v[88:91], a[104:107], a[88:91], v[88:91]\n\t"
    "v_exp_f32 v81, v81\n\t"
    "v_mfma_f32_16x16x32_bf16 v[88:91], a[108:111], a[92:95], v[88:91]\n\t"
    "v_add_f32 v44, v44, v81\n\t"
    "v_cvt_pk_bf16_f32 v72, v80, v81\n\t"
    "v_mfma_f32_16x16x32_bf16 v[92:95], a[112:115], a[80:83], v[68:71]\n\t"
    "v_exp_f32 v82, v82\n\t"
    "v_mfma_f32_16x16x32_bf16 v[92:95], a[116:119], a[84:87], v[92:95]\n\t"
    "ds_read_b64_tr_b16 a[202:203], v10 offset:61440\n\t"
    "v_add_f32 v44, v44, v82\n\t"
    "v_mfma_f32_16x16x32_bf16 v[92:95], a[120:123], a[88:91], v[92:95]\n\t"
    "v_exp_f32 v83, v83\n\t"
    "v_mfma_f32_16x16x32_bf16 v[92:95], a[124:127], a[92:95], v[92:95]\n\t"
    "v_add_f32 v44, v44, v83\n\t"
    "v_cvt_pk_bf16_f32 v73, v82, v83\n\t"
    "v_mfma_f32_16x16x32_bf16 v[160:163], a[160:163], v[76:79], v[160:163]\n\t"
    "v_exp_f32 v84, v84\n\t"
    "v_mfma_f32_16x16x32_bf16 v[164:167], a[164:167], v[76:79], v[164:167]\n\t"
    "ds_read_b64_tr_b16 a[204:205], v11 offset:57344\n\t"
    "v_add_f32 v44, v44, v84\n\t"
    "v_mfma_f32_16x16x32_bf16 v[168:171], a[168:171], v[76:79], v[168:171]\n\t"
    "v_exp_f32 v85, v85\n\t"
    "v_mfma_f32_16x16x32_bf16 v[172:175], a[172:175], v[76:79], v[172:175]\n\t"
    "v_add_f32 v44, v44, v85\n\t"
    "v_cvt_pk_bf16_f32 v74, v84, v85\n\t"
    "v_mfma_f32_16x16x32_bf16 v[176:179], a[176:179], v[76:79], v[176:179]\n\t"
    "v_exp_f32 v86, v86\n\t"
    "v_mfma_f32_16x16x32_bf16 v[180:183], a[180:183], v[76:79], v[180:183]\n\t"
    "ds_read_b64_tr_b16 a[206:207], v11 offset:61440\n\t"
    "v_add_f32 v44, v44, v86\n\t"
    "v_mfma_f32_16x16x32_bf16 v[184:187], a[184:187], v[76:79], v[184:187]\n\t"
    "v_exp_f32 v87, v87\n\t"
    "v_mfma_f32_16x16x32_bf16 v[188:191], a[188:191], v[76:79], v[188:191]\n\t"
    "s_waitcnt lgkmcnt(8)\n\t"
    "v_add_f32 v44, v44, v87\n\t"
    "v_cvt_pk_bf16_f32 v75, v86, v87\n\t"
    "v_mfma_f32_16x16x32_bf16 v[80:83], a[128:131], a[0:3], v[48:51]\n\t"
    "v_exp_f32 v88, v88\n\t"
    "v_mfma_f32_16x16x32_bf16 v[80:83], a[132:135], a[4:7], v[80:83]\n\t"
    "ds_read_b64_tr_b16 a[208:209], v8 offset:58368\n\t"
    "v_add_f32 v45, v45, v88\n\t"
    "v_mfma_f32_16x16x32_bf16 v[80:83], a[136:139], a[8:11], v[80:83]\n\t"
    "v_exp_f32 v89, v89\n\t"
    "v_mfma_f32_16x16x32_bf16 v[80:83], a[140:143], a[12:15], v[80:83]\n\t"
    "v_add_f32 v45, v45, v89\n\t"
    "v_cvt_pk_bf16_f32 v76, v88, v89\n\t"
    "v_mfma_f32_16x16x32_bf16 v[84:87], a[144:147], a[0:3], v[48:51]\n\t"
    "v_exp_f32 v90, v90\n\t"
    "v_mfma_f32_16x16x32_bf16 v[84:87], a[148:151], a[4:7], v[84:87]\n\t"
    "ds_read_b64_tr_b16 a[210:211], v8 offset:62464\n\t"
    "v_add_f32 v45, v45, v90\n\t"
    "v_mfma_f32_16x16x32_bf16 v[84:87], a[152:155], a[8:11], v[84:87]\n\t"
    "v_exp_f32 v91, v91\n\t"
    "v_mfma_f32_16x16x32_bf16 v[84:87], a[156:159], a[12:15], v[84:87]\n\t"
    "v_add_f32 v45, v45, v91\n\t"
    "v_cvt_pk_bf16_f32 v77, v90, v91\n\t"
    "v_mfma_f32_16x16x32_bf16 v[192:195], a[160:163], v[72:75], v[192:195]\n\t"
    "v_exp_f32 v92, v92\n\t"
    "v_mfma_f32_16x16x32_bf16 v[196:199], a[164:167], v[72:75], v[196:199]\n\t"
    "ds_read_b64_tr_b16 a[212:213], v9 offset:58368\n\t"
    "v_add_f32 v45, v45, v92\n\t"
    "v_mfma_f32_16x16x32_bf16 v[200:203], a[168:171], v[72:75], v[200:203]\n\t"
    "v_exp_f32 v93, v93\n\t"
    "v_mfma_f32_16x16x32_bf16 v[204:207], a[172:175], v[72:75], v[204:207]\n\t"
    "v_add_f32 v45, v45, v93\n\t"
    "v_cvt_pk_bf16_f32 v78, v92, v93\n\t"
    "v_mfma_f32_16x16x32_bf16 v[208:211], a[176:179], v[72:75], v[208:211]\n\t"
    "v_exp_f32 v94, v94\n\t"
    "v_mfma_f32_16x16x32_bf16 v[212:215], a[180:183], v[72:75], v[212:215]\n\t"
    "ds_read_b64_tr_b16 a[214:215], v9 offset:62464\n\t"
    "v_add_f32 v45, v45, v94\n\t"
    "v_mfma_f32_16x16x32_bf16 v[216:219], a[184:187], v[72:75], v[216:219]\n\t"
    "v_exp_f32 v95, v95\n\t"
    "v_mfma_f32_16x16x32_bf16 v[220:223], a[188:191], v[72:75], v[220:223]\n\t"
    "v_add_f32 v45, v45, v95\n\t"
    "v_cvt_pk_bf16_f32 v79, v94, v95\n\t"
    "v_mfma_f32_16x16x32_bf16 v[88:91], a[128:131], a[16:19], v[52:55]\n\t"
    "v_exp_f32 v80, v80\n\t"
    "v_mfma_f32_16x16x32_bf16 v[88:91], a[132:135], a[20:23], v[88:91]\n\t"
    "ds_read_b64_tr_b16 a[216:217], v10 offset:58368\n\t"
    "v_add_f32 v40, v40, v80\n\t"
    "v_xor_b32 v4, 0x10000, v4\n\t"
    "v_mfma_f32_16x16x32_bf16 v[88:91], a[136:139], a[24:27], v[88:91]\n\t"
    "v_exp_f32 v81, v81\n\t"
    "v_mfma_f32_16x16x32_bf16 v[88:91], a[140:143], a[28:31], v[88:91]\n\t"
    "v_add_f32 v40, v40, v81\n\t"
    "v_cvt_pk_bf16_f32 v72, v80, v81\n\t"
    "v_mfma_f32_16x16x32_bf16 v[92:95], a[144:147], a[16:19], v[52:55]\n\t"
    "v_exp_f32 v82, v82\n\t"
    "v_mfma_f32_16x16x32_bf16 v[92:95], a[148:151], a[20:23], v[92:95]\n\t"
    "ds_read_b64_tr_b16 a[218:219], v10 offset:62464\n\t"
    "v_add_f32 v40, v40, v82\n\t"
    "v_xor_b32 v5, 0x10000, v5\n\t"
    "v_mfma_f32_16x16x32_bf16 v[92:95], a[152:155], a[24:27], v[92:95]\n\t"
    "v_exp_f32 v83, v83\n\t"
    "v_mfma_f32_16x16x32_bf16 v[92:95], a[156:159], a[28:31], v[92:95]\n\t"
    "v_add_f32 v40, v40, v83\n\t"
    "v_cvt_pk_bf16_f32 v73, v82, v83\n\t"
    "v_mfma_f32_16x16x32_bf16 v[224:227], a[160:163], v[76:79], v[224:227]\n\t"
    "v_exp_f32 v84, v84\n\t"
    "v_mfma_f32_16x16x32_bf16 v[228:231], a[164:167], v[76:79], v[228:231]\n\t"
    "ds_read_b64_tr_b16 a[220:221], v11 offset:58368\n\t"
    "v_add_f32 v40, v40, v84\n\t"
    "v_xor_b32 v6, 0x10000, v6\n\t"
    "v_mfma_f32_16x16x32_bf16 v[232:235], a[168:171], v[76:79], v[232:235]\n\t"
    "v_exp_f32 v85, v85\n\t"
    "v_mfma_f32_16x16x32_bf16 v[236:239], a[172:175], v[76:79], v[236:239]\n\t"
    "v_add_f32 v40, v40, v85\n\t"
    "v_cvt_pk_bf16_f32 v74, v84, v85\n\t"
    "v_mfma_f32_16x16x32_bf16 v[240:243], a[176:179], v[76:79], v[240:243]\n\t"
    "v_exp_f32 v86, v86\n\t"
    "v_mfma_f32_16x16x32_bf16 v[244:247], a[180:183], v[76:79], v[244:247]\n\t"
    "ds_read_b64_tr_b16 a[222:223], v11 offset:62464\n\t"
    "v_add_f32 v40, v40, v86\n\t"
    "v_xor_b32 v7, 0x10000, v7\n\t"
    "v_mfma_f32_16x16x32_bf16 v[248:251], a[184:187], v[76:79], v[248:251]\n\t"
    "v_exp_f32 v87, v87\n\t"
    "v_mfma_f32_16x16x32_bf16 v[252:255], a[188:191], v[76:79], v[252:255]\n\t"
    "s_barrier\n\t"
    "v_add_f32 v40, v40, v87\n\t"
    "v_cvt_pk_bf16_f32 v75, v86, v87\n\t"
    "v_mfma_f32_16x16x32_bf16 v[80:83], a[128:131], a[32:35], v[56:59]\n\t"
    "v_exp_f32 v88, v88\n\t"
    "v_mfma_f32_16x16x32_bf16 v[80:83], a[132:135], a[36:39], v[80:83]\n\t"
    "ds_read_b128 a[96:99], v4 offset:0\n\t"
    "v_add_f32 v41, v41, v88\n\t"
    "v_mfma_f32_16x16x32_bf16 v[80:83], a[136:139], a[40:43], v[80:83]\n\t"
    "v_exp_f32 v89, v89\n\t"
    "v_mfma_f32_16x16x32_bf16 v[80:83], a[140:143], a[44:47], v[80:83]\n\t"
    "v_add_f32 v41, v41, v89\n\t"
    "v_cvt_pk_bf16_f32 v76, v88, v89\n\t"
    "v_mfma_f32_16x16x32_bf16 v[84:87], a[144:147], a[32:35], v[56:59]\n\t"
    "v_exp_f32 v90, v90\n\t"
    "v_mfma_f32_16x16x32_bf16 v[84:87], a[148:151], a[36:39], v[84:87]\n\t"
    "ds_read_b128 a[100:103], v5 offset:0\n\t"
    "v_add_f32 v41, v41, v90\n\t"
    "v_mfma_f32_16x16x32_bf16 v[84:87], a[152:155], a[40:43], v[84:87]\n\t"
    "v_exp_f32 v91, v91\n\t"
    "v_mfma_f32_16x16x32_bf16 v[84:87], a[156:159], a[44:47], v[84:87]\n\t"
    "s_waitcnt lgkmcnt(2)\n\t"
    "v_add_f32 v41, v41, v91\n\t"
    "v_cvt_pk_bf16_f32 v77, v90, v91\n\t"
    "v_mfma_f32_16x16x32_bf16 a[224:227], a[192:195], v[72:75], a[224:227]\n\t"
    "v_exp_f32 v92, v92\n\t"
    "v_mfma_f32_16x16x32_bf16 a[228:231], a[196:199], v[72:75], a[228:231]\n\t"
    "ds_read_b128 a[104:107], v6 offset:0\n\t"
    "v_add_f32 v41, v41, v92\n\t"
    "v_mfma_f32_16x16x32_bf16 a[232:235], a[200:203], v[72:75], a[232:235]\n\t"
    "v_exp_f32 v93, v93\n\t"
    "v_mfma_f32_16x16x32_bf16 a[236:239], a[204:207], v[72:75], a[236:239]\n\t"
    "v_add_f32 v41, v41, v93\n\t"
    "v_cvt_pk_bf16_f32 v78, v92, v93\n\t"
    "v_mfma_f32_16x16x32_bf16 a[240:243], a[208:211], v[72:75], a[240:243]\n\t"
    "v_exp_f32 v94, v94\n\t"
    "v_mfma_f32_16x16x32_bf16 a[244:247], a[212:215], v[72:75], a[244:247]\n\t"
    "ds_read_b128 a[108:111], v7 offset:0\n\t"
    "v_add_f32 v41, v41, v94\n\t"
    "v_mfma_f32_16x16x32_bf16 a[248:251], a[216:219], v[72:75], a[248:251]\n\t"
    "v_exp_f32 v95, v95\n\t"
    "v_mfma_f32_16x16x32_bf16 a[252:255], a[220:223], v[72:75], a[252:255]\n\t"
    "v_add_f32 v41, v41, v95\n\t"
    "v_cvt_pk_bf16_f32 v79, v94, v95\n\t"
    "v_mfma_f32_16x16x32_bf16 v[88:91], a[128:131], a[48:51], v[60:63]\n\t"
    "v_exp_f32 v80, v80\n\t"
    "v_mfma_f32_16x16x32_bf16 v[88:91], a[132:135], a[52:55], v[88:91]\n\t"
    "ds_read_b128 a[112:115], v4 offset:4096\n\t"
    "v_add_f32 v42, v42, v80\n\t"
    "v_xor_b32 v8, 0x10000, v8\n\t"
    "v_mfma_f32_16x16x32_bf16 v[88:91], a[136:139], a[56:59], v[88:91]\n\t"
    "v_exp_f32 v81, v81\n\t"
    "v_mfma_f32_16x16x32_bf16 v[88:91], a[140:143], a[60:63], v[88:91]\n\t"
    "v_add_f32 v42, v42, v81\n\t"
    "v_cvt_pk_bf16_f32 v72, v80, v81\n\t"
    "v_mfma_f32_16x16x32_bf16 v[92:95], a[144:147], a[48:51], v[60:63]\n\t"
    "v_exp_f32 v82, v82\n\t"
    "v_mfma_f32_16x16x32_bf16 v[92:95], a[148:151], a[52:55], v[92:95]\n\t"
    "ds_read_b128 a[116:119], v5 offset:4096\n\t"
    "v_add_f32 v42, v42, v82\n\t"
    "v_xor_b32 v9, 0x10000, v9\n\t"
    "v_mfma_f32_16x16x32_bf16 v[92:95], a[152:155], a[56:59], v[92:95]\n\t"
    "v_exp_f32 v83, v83\n\t"
    "v_mfma_f32_16x16x32_bf16 v[92:95], a[156:159], a[60:63], v[92:95]\n\t"
    "v_add_f32 v42, v42, v83\n\t"
    "v_cvt_pk_bf16_f32 v73, v82, v83\n\t"
    "v_mfma_f32_16x16x32_bf16 v[96:99], a[192:195], v[76:79], v[96:99]\n\t"
    "v_exp_f32 v84, v84\n\t"
    "v_mfma_f32_16x16x32_bf16 v[100:103], a[196:199], v[76:79], v[100:103]\n\t"
    "ds_read_b128 a[120:123], v6 offset:4096\n\t"
    "v_add_f32 v42, v42, v84\n\t"
    "v_xor_b32 v10, 0x10000, v10\n\t"
    "v_mfma_f32_16x16x32_bf16 v[104:107], a[200:203], v[76:79], v[104:107]\n\t"
    "v_exp_f32 v85, v85\n\t"
    "v_mfma_f32_16x16x32_bf16 v[108:111], a[204:207], v[76:79], v[108:111]\n\t"
    "v_add_f32 v42, v42, v85\n\t"
    "v_cvt_pk_bf16_f32 v74, v84, v85\n\t"
    "v_mfma_f32_16x16x32_bf16 v[112:115], a[208:211], v[76:79], v[112:115]\n\t"
    "v_exp_f32 v86, v86\n\t"
    "v_mfma_f32_16x16x32_bf16 v[116:119], a[212:215], v[76:79], v[116:119]\n\t"
    "ds_read_b128 a[124:127], v7 offset:4096\n\t"
    "v_add_f32 v42, v42, v86\n\t"
    "v_xor_b32 v11, 0x10000, v11\n\t"
    "v_mfma_f32_16x16x32_bf16 v[120:123], a[216:219], v[76:79], v[120:123]\n\t"
    "v_exp_f32 v87, v87\n\t"
    "v_mfma_f32_16x16x32_bf16 v[124:127], a[220:223], v[76:79], v[124:127]\n\t"
    "v_add_f32 v42, v42, v87\n\t"
    "v_cvt_pk_bf16_f32 v75, v86, v87\n\t"
    "v_mfma_f32_16x16x32_bf16 v[80:83], a[128:131], a[64:67], v[64:67]\n\t"
    "v_exp_f32 v88, v88\n\t"
    "v_mfma_f32_16x16x32_bf16 v[80:83], a[132:135], a[68:71], v[80:83]\n\t"
    "ds_read_b64_tr_b16 a[160:161], v8 offset:8192\n\t"
    "v_add_f32 v43, v43, v88\n\t"
    "v_mfma_f32_16x16x32_bf16 v[80:83], a[136:139], a[72:75], v[80:83]\n\t"
    "v_exp_f32 v89, v89\n\t"
    "v_mfma_f32_16x16x32_bf16 v[80:83], a[140:143], a[76:79], v[80:83]\n\t"
    "v_add_f32 v43, v43, v89\n\t"
    "v_cvt_pk_bf16_f32 v76, v88, v89\n\t"
    "v_mfma_f32_16x16x32_bf16 v[84:87], a[144:147], a[64:67], v[64:67]\n\t"
    "v_exp_f32 v90, v90\n\t"
    "v_mfma_f32_16x16x32_bf16 v[84:87], a[148:151], a[68:71], v[84:87]\n\t"
    "ds_read_b64_tr_b16 a[162:163], v8 offset:12288\n\t"
    "v_add_f32 v43, v43, v90\n\t"
    "v_mfma_f32_16x16x32_bf16 v[84:87], a[152:155], a[72:75], v[84:87]\n\t"
    "v_exp_f32 v91, v91\n\t"
    "v_mfma_f32_16x16x32_bf16 v[84:87], a[156:159], a[76:79], v[84:87]\n\t"
    "v_add_f32 v43, v43, v91\n\t"
    "v_cvt_pk_bf16_f32 v77, v90, v91\n\t"
    "v_mfma_f32_16x16x32_bf16 v[128:131], a[192:195], v[72:75], v[128:131]\n\t"
    "v_exp_f32 v92, v92\n\t"
    "v_mfma_f32_16x16x32_bf16 v[132:135], a[196:199], v[72:75], v[132:135]\n\t"
    "ds_read_b64_tr_b16 a[164:165], v9 offset:8192\n\t"
    "v_add_f32 v43, v43, v92\n\t"
    "v_mfma_f32_16x16x32_bf16 v[136:139], a[200:203], v[72:75], v[136:139]\n\t"
    "v_exp_f32 v93, v93\n\t"
    "v_mfma_f32_16x16x32_bf16 v[140:143], a[204:207], v[72:75], v[140:143]\n\t"
    "v_add_f32 v43, v43, v93\n\t"
    "v_cvt_pk_bf16_f32 v78, v92, v93\n\t"
    "v_mfma_f32_16x16x32_bf16 v[144:147], a[208:211], v[72:75], v[144:147]\n\t"
    "v_exp_f32 v94, v94\n\t"
    "v_mfma_f32_16x16x32_bf16 v[148:151], a[212:215], v[72:75], v[148:151]\n\t"
    "ds_read_b64_tr_b16 a[166:167], v9 offset:12288\n\t"
    "v_add_f32 v43, v43, v94\n\t"
    "v_mfma_f32_16x16x32_bf16 v[152:155], a[216:219], v[72:75], v[152:155]\n\t"
    "v_exp_f32 v95, v95\n\t"
    "v_mfma_f32_16x16x32_bf16 v[156:159], a[220:223], v[72:75], v[156:159]\n\t"
    "v_add_f32 v43, v43, v95\n\t"
    "v_cvt_pk_bf16_f32 v79, v94, v95\n\t"
    "v_mfma_f32_16x16x32_bf16 v[88:91], a[128:131], a[80:83], v[68:71]\n\t"
    "v_exp_f32 v80, v80\n\t"
    "v_mfma_f32_16x16x32_bf16 v[88:91], a[132:135], a[84:87], v[88:91]\n\t"
    "ds_read_b64_tr_b16 a[168:169], v10 offset:8192\n\t"
    "v_add_f32 v44, v44, v80\n\t"
    "v_mfma_f32_16x16x32_bf16 v[88:91], a[136:139], a[88:91], v[88:91]\n\t"
    "v_exp_f32 v81, v81\n\t"
    "v_mfma_f32_16x16x32_bf16 v[88:91], a[140:143], a[92:95], v[88:91]\n\t"
    "v_add_f32 v44, v44, v81\n\t"
    "v_cvt_pk_bf16_f32 v72, v80, v81\n\t"
    "v_mfma_f32_16x16x32_bf16 v[92:95], a[144:147], a[80:83], v[68:71]\n\t"
    "v_exp_f32 v82, v82\n\t"
    "v_mfma_f32_16x16x32_bf16 v[92:95], a[148:151], a[84:87], v[92:95]\n\t"
    "ds_read_b64_tr_b16 a[170:171], v10 offset:12288\n\t"
    "v_add_f32 v44, v44, v82\n\t"
    "v_mfma_f32_16x16x32_bf16 v[92:95], a[152:155], a[88:91], v[92:95]\n\t"
    "v_exp_f32 v83, v83\n\t"
    "v_mfma_f32_16x16x32_bf16 v[92:95], a[156:159], a[92:95], v[92:95]\n\t"
    "v_add_f32 v44, v44, v83\n\t"
    "v_cvt_pk_bf16_f32 v73, v82, v83\n\t"
    "v_mfma_f32_16x16x32_bf16 v[160:163], a[192:195], v[76:79], v[160:163]\n\t"
    "v_exp_f32 v84, v84\n\t"
    "v_mfma_f32_16x16x32_bf16 v[164:167], a[196:199], v[76:79], v[164:167]\n\t"
    "ds_read_b64_tr_b16 a[172:173], v11 offset:8192\n\t"
    "v_add_f32 v44, v44, v84\n\t"
    "v_mfma_f32_16x16x32_bf16 v[168:171], a[200:203], v[76:79], v[168:171]\n\t"
    "v_exp_f32 v85, v85\n\t"
    "v_mfma_f32_16x16x32_bf16 v[172:175], a[204:207], v[76:79], v[172:175]\n\t"
    "v_add_f32 v44, v44, v85\n\t"
    "v_cvt_pk_bf16_f32 v74, v84, v85\n\t"
    "v_mfma_f32_16x16x32_bf16 v[176:179], a[208:211], v[76:79], v[176:179]\n\t"
    "v_exp_f32 v86, v86\n\t"
    "v_mfma_f32_16x16x32_bf16 v[180:183], a[212:215], v[76:79], v[180:183]\n\t"
    "ds_read_b64_tr_b16 a[174:175], v11 offset:12288\n\t"
    "v_add_f32 v44, v44, v86\n\t"
    "v_mfma_f32_16x16x32_bf16 v[184:187], a[216:219], v[76:79], v[184:187]\n\t"
    "v_exp_f32 v87, v87\n\t"
    "v_mfma_f32_16x16x32_bf16 v[188:191], a[220:223], v[76:79], v[188:191]\n\t"
    "s_waitcnt lgkmcnt(8)\n\t"
    "v_add_f32 v44, v44, v87\n\t"
    "v_cvt_pk_bf16_f32 v75, v86, v87\n\t"
    "v_mfma_f32_16x16x32_bf16 v[80:83], a[96:99], a[0:3], v[48:51]\n\t"
    "v_exp_f32 v88, v88\n\t"
    "v_mfma_f32_16x16x32_bf16 v[80:83], a[100:103], a[4:7], v[80:83]\n\t"
    "ds_read_b64_tr_b16 a[176:177], v8 offset:9216\n\t"
    "v_add_f32 v45, v45, v88\n\t"
    "v_mfma_f32_16x16x32_bf16 v[80:83], a[104:107], a[8:11], v[80:83]\n\t"
    "v_exp_f32 v89, v89\n\t"
    "v_mfma_f32_16x16x32_bf16 v[80:83], a[108:111], a[12:15], v[80:83]\n\t"
    "v_add_f32 v45, v45, v89\n\t"
    "v_cvt_pk_bf16_f32 v76, v88, v89\n\t"
    "v_mfma_f32_16x16x32_bf16 v[84:87], a[112:115], a[0:3], v[48:51]\n\t"
    "v_exp_f32 v90, v90\n\t"
    "v_mfma_f32_16x16x32_bf16 v[84:87], a[116:119], a[4:7], v[84:87]\n\t"
    "ds_read_b64_tr_b16 a[178:179], v8 offset:13312\n\t"
    "v_add_f32 v45, v45, v90\n\t"
    "v_mfma_f32_16x16x32_bf16 v[84:87], a[120:123], a[8:11], v[84:87]\n\t"
    "v_exp_f32 v91, v91\n\t"
    "v_mfma_f32_16x16x32_bf16 v[84:87], a[124:127], a[12:15], v[84:87]\n\t"
    "v_add_f32 v45, v45, v91\n\t"
    "v_cvt_pk_bf16_f32 v77, v90, v91\n\t"
    "v_mfma_f32_16x16x32_bf16 v[192:195], a[192:195], v[72:75], v[192:195]\n\t"
    "v_exp_f32 v92, v92\n\t"
    "v_mfma_f32_16x16x32_bf16 v[196:199], a[196:199], v[72:75], v[196:199]\n\t"
    "ds_read_b64_tr_b16 a[180:181], v9 offset:9216\n\t"
    "v_add_f32 v45, v45, v92\n\t"
    "v_mfma_f32_16x16x32_bf16 v[200:203], a[200:203], v[72:75], v[200:203]\n\t"
    "v_exp_f32 v93, v93\n\t"
    "v_mfma_f32_16x16x32_bf16 v[204:207], a[204:207], v[72:75], v[204:207]\n\t"
    "v_add_f32 v45, v45, v93\n\t"
    "v_cvt_pk_bf16_f32 v78, v92, v93\n\t"
    "v_mfma_f32_16x16x32_bf16 v[208:211], a[208:211], v[72:75], v[208:211]\n\t"
    "v_exp_f32 v94, v94\n\t"
    "v_mfma_f32_16x16x32_bf16 v[212:215], a[212:215], v[72:75], v[212:215]\n\t"
    "ds_read_b64_tr_b16 a[182:183], v9 offset:13312\n\t"
    "v_add_f32 v45, v45, v94\n\t"
    "v_mfma_f32_16x16x32_bf16 v[216:219], a[216:219], v[72:75], v[216:219]\n\t"
    "v_exp_f32 v95, v95\n\t"
    "v_mfma_f32_16x16x32_bf16 v[220:223], a[220:223], v[72:75], v[220:223]\n\t"
    "v_add_f32 v45, v45, v95\n\t"
    "v_cvt_pk_bf16_f32 v79, v94, v95\n\t"
    "v_mfma_f32_16x16x32_bf16 v[88:91], a[96:99], a[16:19], v[52:55]\n\t"
    "v_exp_f32 v80, v80\n\t"
    "v_mfma_f32_16x16x32_bf16 v[88:91], a[100:103], a[20:23], v[88:91]\n\t"
    "ds_read_b64_tr_b16 a[184:185], v10 offset:9216\n\t"
    "v_add_f32 v40, v40, v80\n\t"
    "v_mfma_f32_16x16x32_bf16 v[88:91], a[104:107], a[24:27], v[88:91]\n\t"
    "v_exp_f32 v81, v81\n\t"
    "v_mfma_f32_16x16x32_bf16 v[88:91], a[108:111], a[28:31], v[88:91]\n\t"
    "v_add_f32 v40, v40, v81\n\t"
    "v_cvt_pk_bf16_f32 v72, v80, v81\n\t"
    "v_mfma_f32_16x16x32_bf16 v[92:95], a[112:115], a[16:19], v[52:55]\n\t"
    "v_exp_f32 v82, v82\n\t"
    "v_mfma_f32_16x16x32_bf16 v[92:95], a[116:119], a[20:23], v[92:95]\n\t"
    "ds_read_b64_tr_b16 a[186:187], v10 offset:13312\n\t"
    "v_add_f32 v40, v40, v82\n\t"
    "v_mfma_f32_16x16x32_bf16 v[92:95], a[120:123], a[24:27], v[92:95]\n\t"
    "v_exp_f32 v83, v83\n\t"
    "v_mfma_f32_16x16x32_bf16 v[92:95], a[124:127], a[28:31], v[92:95]\n\t"
    "v_add_f32 v40, v40, v83\n\t"
    "v_cvt_pk_bf16_f32 v73, v82, v83\n\t"
    "v_mfma_f32_16x16x32_bf16 v[224:227], a[192:195], v[76:79], v[224:227]\n\t"
    "v_exp_f32 v84, v84\n\t"
    "v_mfma_f32_16x16x32_bf16 v[228:231], a[196:199], v[76:79], v[228:231]\n\t"
    "ds_read_b64_tr_b16 a[188:189], v11 offset:9216\n\t"
    "v_add_f32 v40, v40, v84\n\t"
    "v_mfma_f32_16x16x32_bf16 v[232:235], a[200:203], v[76:79], v[232:235]\n\t"
    "v_exp_f32 v85, v85\n\t"
    "v_mfma_f32_16x16x32_bf16 v[236:239], a[204:207], v[76:79], v[236:239]\n\t"
    "v_add_f32 v40, v40, v85\n\t"
    "v_cvt_pk_bf16_f32 v74, v84, v85\n\t"
    "v_mfma_f32_16x16x32_bf16 v[240:243], a[208:211], v[76:79], v[240:243]\n\t"
    "v_exp_f32 v86, v86\n\t"
    "v_mfma_f32_16x16x32_bf16 v[244:247], a[212:215], v[76:79], v[244:247]\n\t"
    "ds_read_b64_tr_b16 a[190:191], v11 offset:13312\n\t"
    "v_add_f32 v40, v40, v86\n\t"
    "v_mfma_f32_16x16x32_bf16 v[248:251], a[216:219], v[76:79], v[248:251]\n\t"
    "v_exp_f32 v87, v87\n\t"
    "v_mfma_f32_16x16x32_bf16 v[252:255], a[220:223], v[76:79], v[252:255]\n\t"
    "s_barrier\n\t"
    "v_add_f32 v40, v40, v87\n\t"
    "v_cvt_pk_bf16_f32 v75, v86, v87\n\t"
    "v_mfma_f32_16x16x32_bf16 v[80:83], a[96:99], a[32:35], v[56:59]\n\t"
    "v_exp_f32 v88, v88\n\t"
    "v_mfma_f32_16x16x32_bf16 v[80:83], a[100:103], a[36:39], v[80:83]\n\t"
    "ds_read_b128 a[128:131], v4 offset:16384\n\t"
    "v_add_f32 v41, v41, v88\n\t"
    "v_mfma_f32_16x16x32_bf16 v[80:83], a[104:107], a[40:43], v[80:83]\n\t"
    "v_exp_f32 v89, v89\n\t"
    "v_mfma_f32_16x16x32_bf16 v[80:83], a[108:111], a[44:47], v[80:83]\n\t"
    "v_add_f32 v41, v41, v89\n\t"
    "v_cvt_pk_bf16_f32 v76, v88, v89\n\t"
    "v_mfma_f32_16x16x32_bf16 v[84:87], a[112:115], a[32:35], v[56:59]\n\t"
    "v_exp_f32 v90, v90\n\t"
    "v_mfma_f32_16x16x32_bf16 v[84:87], a[116:119], a[36:39], v[84:87]\n\t"
    "ds_read_b128 a[132:135], v5 offset:16384\n\t"
    "v_add_f32 v41, v41, v90\n\t"
    "v_mfma_f32_16x16x32_bf16 v[84:87], a[120:123], a[40:43], v[84:87]\n\t"
    "v_exp_f32 v91, v91\n\t"
    "v_mfma_f32_16x16x32_bf16 v[84:87], a[124:127], a[44:47], v[84:87]\n\t"
    "s_waitcnt lgkmcnt(2)\n\t"
    "v_add_f32 v41, v41, v91\n\t"
    "v_cvt_pk_bf16_f32 v77, v90, v91\n\t"
    "v_mfma_f32_16x16x32_bf16 a[224:227], a[160:163], v[72:75], a[224:227]\n\t"
    "v_exp_f32 v92, v92\n\t"
    "v_mfma_f32_16x16x32_bf16 a[228:231], a[164:167], v[72:75], a[228:231]\n\t"
    "ds_read_b128 a[136:139], v6 offset:16384\n\t"
    "v_add_f32 v41, v41, v92\n\t"
    "v_mfma_f32_16x16x32_bf16 a[232:235], a[168:171], v[72:75], a[232:235]\n\t"
    "v_exp_f32 v93, v93\n\t"
    "v_mfma_f32_16x16x32_bf16 a[236:239], a[172:175], v[72:75], a[236:239]\n\t"
    "v_add_f32 v41, v41, v93\n\t"
    "v_cvt_pk_bf16_f32 v78, v92, v93\n\t"
    "v_mfma_f32_16x16x32_bf16 a[240:243], a[176:179], v[72:75], a[240:243]\n\t"
    "v_exp_f32 v94, v94\n\t"
    "v_mfma_f32_16x16x32_bf16 a[244:247], a[180:183], v[72:75], a[244:247]\n\t"
    "ds_read_b128 a[140:143], v7 offset:16384\n\t"
    "v_add_f32 v41, v41, v94\n\t"
    "v_mfma_f32_16x16x32_bf16 a[248:251], a[184:187], v[72:75], a[248:251]\n\t"
    "v_exp_f32 v95, v95\n\t"
    "v_mfma_f32_16x16x32_bf16 a[252:255], a[188:191], v[72:75], a[252:255]\n\t"
    "v_add_f32 v41, v41, v95\n\t"
    "v_cvt_pk_bf16_f32 v79, v94, v95\n\t"
    "v_mfma_f32_16x16x32_bf16 v[88:91], a[96:99], a[48:51], v[60:63]\n\t"
    "v_exp_f32 v80, v80\n\t"
    "v_mfma_f32_16x16x32_bf16 v[88:91], a[100:103], a[52:55], v[88:91]\n\t"
    "ds_read_b128 a[144:147], v4 offset:20480\n\t"
    "v_add_f32 v42, v42, v80\n\t"
    "v_mfma_f32_16x16x32_bf16 v[88:91], a[104:107], a[56:59], v[88:91]\n\t"
    "v_exp_f32 v81, v81\n\t"
    "v_mfma_f32_16x16x32_bf16 v[88:91], a[108:111], a[60:63], v[88:91]\n\t"
    "v_add_f32 v42, v42, v81\n\t"
    "v_cvt_pk_bf16_f32 v72, v80, v81\n\t"
    "v_mfma_f32_16x16x32_bf16 v[92:95], a[112:115], a[48:51], v[60:63]\n\t"
    "v_exp_f32 v82, v82\n\t"
    "v_mfma_f32_16x16x32_bf16 v[92:95], a[116:119], a[52:55], v[92:95]\n\t"
    "ds_read_b128 a[148:151], v5 offset:20480\n\t"
    "v_add_f32 v42, v42, v82\n\t"
    "v_mfma_f32_16x16x32_bf16 v[92:95], a[120:123], a[56:59], v[92:95]\n\t"
    "v_exp_f32 v83, v83\n\t"
    "v_mfma_f32_16x16x32_bf16 v[92:95], a[124:127], a[60:63], v[92:95]\n\t"
    "v_add_f32 v42, v42, v83\n\t"
    "v_cvt_pk_bf16_f32 v73, v82, v83\n\t"
    "v_mfma_f32_16x16x32_bf16 v[96:99], a[160:163], v[76:79], v[96:99]\n\t"
    "v_exp_f32 v84, v84\n\t"
    "v_mfma_f32_16x16x32_bf16 v[100:103], a[164:167], v[76:79], v[100:103]\n\t"
    "ds_read_b128 a[152:155], v6 offset:20480\n\t"
    "v_add_f32 v42, v42, v84\n\t"
    "v_mfma_f32_16x16x32_bf16 v[104:107], a[168:171], v[76:79], v[104:107]\n\t"
    "v_exp_f32 v85, v85\n\t"
    "v_mfma_f32_16x16x32_bf16 v[108:111], a[172:175], v[76:79], v[108:111]\n\t"
    "v_add_f32 v42, v42, v85\n\t"
    "v_cvt_pk_bf16_f32 v74, v84, v85\n\t"
    "v_mfma_f32_16x16x32_bf16 v[112:115], a[176:179], v[76:79], v[112:115]\n\t"
    "v_exp_f32 v86, v86\n\t"
    "v_mfma_f32_16x16x32_bf16 v[116:119], a[180:183], v[76:79], v[116:119]\n\t"
    "ds_read_b128 a[156:159], v7 offset:20480\n\t"
    "v_add_f32 v42, v42, v86\n\t"
    "v_mfma_f32_16x16x32_bf16 v[120:123], a[184:187], v[76:79], v[120:123]\n\t"
    "v_exp_f32 v87, v87\n\t"
    "v_mfma_f32_16x16x32_bf16 v[124:127], a[188:191], v[76:79], v[124:127]\n\t"
    "v_add_f32 v42, v42, v87\n\t"
    "v_cvt_pk_bf16_f32 v75, v86, v87\n\t"
    "v_mfma_f32_16x16x32_bf16 v[80:83], a[96:99], a[64:67], v[64:67]\n\t"
    "v_exp_f32 v88, v88\n\t"
    "v_mfma_f32_16x16x32_bf16 v[80:83], a[100:103], a[68:71], v[80:83]\n\t"
    "ds_read_b64_tr_b16 a[192:193], v8 offset:24576\n\t"
    "v_add_f32 v43, v43, v88\n\t"
    "v_mfma_f32_16x16x32_bf16 v[80:83], a[104:107], a[72:75], v[80:83]\n\t"
    "v_exp_f32 v89, v89\n\t"
    "v_mfma_f32_16x16x32_bf16 v[80:83], a[108:111], a[76:79], v[80:83]\n\t"
    "v_add_f32 v43, v43, v89\n\t"
    "v_cvt_pk_bf16_f32 v76, v88, v89\n\t"
    "v_mfma_f32_16x16x32_bf16 v[84:87], a[112:115], a[64:67], v[64:67]\n\t"
    "v_exp_f32 v90, v90\n\t"
    "v_mfma_f32_16x16x32_bf16 v[84:87], a[116:119], a[68:71], v[84:87]\n\t"
    "ds_read_b64_tr_b16 a[194:195], v8 offset:28672\n\t"
    "v_add_f32 v43, v43, v90\n\t"
    "v_mfma_f32_16x16x32_bf16 v[84:87], a[120:123], a[72:75], v[84:87]\n\t"
    "v_exp_f32 v91, v91\n\t"
    "v_mfma_f32_16x16x32_bf16 v[84:87], a[124:127], a[76:79], v[84:87]\n\t"
    "v_add_f32 v43, v43, v91\n\t"
    "v_cvt_pk_bf16_f32 v77, v90, v91\n\t"
    "v_mfma_f32_16x16x32_bf16 v[128:131], a[160:163], v[72:75], v[128:131]\n\t"
    "v_exp_f32 v92, v92\n\t"
    "v_mfma_f32_16x16x32_bf16 v[132:135], a[164:167], v[72:75], v[132:135]\n\t"
    "ds_read_b64_tr_b16 a[196:197], v9 offset:24576\n\t"
    "v_add_f32 v43, v43, v92\n\t"
    "v_mfma_f32_16x16x32_bf16 v[136:139], a[168:171], v[72:75], v[136:139]\n\t"
    "v_exp_f32 v93, v93\n\t"
    "v_mfma_f32_16x16x32_bf16 v[140:143], a[172:175], v[72:75], v[140:143]\n\t"
    "v_add_f32 v43, v43, v93\n\t"
    "v_cvt_pk_bf16_f32 v78, v92, v93\n\t"
    "v_mfma_f32_16x16x32_bf16 v[144:147], a[176:179], v[72:75], v[144:147]\n\t"
    "v_exp_f32 v94, v94\n\t"
    "v_mfma_f32_16x16x32_bf16 v[148:151], a[180:183], v[72:75], v[148:151]\n\t"
    "ds_read_b64_tr_b16 a[198:199], v9 offset:28672\n\t"
    "v_add_f32 v43, v43, v94\n\t"
    "v_mfma_f32_16x16x32_bf16 v[152:155], a[184:187], v[72:75], v[152:155]\n\t"
    "v_exp_f32 v95, v95\n\t"
    "v_mfma_f32_16x16x32_bf16 v[156:159], a[188:191], v[72:75], v[156:159]\n\t"
    "v_add_f32 v43, v43, v95\n\t"
    "v_cvt_pk_bf16_f32 v79, v94, v95\n\t"
    "v_mfma_f32_16x16x32_bf16 v[88:91], a[96:99], a[80:83], v[68:71]\n\t"
    "v_exp_f32 v80, v80\n\t"
    "v_mfma_f32_16x16x32_bf16 v[88:91], a[100:103], a[84:87], v[88:91]\n\t"
    "ds_read_b64_tr_b16 a[200:201], v10 offset:24576\n\t"
    "v_add_f32 v44, v44, v80\n\t"
    "v_mfma_f32_16x16x32_bf16 v[88:91], a[104:107], a[88:91], v[88:91]\n\t"
    "v_exp_f32 v81, v81\n\t"
    "v_mfma_f32_16x16x32_bf16 v[88:91], a[108:111], a[92:95], v[88:91]\n\t"
    "v_add_f32 v44, v44, v81\n\t"
    "v_cvt_pk_bf16_f32 v72, v80, v81\n\t"
    "v_mfma_f32_16x16x32_bf16 v[92:95], a[112:115], a[80:83], v[68:71]\n\t"
    "v_exp_f32 v82, v82\n\t"
    "v_mfma_f32_16x16x32_bf16 v[92:95], a[116:119], a[84:87], v[92:95]\n\t"
    "ds_read_b64_tr_b16 a[202:203], v10 offset:28672\n\t"
    "v_add_f32 v44, v44, v82\n\t"
    "v_mfma_f32_16x16x32_bf16 v[92:95], a[120:123], a[88:91], v[92:95]\n\t"
    "v_exp_f32 v83, v83\n\t"
    "v_mfma_f32_16x16x32_bf16 v[92:95], a[124:127], a[92:95], v[92:95]\n\t"
    "v_add_f32 v44, v44, v83\n\t"
    "v_cvt_pk_bf16_f32 v73, v82, v83\n\t"
    "v_mfma_f32_16x16x32_bf16 v[160:163], a[160:163], v[76:79], v[160:163]\n\t"
    "v_exp_f32 v84, v84\n\t"
    "v_mfma_f32_16x16x32_bf16 v[164:167], a[164:167], v[76:79], v[164:167]\n\t"
    "ds_read_b64_tr_b16 a[204:205], v11 offset:24576\n\t"
    "v_add_f32 v44, v44, v84\n\t"
    "v_mfma_f32_16x16x32_bf16 v[168:171], a[168:171], v[76:79], v[168:171]\n\t"
    "v_exp_f32 v85, v85\n\t"
    "v_mfma_f32_16x16x32_bf16 v[172:175], a[172:175], v[76:79], v[172:175]\n\t"
    "v_add_f32 v44, v44, v85\n\t"
    "v_cvt_pk_bf16_f32 v74, v84, v85\n\t"
    "v_mfma_f32_16x16x32_bf16 v[176:179], a[176:179], v[76:79], v[176:179]\n\t"
    "v_exp_f32 v86, v86\n\t"
    "v_mfma_f32_16x16x32_bf16 v[180:183], a[180:183], v[76:79], v[180:183]\n\t"
    "ds_read_b64_tr_b16 a[206:207], v11 offset:28672\n\t"
    "v_add_f32 v44, v44, v86\n\t"
    "v_mfma_f32_16x16x32_bf16 v[184:187], a[184:187], v[76:79], v[184:187]\n\t"
    "v_exp_f32 v87, v87\n\t"
    "v_mfma_f32_16x16x32_bf16 v[188:191], a[188:191], v[76:79], v[188:191]\n\t"
    "s_waitcnt lgkmcnt(8)\n\t"
    "v_add_f32 v44, v44, v87\n\t"
    "v_cvt_pk_bf16_f32 v75, v86, v87\n\t"
    "v_mfma_f32_16x16x32_bf16 v[80:83], a[128:131], a[0:3], v[48:51]\n\t"
    "v_exp_f32 v88, v88\n\t"
    "v_mfma_f32_16x16x32_bf16 v[80:83], a[132:135], a[4:7], v[80:83]\n\t"
    "ds_read_b64_tr_b16 a[208:209], v8 offset:25600\n\t"
    "v_add_f32 v45, v45, v88\n\t"
    "v_mfma_f32_16x16x32_bf16 v[80:83], a[136:139], a[8:11], v[80:83]\n\t"
    "v_exp_f32 v89, v89\n\t"
    "v_mfma_f32_16x16x32_bf16 v[80:83], a[140:143], a[12:15], v[80:83]\n\t"
    "v_add_f32 v45, v45, v89\n\t"
    "v_cvt_pk_bf16_f32 v76, v88, v89\n\t"
    "v_mfma_f32_16x16x32_bf16 v[84:87], a[144:147], a[0:3], v[48:51]\n\t"
    "v_exp_f32 v90, v90\n\t"
    "v_mfma_f32_16x16x32_bf16 v[84:87], a[148:151], a[4:7], v[84:87]\n\t"
    "ds_read_b64_tr_b16 a[210:211], v8 offset:29696\n\t"
    "v_add_f32 v45, v45, v90\n\t"
    "v_mfma_f32_16x16x32_bf16 v[84:87], a[152:155], a[8:11], v[84:87]\n\t"
    "v_exp_f32 v91, v91\n\t"
    "v_mfma_f32_16x16x32_bf16 v[84:87], a[156:159], a[12:15], v[84:87]\n\t"
    "v_add_f32 v45, v45, v91\n\t"
    "v_cvt_pk_bf16_f32 v77, v90, v91\n\t"
    "v_mfma_f32_16x16x32_bf16 v[192:195], a[160:163], v[72:75], v[192:195]\n\t"
    "v_exp_f32 v92, v92\n\t"
    "v_mfma_f32_16x16x32_bf16 v[196:199], a[164:167], v[72:75], v[196:199]\n\t"
    "ds_read_b64_tr_b16 a[212:213], v9 offset:25600\n\t"
    "v_add_f32 v45, v45, v92\n\t"
    "v_mfma_f32_16x16x32_bf16 v[200:203], a[168:171], v[72:75], v[200:203]\n\t"
    "v_exp_f32 v93, v93\n\t"
    "v_mfma_f32_16x16x32_bf16 v[204:207], a[172:175], v[72:75], v[204:207]\n\t"
    "v_add_f32 v45, v45, v93\n\t"
    "v_cvt_pk_bf16_f32 v78, v92, v93\n\t"
    "v_mfma_f32_16x16x32_bf16 v[208:211], a[176:179], v[72:75], v[208:211]\n\t"
    "v_exp_f32 v94, v94\n\t"
    "v_mfma_f32_16x16x32_bf16 v[212:215], a[180:183], v[72:75], v[212:215]\n\t"
    "ds_read_b64_tr_b16 a[214:215], v9 offset:29696\n\t"
    "v_add_f32 v45, v45, v94\n\t"
    "v_mfma_f32_16x16x32_bf16 v[216:219], a[184:187], v[72:75], v[216:219]\n\t"
    "v_exp_f32 v95, v95\n\t"
    "v_mfma_f32_16x16x32_bf16 v[220:223], a[188:191], v[72:75], v[220:223]\n\t"
    "v_add_f32 v45, v45, v95\n\t"
    "v_cvt_pk_bf16_f32 v79, v94, v95\n\t"
    "v_mfma_f32_16x16x32_bf16 v[88:91], a[128:131], a[16:19], v[52:55]\n\t"
    "v_exp_f32 v80, v80\n\t"
    "v_mfma_f32_16x16x32_bf16 v[88:91], a[132:135], a[20:23], v[88:91]\n\t"
    "ds_read_b64_tr_b16 a[216:217], v10 offset:25600\n\t"
    "v_add_f32 v40, v40, v80\n\t"
    "v_mfma_f32_16x16x32_bf16 v[88:91], a[136:139], a[24:27], v[88:91]\n\t"
    "v_exp_f32 v81, v81\n\t"
    "v_mfma_f32_16x16x32_bf16 v[88:91], a[140:143], a[28:31], v[88:91]\n\t"
    "v_add_f32 v40, v40, v81\n\t"
    "v_cvt_pk_bf16_f32 v72, v80, v81\n\t"
    "v_mfma_f32_16x16x32_bf16 v[92:95], a[144:147], a[16:19], v[52:55]\n\t"
    "v_exp_f32 v82, v82\n\t"
    "v_mfma_f32_16x16x32_bf16 v[92:95], a[148:151], a[20:23], v[92:95]\n\t"
    "ds_read_b64_tr_b16 a[218:219], v10 offset:29696\n\t"
    "v_add_f32 v40, v40, v82\n\t"
    "v_mfma_f32_16x16x32_bf16 v[92:95], a[152:155], a[24:27], v[92:95]\n\t"
    "v_exp_f32 v83, v83\n\t"
    "v_mfma_f32_16x16x32_bf16 v[92:95], a[156:159], a[28:31], v[92:95]\n\t"
    "v_add_f32 v40, v40, v83\n\t"
    "v_cvt_pk_bf16_f32 v73, v82, v83\n\t"
    "v_mfma_f32_16x16x32_bf16 v[224:227], a[160:163], v[76:79], v[224:227]\n\t"
    "v_exp_f32 v84, v84\n\t"
    "v_mfma_f32_16x16x32_bf16 v[228:231], a[164:167], v[76:79], v[228:231]\n\t"
    "ds_read_b64_tr_b16 a[220:221], v11 offset:25600\n\t"
    "v_add_f32 v40, v40, v84\n\t"
    "v_mfma_f32_16x16x32_bf16 v[232:235], a[168:171], v[76:79], v[232:235]\n\t"
    "v_exp_f32 v85, v85\n\t"
    "v_mfma_f32_16x16x32_bf16 v[236:239], a[172:175], v[76:79], v[236:239]\n\t"
    "v_add_f32 v40, v40, v85\n\t"
    "v_cvt_pk_bf16_f32 v74, v84, v85\n\t"
    "v_mfma_f32_16x16x32_bf16 v[240:243], a[176:179], v[76:79], v[240:243]\n\t"
    "v_exp_f32 v86, v86\n\t"
    "v_mfma_f32_16x16x32_bf16 v[244:247], a[180:183], v[76:79], v[244:247]\n\t"
    "ds_read_b64_tr_b16 a[222:223], v11 offset:29696\n\t"
    "v_add_f32 v40, v40, v86\n\t"
    "v_mfma_f32_16x16x32_bf16 v[248:251], a[184:187], v[76:79], v[248:251]\n\t"
    "v_exp_f32 v87, v87\n\t"
    "v_mfma_f32_16x16x32_bf16 v[252:255], a[188:191], v[76:79], v[252:255]\n\t"
    "s_barrier\n\t"
    "v_add_f32 v40, v40, v87\n\t"
    "v_cvt_pk_bf16_f32 v75, v86, v87\n\t"
    "v_mfma_f32_16x16x32_bf16 v[80:83], a[128:131], a[32:35], v[56:59]\n\t"
    "v_exp_f32 v88, v88\n\t"
    "v_mfma_f32_16x16x32_bf16 v[80:83], a[132:135], a[36:39], v[80:83]\n\t"
    "ds_read_b128 a[96:99], v4 offset:32768\n\t"
    "v_add_f32 v41, v41, v88\n\t"
    "v_mfma_f32_16x16x32_bf16 v[80:83], a[136:139], a[40:43], v[80:83]\n\t"
    "v_exp_f32 v89, v89\n\t"
    "v_mfma_f32_16x16x32_bf16 v[80:83], a[140:143], a[44:47], v[80:83]\n\t"
    "v_add_f32 v41, v41, v89\n\t"
    "v_cvt_pk_bf16_f32 v76, v88, v89\n\t"
    "v_mfma_f32_16x16x32_bf16 v[84:87], a[144:147], a[32:35], v[56:59]\n\t"
    "v_exp_f32 v90, v90\n\t"
    "v_mfma_f32_16x16x32_bf16 v[84:87], a[148:151], a[36:39], v[84:87]\n\t"
    "ds_read_b128 a[100:103], v5 offset:32768\n\t"
    "v_add_f32 v41, v41, v90\n\t"
    "v_mfma_f32_16x16x32_bf16 v[84:87], a[152:155], a[40:43], v[84:87]\n\t"
    "v_exp_f32 v91, v91\n\t"
    "v_mfma_f32_16x16x32_bf16 v[84:87], a[156:159], a[44:47], v[84:87]\n\t"
    "s_waitcnt lgkmcnt(2)\n\t"
    "v_add_f32 v41, v41, v91\n\t"
    "v_cvt_pk_bf16_f32 v77, v90, v91\n\t"
    "v_mfma_f32_16x16x32_bf16 a[224:227], a[192:195], v[72:75], a[224:227]\n\t"
    "v_exp_f32 v92, v92\n\t"
    "v_mfma_f32_16x16x32_bf16 a[228:231], a[196:199], v[72:75], a[228:231]\n\t"
    "ds_read_b128 a[104:107], v6 offset:32768\n\t"
    "v_add_f32 v41, v41, v92\n\t"
    "v_mfma_f32_16x16x32_bf16 a[232:235], a[200:203], v[72:75], a[232:235]\n\t"
    "v_exp_f32 v93, v93\n\t"
    "v_mfma_f32_16x16x32_bf16 a[236:239], a[204:207], v[72:75], a[236:239]\n\t"
    "v_add_f32 v41, v41, v93\n\t"
    "v_cvt_pk_bf16_f32 v78, v92, v93\n\t"
    "v_mfma_f32_16x16x32_bf16 a[240:243], a[208:211], v[72:75], a[240:243]\n\t"
    "v_exp_f32 v94, v94\n\t"
    "v_mfma_f32_16x16x32_bf16 a[244:247], a[212:215], v[72:75], a[244:247]\n\t"
    "ds_read_b128 a[108:111], v7 offset:32768\n\t"
    "v_add_f32 v41, v41, v94\n\t"
    "v_mfma_f32_16x16x32_bf16 a[248:251], a[216:219], v[72:75], a[248:251]\n\t"
    "v_exp_f32 v95, v95\n\t"
    "v_mfma_f32_16x16x32_bf16 a[252:255], a[220:223], v[72:75], a[252:255]\n\t"
    "v_add_f32 v41, v41, v95\n\t"
    "v_cvt_pk_bf16_f32 v79, v94, v95\n\t"
    "v_mfma_f32_16x16x32_bf16 v[88:91], a[128:131], a[48:51], v[60:63]\n\t"
    "v_exp_f32 v80, v80\n\t"
    "v_mfma_f32_16x16x32_bf16 v[88:91], a[132:135], a[52:55], v[88:91]\n\t"
    "ds_read_b128 a[112:115], v4 offset:36864\n\t"
    "v_add_f32 v42, v42, v80\n\t"
    "v_mfma_f32_16x16x32_bf16 v[88:91], a[136:139], a[56:59], v[88:91]\n\t"
    "v_exp_f32 v81, v81\n\t"
    "v_mfma_f32_16x16x32_bf16 v[88:91], a[140:143], a[60:63], v[88:91]\n\t"
    "v_add_f32 v42, v42, v81\n\t"
    "v_cvt_pk_bf16_f32 v72, v80, v81\n\t"
    "v_mfma_f32_16x16x32_bf16 v[92:95], a[144:147], a[48:51], v[60:63]\n\t"
    "v_exp_f32 v82, v82\n\t"
    "v_mfma_f32_16x16x32_bf16 v[92:95], a[148:151], a[52:55], v[92:95]\n\t"
    "ds_read_b128 a[116:119], v5 offset:36864\n\t"
    "v_add_f32 v42, v42, v82\n\t"
    "v_mfma_f32_16x16x32_bf16 v[92:95], a[152:155], a[56:59], v[92:95]\n\t"
    "v_exp_f32 v83, v83\n\t"
    "v_mfma_f32_16x16x32_bf16 v[92:95], a[156:159], a[60:63], v[92:95]\n\t"
    "v_add_f32 v42, v42, v83\n\t"
    "v_cvt_pk_bf16_f32 v73, v82, v83\n\t"
    "v_mfma_f32_16x16x32_bf16 v[96:99], a[192:195], v[76:79], v[96:99]\n\t"
    "v_exp_f32 v84, v84\n\t"
    "v_mfma_f32_16x16x32_bf16 v[100:103], a[196:199], v[76:79], v[100:103]\n\t"
    "ds_read_b128 a[120:123], v6 offset:36864\n\t"
    "v_add_f32 v42, v42, v84\n\t"
    "v_mfma_f32_16x16x32_bf16 v[104:107], a[200:203], v[76:79], v[104:107]\n\t"
    "v_exp_f32 v85, v85\n\t"
    "v_mfma_f32_16x16x32_bf16 v[108:111], a[204:207], v[76:79], v[108:111]\n\t"
    "v_add_f32 v42, v42, v85\n\t"
    "v_cvt_pk_bf16_f32 v74, v84, v85\n\t"
    "v_mfma_f32_16x16x32_bf16 v[112:115], a[208:211], v[76:79], v[112:115]\n\t"
    "v_exp_f32 v86, v86\n\t"
    "v_mfma_f32_16x16x32_bf16 v[116:119], a[212:215], v[76:79], v[116:119]\n\t"
    "ds_read_b128 a[124:127], v7 offset:36864\n\t"
    "v_add_f32 v42, v42, v86\n\t"
    "v_mfma_f32_16x16x32_bf16 v[120:123], a[216:219], v[76:79], v[120:123]\n\t"
    "v_exp_f32 v87, v87\n\t"
    "v_mfma_f32_16x16x32_bf16 v[124:127], a[220:223], v[76:79], v[124:127]\n\t"
    "v_add_f32 v42, v42, v87\n\t"
    "v_cvt_pk_bf16_f32 v75, v86, v87\n\t"
    "v_mfma_f32_16x16x32_bf16 v[80:83], a[128:131], a[64:67], v[64:67]\n\t"
    "v_exp_f32 v88, v88\n\t"
    "v_mfma_f32_16x16x32_bf16 v[80:83], a[132:135], a[68:71], v[80:83]\n\t"
    "ds_read_b64_tr_b16 a[160:161], v8 offset:40960\n\t"
    "v_add_f32 v43, v43, v88\n\t"
    "v_mfma_f32_16x16x32_bf16 v[80:83], a[136:139], a[72:75], v[80:83]\n\t"
    "v_exp_f32 v89, v89\n\t"
    "v_mfma_f32_16x16x32_bf16 v[80:83], a[140:143], a[76:79], v[80:83]\n\t"
    "v_add_f32 v43, v43, v89\n\t"
    "v_cvt_pk_bf16_f32 v76, v88, v89\n\t"
    "v_mfma_f32_16x16x32_bf16 v[84:87], a[144:147], a[64:67], v[64:67]\n\t"
    "v_exp_f32 v90, v90\n\t"
    "v_mfma_f32_16x16x32_bf16 v[84:87], a[148:151], a[68:71], v[84:87]\n\t"
    "ds_read_b64_tr_b16 a[162:163], v8 offset:45056\n\t"
    "v_add_f32 v43, v43, v90\n\t"
    "v_mfma_f32_16x16x32_bf16 v[84:87], a[152:155], a[72:75], v[84:87]\n\t"
    "v_exp_f32 v91, v91\n\t"
    "v_mfma_f32_16x16x32_bf16 v[84:87], a[156:159], a[76:79], v[84:87]\n\t"
    "v_add_f32 v43, v43, v91\n\t"
    "v_cvt_pk_bf16_f32 v77, v90, v91\n\t"
    "v_mfma_f32_16x16x32_bf16 v[128:131], a[192:195], v[72:75], v[128:131]\n\t"
    "v_exp_f32 v92, v92\n\t"
    "v_mfma_f32_16x16x32_bf16 v[132:135], a[196:199], v[72:75], v[132:135]\n\t"
    "ds_read_b64_tr_b16 a[164:165], v9 offset:40960\n\t"
    "v_add_f32 v43, v43, v92\n\t"
    "v_mfma_f32_16x16x32_bf16 v[136:139], a[200:203], v[72:75], v[136:139]\n\t"
    "v_exp_f32 v93, v93\n\t"
    "v_mfma_f32_16x16x32_bf16 v[140:143], a[204:207], v[72:75], v[140:143]\n\t"
    "v_add_f32 v43, v43, v93\n\t"
    "v_cvt_pk_bf16_f32 v78, v92, v93\n\t"
    "v_mfma_f32_16x16x32_bf16 v[144:147], a[208:211], v[72:75], v[144:147]\n\t"
    "v_exp_f32 v94, v94\n\t"
    "v_mfma_f32_16x16x32_bf16 v[148:151], a[212:215], v[72:75], v[148:151]\n\t"
    "ds_read_b64_tr_b16 a[166:167], v9 offset:45056\n\t"
    "v_add_f32 v43, v43, v94\n\t"
    "v_mfma_f32_16x16x32_bf16 v[152:155], a[216:219], v[72:75], v[152:155]\n\t"
    "v_exp_f32 v95, v95\n\t"
    "v_mfma_f32_16x16x32_bf16 v[156:159], a[220:223], v[72:75], v[156:159]\n\t"
    "v_add_f32 v43, v43, v95\n\t"
    "v_cvt_pk_bf16_f32 v79, v94, v95\n\t"
    "v_mfma_f32_16x16x32_bf16 v[88:91], a[128:131], a[80:83], v[68:71]\n\t"
    "v_exp_f32 v80, v80\n\t"
    "v_mfma_f32_16x16x32_bf16 v[88:91], a[132:135], a[84:87], v[88:91]\n\t"
    "ds_read_b64_tr_b16 a[168:169], v10 offset:40960\n\t"
    "v_add_f32 v44, v44, v80\n\t"
    "v_mfma_f32_16x16x32_bf16 v[88:91], a[136:139], a[88:91], v[88:91]\n\t"
    "v_exp_f32 v81, v81\n\t"
    "v_mfma_f32_16x16x32_bf16 v[88:91], a[140:143], a[92:95], v[88:91]\n\t"
    "v_add_f32 v44, v44, v81\n\t"
    "v_cvt_pk_bf16_f32 v72, v80, v81\n\t"
    "v_mfma_f32_16x16x32_bf16 v[92:95], a[144:147], a[80:83], v[68:71]\n\t"
    "v_exp_f32 v82, v82\n\t"
    "v_mfma_f32_16x16x32_bf16 v[92:95], a[148:151], a[84:87], v[92:95]\n\t"
    "ds_read_b64_tr_b16 a[170:171], v10 offset:45056\n\t"
    "v_add_f32 v44, v44, v82\n\t"
    "v_mfma_f32_16x16x32_bf16 v[92:95], a[152:155], a[88:91], v[92:95]\n\t"
    "v_exp_f32 v83, v83\n\t"
    "v_mfma_f32_16x16x32_bf16 v[92:95], a[156:159], a[92:95], v[92:95]\n\t"
    "v_add_f32 v44, v44, v83\n\t"
    "v_cvt_pk_bf16_f32 v73, v82, v83\n\t"
    "v_mfma_f32_16x16x32_bf16 v[160:163], a[192:195], v[76:79], v[160:163]\n\t"
    "v_exp_f32 v84, v84\n\t"
    "v_mfma_f32_16x16x32_bf16 v[164:167], a[196:199], v[76:79], v[164:167]\n\t"
    "ds_read_b64_tr_b16 a[172:173], v11 offset:40960\n\t"
    "v_add_f32 v44, v44, v84\n\t"
    "v_mfma_f32_16x16x32_bf16 v[168:171], a[200:203], v[76:79], v[168:171]\n\t"
    "v_exp_f32 v85, v85\n\t"
    "v_mfma_f32_16x16x32_bf16 v[172:175], a[204:207], v[76:79], v[172:175]\n\t"
    "v_add_f32 v44, v44, v85\n\t"
    "v_cvt_pk_bf16_f32 v74, v84, v85\n\t"
    "v_mfma_f32_16x16x32_bf16 v[176:179], a[208:211], v[76:79], v[176:179]\n\t"
    "v_exp_f32 v86, v86\n\t"
    "v_mfma_f32_16x16x32_bf16 v[180:183], a[212:215], v[76:79], v[180:183]\n\t"
    "ds_read_b64_tr_b16 a[174:175], v11 offset:45056\n\t"
    "v_add_f32 v44, v44, v86\n\t"
    "v_mfma_f32_16x16x32_bf16 v[184:187], a[216:219], v[76:79], v[184:187]\n\t"
    "v_exp_f32 v87, v87\n\t"
    "v_mfma_f32_16x16x32_bf16 v[188:191], a[220:223], v[76:79], v[188:191]\n\t"
    "s_waitcnt lgkmcnt(8)\n\t"
    "v_add_f32 v44, v44, v87\n\t"
    "v_cvt_pk_bf16_f32 v75, v86, v87\n\t"
    "v_mfma_f32_16x16x32_bf16 v[80:83], a[96:99], a[0:3], v[48:51]\n\t"
    "v_exp_f32 v88, v88\n\t"
    "v_mfma_f32_16x16x32_bf16 v[80:83], a[100:103], a[4:7], v[80:83]\n\t"
    "ds_read_b64_tr_b16 a[176:177], v8 offset:41984\n\t"
    "v_add_f32 v45, v45, v88\n\t"
    "v_mfma_f32_16x16x32_bf16 v[80:83], a[104:107], a[8:11], v[80:83]\n\t"
    "v_exp_f32 v89, v89\n\t"
    "v_mfma_f32_16x16x32_bf16 v[80:83], a[108:111], a[12:15], v[80:83]\n\t"
    "v_add_f32 v45, v45, v89\n\t"
    "v_cvt_pk_bf16_f32 v76, v88, v89\n\t"
    "v_mfma_f32_16x16x32_bf16 v[84:87], a[112:115], a[0:3], v[48:51]\n\t"
    "v_exp_f32 v90, v90\n\t"
    "v_mfma_f32_16x16x32_bf16 v[84:87], a[116:119], a[4:7], v[84:87]\n\t"
    "ds_read_b64_tr_b16 a[178:179], v8 offset:46080\n\t"
    "v_add_f32 v45, v45, v90\n\t"
    "v_mfma_f32_16x16x32_bf16 v[84:87], a[120:123], a[8:11], v[84:87]\n\t"
    "v_exp_f32 v91, v91\n\t"
    "v_mfma_f32_16x16x32_bf16 v[84:87], a[124:127], a[12:15], v[84:87]\n\t"
    "v_add_f32 v45, v45, v91\n\t"
    "v_cvt_pk_bf16_f32 v77, v90, v91\n\t"
    "v_mfma_f32_16x16x32_bf16 v[192:195], a[192:195], v[72:75], v[192:195]\n\t"
    "v_exp_f32 v92, v92\n\t"
    "v_mfma_f32_16x16x32_bf16 v[196:199], a[196:199], v[72:75], v[196:199]\n\t"
    "ds_read_b64_tr_b16 a[180:181], v9 offset:41984\n\t"
    "v_add_f32 v45, v45, v92\n\t"
    "v_mfma_f32_16x16x32_bf16 v[200:203], a[200:203], v[72:75], v[200:203]\n\t"
    "v_exp_f32 v93, v93\n\t"
    "v_mfma_f32_16x16x32_bf16 v[204:207], a[204:207], v[72:75], v[204:207]\n\t"
    "v_add_f32 v45, v45, v93\n\t"
    "v_cvt_pk_bf16_f32 v78, v92, v93\n\t"
    "v_mfma_f32_16x16x32_bf16 v[208:211], a[208:211], v[72:75], v[208:211]\n\t"
    "v_exp_f32 v94, v94\n\t"
    "v_mfma_f32_16x16x32_bf16 v[212:215], a[212:215], v[72:75], v[212:215]\n\t"
    "ds_read_b64_tr_b16 a[182:183], v9 offset:46080\n\t"
    "v_add_f32 v45, v45, v94\n\t"
    "v_mfma_f32_16x16x32_bf16 v[216:219], a[216:219], v[72:75], v[216:219]\n\t"
    "v_exp_f32 v95, v95\n\t"
    "v_mfma_f32_16x16x32_bf16 v[220:223], a[220:223], v[72:75], v[220:223]\n\t"
    "v_add_f32 v45, v45, v95\n\t"
    "v_cvt_pk_bf16_f32 v79, v94, v95\n\t"
    "v_mfma_f32_16x16x32_bf16 v[88:91], a[96:99], a[16:19], v[52:55]\n\t"
    "v_exp_f32 v80, v80\n\t"
    "v_mfma_f32_16x16x32_bf16 v[88:91], a[100:103], a[20:23], v[88:91]\n\t"
    "ds_read_b64_tr_b16 a[184:185], v10 offset:41984\n\t"
    "v_add_f32 v40, v40, v80\n\t"
    "v_mfma_f32_16x16x32_bf16 v[88:91], a[104:107], a[24:27], v[88:91]\n\t"
    "v_exp_f32 v81, v81\n\t"
    "v_mfma_f32_16x16x32_bf16 v[88:91], a[108:111], a[28:31], v[88:91]\n\t"
    "v_add_f32 v40, v40, v81\n\t"
    "v_cvt_pk_bf16_f32 v72, v80, v81\n\t"
    "v_mfma_f32_16x16x32_bf16 v[92:95], a[112:115], a[16:19], v[52:55]\n\t"
    "v_exp_f32 v82, v82\n\t"
    "v_mfma_f32_16x16x32_bf16 v[92:95], a[116:119], a[20:23], v[92:95]\n\t"
    "ds_read_b64_tr_b16 a[186:187], v10 offset:46080\n\t"
    "v_add_f32 v40, v40, v82\n\t"
    "v_mfma_f32_16x16x32_bf16 v[92:95], a[120:123], a[24:27], v[92:95]\n\t"
    "v_exp_f32 v83, v83\n\t"
    "v_mfma_f32_16x16x32_bf16 v[92:95], a[124:127], a[28:31], v[92:95]\n\t"
    "v_add_f32 v40, v40, v83\n\t"
    "v_cvt_pk_bf16_f32 v73, v82, v83\n\t"
    "v_mfma_f32_16x16x32_bf16 v[224:227], a[192:195], v[76:79], v[224:227]\n\t"
    "v_exp_f32 v84, v84\n\t"
    "v_mfma_f32_16x16x32_bf16 v[228:231], a[196:199], v[76:79], v[228:231]\n\t"
    "ds_read_b64_tr_b16 a[188:189], v11 offset:41984\n\t"
    "v_add_f32 v40, v40, v84\n\t"
    "v_mfma_f32_16x16x32_bf16 v[232:235], a[200:203], v[76:79], v[232:235]\n\t"
    "v_exp_f32 v85, v85\n\t"
    "v_mfma_f32_16x16x32_bf16 v[236:239], a[204:207], v[76:79], v[236:239]\n\t"
    "v_add_f32 v40, v40, v85\n\t"
    "v_cvt_pk_bf16_f32 v74, v84, v85\n\t"
    "v_mfma_f32_16x16x32_bf16 v[240:243], a[208:211], v[76:79], v[240:243]\n\t"
    "v_exp_f32 v86, v86\n\t"
    "v_mfma_f32_16x16x32_bf16 v[244:247], a[212:215], v[76:79], v[244:247]\n\t"
    "ds_read_b64_tr_b16 a[190:191], v11 offset:46080\n\t"
    "v_add_f32 v40, v40, v86\n\t"
    "v_mfma_f32_16x16x32_bf16 v[248:251], a[216:219], v[76:79], v[248:251]\n\t"
    "v_exp_f32 v87, v87\n\t"
    "v_mfma_f32_16x16x32_bf16 v[252:255], a[220:223], v[76:79], v[252:255]\n\t"
    "s_barrier\n\t"
    "v_add_f32 v40, v40, v87\n\t"
    "v_cvt_pk_bf16_f32 v75, v86, v87\n\t"
    "v_mfma_f32_16x16x32_bf16 v[80:83], a[96:99], a[32:35], v[56:59]\n\t"
    "v_exp_f32 v88, v88\n\t"
    "v_mfma_f32_16x16x32_bf16 v[80:83], a[100:103], a[36:39], v[80:83]\n\t"
    "ds_read_b128 a[128:131], v4 offset:49152\n\t"
    "v_add_f32 v41, v41, v88\n\t"
    "v_mfma_f32_16x16x32_bf16 v[80:83], a[104:107], a[40:43], v[80:83]\n\t"
    "v_exp_f32 v89, v89\n\t"
    "v_mfma_f32_16x16x32_bf16 v[80:83], a[108:111], a[44:47], v[80:83]\n\t"
    "v_add_f32 v41, v41, v89\n\t"
    "v_cvt_pk_bf16_f32 v76, v88, v89\n\t"
    "v_mfma_f32_16x16x32_bf16 v[84:87], a[112:115], a[32:35], v[56:59]\n\t"
    "v_exp_f32 v90, v90\n\t"
    "v_mfma_f32_16x16x32_bf16 v[84:87], a[116:119], a[36:39], v[84:87]\n\t"
    "ds_read_b128 a[132:135], v5 offset:49152\n\t"
    "v_add_f32 v41, v41, v90\n\t"
    "v_mfma_f32_16x16x32_bf16 v[84:87], a[120:123], a[40:43], v[84:87]\n\t"
    "v_exp_f32 v91, v91\n\t"
    "v_mfma_f32_16x16x32_bf16 v[84:87], a[124:127], a[44:47], v[84:87]\n\t"
    "s_waitcnt lgkmcnt(2)\n\t"
    "v_add_f32 v41, v41, v91\n\t"
    "v_cvt_pk_bf16_f32 v77, v90, v91\n\t"
    "v_mfma_f32_16x16x32_bf16 a[224:227], a[160:163], v[72:75], a[224:227]\n\t"
    "v_exp_f32 v92, v92\n\t"
    "v_mfma_f32_16x16x32_bf16 a[228:231], a[164:167], v[72:75], a[228:231]\n\t"
    "ds_read_b128 a[136:139], v6 offset:49152\n\t"
    "v_add_f32 v41, v41, v92\n\t"
    "v_mfma_f32_16x16x32_bf16 a[232:235], a[168:171], v[72:75], a[232:235]\n\t"
    "v_exp_f32 v93, v93\n\t"
    "v_mfma_f32_16x16x32_bf16 a[236:239], a[172:175], v[72:75], a[236:239]\n\t"
    "v_add_f32 v41, v41, v93\n\t"
    "v_cvt_pk_bf16_f32 v78, v92, v93\n\t"
    "v_mfma_f32_16x16x32_bf16 a[240:243], a[176:179], v[72:75], a[240:243]\n\t"
    "v_exp_f32 v94, v94\n\t"
    "v_mfma_f32_16x16x32_bf16 a[244:247], a[180:183], v[72:75], a[244:247]\n\t"
    "ds_read_b128 a[140:143], v7 offset:49152\n\t"
    "v_add_f32 v41, v41, v94\n\t"
    "v_mfma_f32_16x16x32_bf16 a[248:251], a[184:187], v[72:75], a[248:251]\n\t"
    "v_exp_f32 v95, v95\n\t"
    "v_mfma_f32_16x16x32_bf16 a[252:255], a[188:191], v[72:75], a[252:255]\n\t"
    "v_add_f32 v41, v41, v95\n\t"
    "v_cvt_pk_bf16_f32 v79, v94, v95\n\t"
    "v_mfma_f32_16x16x32_bf16 v[88:91], a[96:99], a[48:51], v[60:63]\n\t"
    "v_exp_f32 v80, v80\n\t"
    "v_mfma_f32_16x16x32_bf16 v[88:91], a[100:103], a[52:55], v[88:91]\n\t"
    "ds_read_b128 a[144:147], v4 offset:53248\n\t"
    "v_add_f32 v42, v42, v80\n\t"
    "v_mfma_f32_16x16x32_bf16 v[88:91], a[104:107], a[56:59], v[88:91]\n\t"
    "v_exp_f32 v81, v81\n\t"
    "v_mfma_f32_16x16x32_bf16 v[88:91], a[108:111], a[60:63], v[88:91]\n\t"
    "v_add_f32 v42, v42, v81\n\t"
    "v_cvt_pk_bf16_f32 v72, v80, v81\n\t"
    "v_mfma_f32_16x16x32_bf16 v[92:95], a[112:115], a[48:51], v[60:63]\n\t"
    "v_exp_f32 v82, v82\n\t"
    "v_mfma_f32_16x16x32_bf16 v[92:95], a[116:119], a[52:55], v[92:95]\n\t"
    "ds_read_b128 a[148:151], v5 offset:53248\n\t"
    "v_add_f32 v42, v42, v82\n\t"
    "v_mfma_f32_16x16x32_bf16 v[92:95], a[120:123], a[56:59], v[92:95]\n\t"
    "v_exp_f32 v83, v83\n\t"
    "v_mfma_f32_16x16x32_bf16 v[92:95], a[124:127], a[60:63], v[92:95]\n\t"
    "v_add_f32 v42, v42, v83\n\t"
    "v_cvt_pk_bf16_f32 v73, v82, v83\n\t"
    "v_mfma_f32_16x16x32_bf16 v[96:99], a[160:163], v[76:79], v[96:99]\n\t"
    "v_exp_f32 v84, v84\n\t"
    "v_mfma_f32_16x16x32_bf16 v[100:103], a[164:167], v[76:79], v[100:103]\n\t"
    "ds_read_b128 a[152:155], v6 offset:53248\n\t"
    "v_add_f32 v42, v42, v84\n\t"
    "v_mfma_f32_16x16x32_bf16 v[104:107], a[168:171], v[76:79], v[104:107]\n\t"
    "v_exp_f32 v85, v85\n\t"
    "v_mfma_f32_16x16x32_bf16 v[108:111], a[172:175], v[76:79], v[108:111]\n\t"
    "v_add_f32 v42, v42, v85\n\t"
    "v_cvt_pk_bf16_f32 v74, v84, v85\n\t"
    "v_mfma_f32_16x16x32_bf16 v[112:115], a[176:179], v[76:79], v[112:115]\n\t"
    "v_exp_f32 v86, v86\n\t"
    "v_mfma_f32_16x16x32_bf16 v[116:119], a[180:183], v[76:79], v[116:119]\n\t"
    "ds_read_b128 a[156:159], v7 offset:53248\n\t"
    "v_add_f32 v42, v42, v86\n\t"
    "v_mfma_f32_16x16x32_bf16 v[120:123], a[184:187], v[76:79], v[120:123]\n\t"
    "v_exp_f32 v87, v87\n\t"
    "v_mfma_f32_16x16x32_bf16 v[124:127], a[188:191], v[76:79], v[124:127]\n\t"
    "v_add_f32 v42, v42, v87\n\t"
    "v_cvt_pk_bf16_f32 v75, v86, v87\n\t"
    "v_mfma_f32_16x16x32_bf16 v[80:83], a[96:99], a[64:67], v[64:67]\n\t"
    "v_exp_f32 v88, v88\n\t"
    "v_mfma_f32_16x16x32_bf16 v[80:83], a[100:103], a[68:71], v[80:83]\n\t"
    "ds_read_b64_tr_b16 a[192:193], v8 offset:57344\n\t"
    "v_add_f32 v43, v43, v88\n\t"
    "v_mfma_f32_16x16x32_bf16 v[80:83], a[104:107], a[72:75], v[80:83]\n\t"
    "v_exp_f32 v89, v89\n\t"
    "v_mfma_f32_16x16x32_bf16 v[80:83], a[108:111], a[76:79], v[80:83]\n\t"
    "v_add_f32 v43, v43, v89\n\t"
    "v_cvt_pk_bf16_f32 v76, v88, v89\n\t"
    "v_mfma_f32_16x16x32_bf16 v[84:87], a[112:115], a[64:67], v[64:67]\n\t"
    "v_exp_f32 v90, v90\n\t"
    "v_mfma_f32_16x16x32_bf16 v[84:87], a[116:119], a[68:71], v[84:87]\n\t"
    "ds_read_b64_tr_b16 a[194:195], v8 offset:61440\n\t"
    "v_add_f32 v43, v43, v90\n\t"
    "v_mfma_f32_16x16x32_bf16 v[84:87], a[120:123], a[72:75], v[84:87]\n\t"
    "v_exp_f32 v91, v91\n\t"
    "v_mfma_f32_16x16x32_bf16 v[84:87], a[124:127], a[76:79], v[84:87]\n\t"
    "v_add_f32 v43, v43, v91\n\t"
    "v_cvt_pk_bf16_f32 v77, v90, v91\n\t"
    "v_mfma_f32_16x16x32_bf16 v[128:131], a[160:163], v[72:75], v[128:131]\n\t"
    "v_exp_f32 v92, v92\n\t"
    "v_mfma_f32_16x16x32_bf16 v[132:135], a[164:167], v[72:75], v[132:135]\n\t"
    "ds_read_b64_tr_b16 a[196:197], v9 offset:57344\n\t"
    "v_add_f32 v43, v43, v92\n\t"
    "v_mfma_f32_16x16x32_bf16 v[136:139], a[168:171], v[72:75], v[136:139]\n\t"
    "v_exp_f32 v93, v93\n\t"
    "v_mfma_f32_16x16x32_bf16 v[140:143], a[172:175], v[72:75], v[140:143]\n\t"
    "v_add_f32 v43, v43, v93\n\t"
    "v_cvt_pk_bf16_f32 v78, v92, v93\n\t"
    "v_mfma_f32_16x16x32_bf16 v[144:147], a[176:179], v[72:75], v[144:147]\n\t"
    "v_exp_f32 v94, v94\n\t"
    "v_mfma_f32_16x16x32_bf16 v[148:151], a[180:183], v[72:75], v[148:151]\n\t"
    "ds_read_b64_tr_b16 a[198:199], v9 offset:61440\n\t"
    "v_add_f32 v43, v43, v94\n\t"
    "v_mfma_f32_16x16x32_bf16 v[152:155], a[184:187], v[72:75], v[152:155]\n\t"
    "v_exp_f32 v95, v95\n\t"
    "v_mfma_f32_16x16x32_bf16 v[156:159], a[188:191], v[72:75], v[156:159]\n\t"
    "v_add_f32 v43, v43, v95\n\t"
    "v_cvt_pk_bf16_f32 v79, v94, v95\n\t"
    "v_mfma_f32_16x16x32_bf16 v[88:91], a[96:99], a[80:83], v[68:71]\n\t"
    "v_exp_f32 v80, v80\n\t"
    "v_mfma_f32_16x16x32_bf16 v[88:91], a[100:103], a[84:87], v[88:91]\n\t"
    "ds_read_b64_tr_b16 a[200:201], v10 offset:57344\n\t"
    "v_add_f32 v44, v44, v80\n\t"
    "v_mfma_f32_16x16x32_bf16 v[88:91], a[104:107], a[88:91], v[88:91]\n\t"
    "v_exp_f32 v81, v81\n\t"
    "v_mfma_f32_16x16x32_bf16 v[88:91], a[108:111], a[92:95], v[88:91]\n\t"
    "v_add_f32 v44, v44, v81\n\t"
    "v_cvt_pk_bf16_f32 v72, v80, v81\n\t"
    "v_mfma_f32_16x16x32_bf16 v[92:95], a[112:115], a[80:83], v[68:71]\n\t"
    "v_exp_f32 v82, v82\n\t"
    "v_mfma_f32_16x16x32_bf16 v[92:95], a[116:119], a[84:87], v[92:95]\n\t"
    "ds_read_b64_tr_b16 a[202:203], v10 offset:61440\n\t"
    "v_add_f32 v44, v44, v82\n\t"
    "v_mfma_f32_16x16x32_bf16 v[92:95], a[120:123], a[88:91], v[92:95]\n\t"
    "v_exp_f32 v83, v83\n\t"
    "v_mfma_f32_16x16x32_bf16 v[92:95], a[124:127], a[92:95], v[92:95]\n\t"
    "v_add_f32 v44, v44, v83\n\t"
    "v_cvt_pk_bf16_f32 v73, v82, v83\n\t"
    "v_mfma_f32_16x16x32_bf16 v[160:163], a[160:163], v[76:79], v[160:163]\n\t"
    "v_exp_f32 v84, v84\n\t"
    "v_mfma_f32_16x16x32_bf16 v[164:167], a[164:167], v[76:79], v[164:167]\n\t"
    "ds_read_b64_tr_b16 a[204:205], v11 offset:57344\n\t"
    "v_add_f32 v44, v44, v84\n\t"
    "v_mfma_f32_16x16x32_bf16 v[168:171], a[168:171], v[76:79], v[168:171]\n\t"
    "v_exp_f32 v85, v85\n\t"
    "v_mfma_f32_16x16x32_bf16 v[172:175], a[172:175], v[76:79], v[172:175]\n\t"
    "v_add_f32 v44, v44, v85\n\t"
    "v_cvt_pk_bf16_f32 v74, v84, v85\n\t"
    "v_mfma_f32_16x16x32_bf16 v[176:179], a[176:179], v[76:79], v[176:179]\n\t"
    "v_exp_f32 v86, v86\n\t"
    "v_mfma_f32_16x16x32_bf16 v[180:183], a[180:183], v[76:79], v[180:183]\n\t"
    "ds_read_b64_tr_b16 a[206:207], v11 offset:61440\n\t"
    "v_add_f32 v44, v44, v86\n\t"
    "v_mfma_f32_16x16x32_bf16 v[184:187], a[184:187], v[76:79], v[184:187]\n\t"
    "v_exp_f32 v87, v87\n\t"
    "v_mfma_f32_16x16x32_bf16 v[188:191], a[188:191], v[76:79], v[188:191]\n\t"
    "s_waitcnt lgkmcnt(8)\n\t"
    "v_add_f32 v44, v44, v87\n\t"
    "v_cvt_pk_bf16_f32 v75, v86, v87\n\t"
    "v_mfma_f32_16x16x32_bf16 v[80:83], a[128:131], a[0:3], v[48:51]\n\t"
    "v_exp_f32 v88, v88\n\t"
    "v_mfma_f32_16x16x32_bf16 v[80:83], a[132:135], a[4:7], v[80:83]\n\t"
    "ds_read_b64_tr_b16 a[208:209], v8 offset:58368\n\t"
    "v_add_f32 v45, v45, v88\n\t"
    "v_mfma_f32_16x16x32_bf16 v[80:83], a[136:139], a[8:11], v[80:83]\n\t"
    "v_exp_f32 v89, v89\n\t"
    "v_mfma_f32_16x16x32_bf16 v[80:83], a[140:143], a[12:15], v[80:83]\n\t"
    "v_add_f32 v45, v45, v89\n\t"
    "v_cvt_pk_bf16_f32 v76, v88, v89\n\t"
    "v_mfma_f32_16x16x32_bf16 v[84:87], a[144:147], a[0:3], v[48:51]\n\t"
    "v_exp_f32 v90, v90\n\t"
    "v_mfma_f32_16x16x32_bf16 v[84:87], a[148:151], a[4:7], v[84:87]\n\t"
    "ds_read_b64_tr_b16 a[210:211], v8 offset:62464\n\t"
    "v_add_f32 v45, v45, v90\n\t"
    "v_mfma_f32_16x16x32_bf16 v[84:87], a[152:155], a[8:11], v[84:87]\n\t"
    "v_exp_f32 v91, v91\n\t"
    "v_mfma_f32_16x16x32_bf16 v[84:87], a[156:159], a[12:15], v[84:87]\n\t"
    "v_add_f32 v45, v45, v91\n\t"
    "v_cvt_pk_bf16_f32 v77, v90, v91\n\t"
    "v_mfma_f32_16x16x32_bf16 v[192:195], a[160:163], v[72:75], v[192:195]\n\t"
    "v_exp_f32 v92, v92\n\t"
    "v_mfma_f32_16x16x32_bf16 v[196:199], a[164:167], v[72:75], v[196:199]\n\t"
    "ds_read_b64_tr_b16 a[212:213], v9 offset:58368\n\t"
    "v_add_f32 v45, v45, v92\n\t"
    "v_mfma_f32_16x16x32_bf16 v[200:203], a[168:171], v[72:75], v[200:203]\n\t"
    "v_exp_f32 v93, v93\n\t"
    "v_mfma_f32_16x16x32_bf16 v[204:207], a[172:175], v[72:75], v[204:207]\n\t"
    "v_add_f32 v45, v45, v93\n\t"
    "v_cvt_pk_bf16_f32 v78, v92, v93\n\t"
    "v_mfma_f32_16x16x32_bf16 v[208:211], a[176:179], v[72:75], v[208:211]\n\t"
    "v_exp_f32 v94, v94\n\t"
    "v_mfma_f32_16x16x32_bf16 v[212:215], a[180:183], v[72:75], v[212:215]\n\t"
    "ds_read_b64_tr_b16 a[214:215], v9 offset:62464\n\t"
    "v_add_f32 v45, v45, v94\n\t"
    "v_mfma_f32_16x16x32_bf16 v[216:219], a[184:187], v[72:75], v[216:219]\n\t"
    "v_exp_f32 v95, v95\n\t"
    "v_mfma_f32_16x16x32_bf16 v[220:223], a[188:191], v[72:75], v[220:223]\n\t"
    "v_add_f32 v45, v45, v95\n\t"
    "v_cvt_pk_bf16_f32 v79, v94, v95\n\t"
    "v_mfma_f32_16x16x32_bf16 v[88:91], a[128:131], a[16:19], v[52:55]\n\t"
    "v_exp_f32 v80, v80\n\t"
    "v_mfma_f32_16x16x32_bf16 v[88:91], a[132:135], a[20:23], v[88:91]\n\t"
    "ds_read_b64_tr_b16 a[216:217], v10 offset:58368\n\t"
    "v_add_f32 v40, v40, v80\n\t"
    "v_xor_b32 v4, 0x10000, v4\n\t"
    "v_mfma_f32_16x16x32_bf16 v[88:91], a[136:139], a[24:27], v[88:91]\n\t"
    "v_exp_f32 v81, v81\n\t"
    "v_mfma_f32_16x16x32_bf16 v[88:91], a[140:143], a[28:31], v[88:91]\n\t"
    "v_add_f32 v40, v40, v81\n\t"
    "v_cvt_pk_bf16_f32 v72, v80, v81\n\t"
    "v_mfma_f32_16x16x32_bf16 v[92:95], a[144:147], a[16:19], v[52:55]\n\t"
    "v_exp_f32 v82, v82\n\t"
    "v_mfma_f32_16x16x32_bf16 v[92:95], a[148:151], a[20:23], v[92:95]\n\t"
    "ds_read_b64_tr_b16 a[218:219], v10 offset:62464\n\t"
    "v_add_f32 v40, v40, v82\n\t"
    "v_xor_b32 v5, 0x10000, v5\n\t"
    "v_mfma_f32_16x16x32_bf16 v[92:95], a[152:155], a[24:27], v[92:95]\n\t"
    "v_exp_f32 v83, v83\n\t"
    "v_mfma_f32_16x16x32_bf16 v[92:95], a[156:159], a[28:31], v[92:95]\n\t"
    "v_add_f32 v40, v40, v83\n\t"
    "v_cvt_pk_bf16_f32 v73, v82, v83\n\t"
    "v_mfma_f32_16x16x32_bf16 v[224:227], a[160:163], v[76:79], v[224:227]\n\t"
    "v_exp_f32 v84, v84\n\t"
    "v_mfma_f32_16x16x32_bf16 v[228:231], a[164:167], v[76:79], v[228:231]\n\t"
    "ds_read_b64_tr_b16 a[220:221], v11 offset:58368\n\t"
    "v_add_f32 v40, v40, v84\n\t"
    "v_xor_b32 v6, 0x10000, v6\n\t"
    "v_mfma_f32_16x16x32_bf16 v[232:235], a[168:171], v[76:79], v[232:235]\n\t"
    "v_exp_f32 v85, v85\n\t"
    "v_mfma_f32_16x16x32_bf16 v[236:239], a[172:175], v[76:79], v[236:239]\n\t"
    "v_add_f32 v40, v40, v85\n\t"
    "v_cvt_pk_bf16_f32 v74, v84, v85\n\t"
    "v_mfma_f32_16x16x32_bf16 v[240:243], a[176:179], v[76:79], v[240:243]\n\t"
    "v_exp_f32 v86, v86\n\t"
    "v_mfma_f32_16x16x32_bf16 v[244:247], a[180:183], v[76:79], v[244:247]\n\t"
    "ds_read_b64_tr_b16 a[222:223], v11 offset:62464\n\t"
    "v_add_f32 v40, v40, v86\n\t"
    "v_xor_b32 v7, 0x10000, v7\n\t"
    "v_mfma_f32_16x16x32_bf16 v[248:251], a[184:187], v[76:79], v[248:251]\n\t"
    "v_exp_f32 v87, v87\n\t"
    "v_mfma_f32_16x16x32_bf16 v[252:255], a[188:191], v[76:79], v[252:255]\n\t"
    "s_barrier\n\t"
    "v_add_f32 v40, v40, v87\n\t"
    "v_cvt_pk_bf16_f32 v75, v86, v87\n\t"
    "v_mfma_f32_16x16x32_bf16 v[80:83], a[128:131], a[32:35], v[56:59]\n\t"
    "v_exp_f32 v88, v88\n\t"
    "v_mfma_f32_16x16x32_bf16 v[80:83], a[132:135], a[36:39], v[80:83]\n\t"
    "ds_read_b128 a[96:99], v4 offset:0\n\t"
    "v_add_f32 v41, v41, v88\n\t"
    "v_mfma_f32_16x16x32_bf16 v[80:83], a[136:139], a[40:43], v[80:83]\n\t"
    "v_exp_f32 v89, v89\n\t"
    "v_mfma_f32_16x16x32_bf16 v[80:83], a[140:143], a[44:47], v[80:83]\n\t"
    "v_add_f32 v41, v41, v89\n\t"
    "v_cvt_pk_bf16_f32 v76, v88, v89\n\t"
    "v_mfma_f32_16x16x32_bf16 v[84:87], a[144:147], a[32:35], v[56:59]\n\t"
    "v_exp_f32 v90, v90\n\t"
    "v_mfma_f32_16x16x32_bf16 v[84:87], a[148:151], a[36:39], v[84:87]\n\t"
    "ds_read_b128 a[100:103], v5 offset:0\n\t"
    "v_add_f32 v41, v41, v90\n\t"
    "v_mfma_f32_16x16x32_bf16 v[84:87], a[152:155], a[40:43], v[84:87]\n\t"
    "v_exp_f32 v91, v91\n\t"
    "v_mfma_f32_16x16x32_bf16 v[84:87], a[156:159], a[44:47], v[84:87]\n\t"
    "s_waitcnt lgkmcnt(2)\n\t"
    "v_add_f32 v41, v41, v91\n\t"
    "v_cvt_pk_bf16_f32 v77, v90, v91\n\t"
    "v_mfma_f32_16x16x32_bf16 a[224:227], a[192:195], v[72:75], a[224:227]\n\t"
    "v_exp_f32 v92, v92\n\t"
    "v_mfma_f32_16x16x32_bf16 a[228:231], a[196:199], v[72:75], a[228:231]\n\t"
    "ds_read_b128 a[104:107], v6 offset:0\n\t"
    "v_add_f32 v41, v41, v92\n\t"
    "v_mfma_f32_16x16x32_bf16 a[232:235], a[200:203], v[72:75], a[232:235]\n\t"
    "v_exp_f32 v93, v93\n\t"
    "v_mfma_f32_16x16x32_bf16 a[236:239], a[204:207], v[72:75], a[236:239]\n\t"
    "v_add_f32 v41, v41, v93\n\t"
    "v_cvt_pk_bf16_f32 v78, v92, v93\n\t"
    "v_mfma_f32_16x16x32_bf16 a[240:243], a[208:211], v[72:75], a[240:243]\n\t"
    "v_exp_f32 v94, v94\n\t"
    "v_mfma_f32_16x16x32_bf16 a[244:247], a[212:215], v[72:75], a[244:247]\n\t"
    "ds_read_b128 a[108:111], v7 offset:0\n\t"
    "v_add_f32 v41, v41, v94\n\t"
    "v_mfma_f32_16x16x32_bf16 a[248:251], a[216:219], v[72:75], a[248:251]\n\t"
    "v_exp_f32 v95, v95\n\t"
    "v_mfma_f32_16x16x32_bf16 a[252:255], a[220:223], v[72:75], a[252:255]\n\t"
    "v_add_f32 v41, v41, v95\n\t"
    "v_cvt_pk_bf16_f32 v79, v94, v95\n\t"
    "v_mfma_f32_16x16x32_bf16 v[88:91], a[128:131], a[48:51], v[60:63]\n\t"
    "v_exp_f32 v80, v80\n\t"
    "v_mfma_f32_16x16x32_bf16 v[88:91], a[132:135], a[52:55], v[88:91]\n\t"
    "ds_read_b128 a[112:115], v4 offset:4096\n\t"
    "v_add_f32 v42, v42, v80\n\t"
    "v_xor_b32 v8, 0x10000, v8\n\t"
    "v_mfma_f32_16x16x32_bf16 v[88:91], a[136:139], a[56:59], v[88:91]\n\t"
    "v_exp_f32 v81, v81\n\t"
    "v_mfma_f32_16x16x32_bf16 v[88:91], a[140:143], a[60:63], v[88:91]\n\t"
    "v_add_f32 v42, v42, v81\n\t"
    "v_cvt_pk_bf16_f32 v72, v80, v81\n\t"
    "v_mfma_f32_16x16x32_bf16 v[92:95], a[144:147], a[48:51], v[60:63]\n\t"
    "v_exp_f32 v82, v82\n\t"
    "v_mfma_f32_16x16x32_bf16 v[92:95], a[148:151], a[52:55], v[92:95]\n\t"
    "ds_read_b128 a[116:119], v5 offset:4096\n\t"
    "v_add_f32 v42, v42, v82\n\t"
    "v_xor_b32 v9, 0x10000, v9\n\t"
    "v_mfma_f32_16x16x32_bf16 v[92:95], a[152:155], a[56:59], v[92:95]\n\t"
    "v_exp_f32 v83, v83\n\t"
    "v_mfma_f32_16x16x32_bf16 v[92:95], a[156:159], a[60:63], v[92:95]\n\t"
    "v_add_f32 v42, v42, v83\n\t"
    "v_cvt_pk_bf16_f32 v73, v82, v83\n\t"
    "v_mfma_f32_16x16x32_bf16 v[96:99], a[192:195], v[76:79], v[96:99]\n\t"
    "v_exp_f32 v84, v84\n\t"
    "v_mfma_f32_16x16x32_bf16 v[100:103], a[196:199], v[76:79], v[100:103]\n\t"
    "ds_read_b128 a[120:123], v6 offset:4096\n\t"
    "v_add_f32 v42, v42, v84\n\t"
    "v_xor_b32 v10, 0x10000, v10\n\t"
    "v_mfma_f32_16x16x32_bf16 v[104:107], a[200:203], v[76:79], v[104:107]\n\t"
    "v_exp_f32 v85, v85\n\t"
    "v_mfma_f32_16x16x32_bf16 v[108:111], a[204:207], v[76:79], v[108:111]\n\t"
    "v_add_f32 v42, v42, v85\n\t"
    "v_cvt_pk_bf16_f32 v74, v84, v85\n\t"
    "v_mfma_f32_16x16x32_bf16 v[112:115], a[208:211], v[76:79], v[112:115]\n\t"
    "v_exp_f32 v86, v86\n\t"
    "v_mfma_f32_16x16x32_bf16 v[116:119], a[212:215], v[76:79], v[116:119]\n\t"
    "ds_read_b128 a[124:127], v7 offset:4096\n\t"
    "v_add_f32 v42, v42, v86\n\t"
    "v_xor_b32 v11, 0x10000, v11\n\t"
    "v_mfma_f32_16x16x32_bf16 v[120:123], a[216:219], v[76:79], v[120:123]\n\t"
    "v_exp_f32 v87, v87\n\t"
    "v_mfma_f32_16x16x32_bf16 v[124:127], a[220:223], v[76:79], v[124:127]\n\t"
    "v_add_f32 v42, v42, v87\n\t"
    "v_cvt_pk_bf16_f32 v75, v86, v87\n\t"
    "v_mfma_f32_16x16x32_bf16 v[80:83], a[128:131], a[64:67], v[64:67]\n\t"
    "v_exp_f32 v88, v88\n\t"
    "v_mfma_f32_16x16x32_bf16 v[80:83], a[132:135], a[68:71], v[80:83]\n\t"
    "ds_read_b64_tr_b16 a[160:161], v8 offset:8192\n\t"
    "v_add_f32 v43, v43, v88\n\t"
    "v_mfma_f32_16x16x32_bf16 v[80:83], a[136:139], a[72:75], v[80:83]\n\t"
    "v_exp_f32 v89, v89\n\t"
    "v_mfma_f32_16x16x32_bf16 v[80:83], a[140:143], a[76:79], v[80:83]\n\t"
    "v_add_f32 v43, v43, v89\n\t"
    "v_cvt_pk_bf16_f32 v76, v88, v89\n\t"
    "v_mfma_f32_16x16x32_bf16 v[84:87], a[144:147], a[64:67], v[64:67]\n\t"
    "v_exp_f32 v90, v90\n\t"
    "v_mfma_f32_16x16x32_bf16 v[84:87], a[148:151], a[68:71], v[84:87]\n\t"
    "ds_read_b64_tr_b16 a[162:163], v8 offset:12288\n\t"
    "v_add_f32 v43, v43, v90\n\t"
    "v_mfma_f32_16x16x32_bf16 v[84:87], a[152:155], a[72:75], v[84:87]\n\t"
    "v_exp_f32 v91, v91\n\t"
    "v_mfma_f32_16x16x32_bf16 v[84:87], a[156:159], a[76:79], v[84:87]\n\t"
    "v_add_f32 v43, v43, v91\n\t"
    "v_cvt_pk_bf16_f32 v77, v90, v91\n\t"
    "v_mfma_f32_16x16x32_bf16 v[128:131], a[192:195], v[72:75], v[128:131]\n\t"
    "v_exp_f32 v92, v92\n\t"
    "v_mfma_f32_16x16x32_bf16 v[132:135], a[196:199], v[72:75], v[132:135]\n\t"
    "ds_read_b64_tr_b16 a[164:165], v9 offset:8192\n\t"
    "v_add_f32 v43, v43, v92\n\t"
    "v_mfma_f32_16x16x32_bf16 v[136:139], a[200:203], v[72:75], v[136:139]\n\t"
    "v_exp_f32 v93, v93\n\t"
    "v_mfma_f32_16x16x32_bf16 v[140:143], a[204:207], v[72:75], v[140:143]\n\t"
    "v_add_f32 v43, v43, v93\n\t"
    "v_cvt_pk_bf16_f32 v78, v92, v93\n\t"
    "v_mfma_f32_16x16x32_bf16 v[144:147], a[208:211], v[72:75], v[144:147]\n\t"
    "v_exp_f32 v94, v94\n\t"
    "v_mfma_f32_16x16x32_bf16 v[148:151], a[212:215], v[72:75], v[148:151]\n\t"
    "ds_read_b64_tr_b16 a[166:167], v9 offset:12288\n\t"
    "v_add_f32 v43, v43, v94\n\t"
    "v_mfma_f32_16x16x32_bf16 v[152:155], a[216:219], v[72:75], v[152:155]\n\t"
    "v_exp_f32 v95, v95\n\t"
    "v_mfma_f32_16x16x32_bf16 v[156:159], a[220:223], v[72:75], v[156:159]\n\t"
    "v_add_f32 v43, v43, v95\n\t"
    "v_cvt_pk_bf16_f32 v79, v94, v95\n\t"
    "v_mfma_f32_16x16x32_bf16 v[88:91], a[128:131], a[80:83], v[68:71]\n\t"
    "v_exp_f32 v80, v80\n\t"
    "v_mfma_f32_16x16x32_bf16 v[88:91], a[132:135], a[84:87], v[88:91]\n\t"
    "ds_read_b64_tr_b16 a[168:169], v10 offset:8192\n\t"
    "v_add_f32 v44, v44, v80\n\t"
    "v_mfma_f32_16x16x32_bf16 v[88:91], a[136:139], a[88:91], v[88:91]\n\t"
    "v_exp_f32 v81, v81\n\t"
    "v_mfma_f32_16x16x32_bf16 v[88:91], a[140:143], a[92:95], v[88:91]\n\t"
    "v_add_f32 v44, v44, v81\n\t"
    "v_cvt_pk_bf16_f32 v72, v80, v81\n\t"
    "v_mfma_f32_16x16x32_bf16 v[92:95], a[144:147], a[80:83], v[68:71]\n\t"
    "v_exp_f32 v82, v82\n\t"
    "v_mfma_f32_16x16x32_bf16 v[92:95], a[148:151], a[84:87], v[92:95]\n\t"
    "ds_read_b64_tr_b16 a[170:171], v10 offset:12288\n\t"
    "v_add_f32 v44, v44, v82\n\t"
    "v_mfma_f32_16x16x32_bf16 v[92:95], a[152:155], a[88:91], v[92:95]\n\t"
    "v_exp_f32 v83, v83\n\t"
    "v_mfma_f32_16x16x32_bf16 v[92:95], a[156:159], a[92:95], v[92:95]\n\t"
    "v_add_f32 v44, v44, v83\n\t"
    "v_cvt_pk_bf16_f32 v73, v82, v83\n\t"
    "v_mfma_f32_16x16x32_bf16 v[160:163], a[192:195], v[76:79], v[160:163]\n\t"
    "v_exp_f32 v84, v84\n\t"
    "v_mfma_f32_16x16x32_bf16 v[164:167], a[196:199], v[76:79], v[164:167]\n\t"
    "ds_read_b64_tr_b16 a[172:173], v11 offset:8192\n\t"
    "v_add_f32 v44, v44, v84\n\t"
    "v_mfma_f32_16x16x32_bf16 v[168:171], a[200:203], v[76:79], v[168:171]\n\t"
    "v_exp_f32 v85, v85\n\t"
    "v_mfma_f32_16x16x32_bf16 v[172:175], a[204:207], v[76:79], v[172:175]\n\t"
    "v_add_f32 v44, v44, v85\n\t"
    "v_cvt_pk_bf16_f32 v74, v84, v85\n\t"
    "v_mfma_f32_16x16x32_bf16 v[176:179], a[208:211], v[76:79], v[176:179]\n\t"
    "v_exp_f32 v86, v86\n\t"
    "v_mfma_f32_16x16x32_bf16 v[180:183], a[212:215], v[76:79], v[180:183]\n\t"
    "ds_read_b64_tr_b16 a[174:175], v11 offset:12288\n\t"
    "v_add_f32 v44, v44, v86\n\t"
    "v_mfma_f32_16x16x32_bf16 v[184:187], a[216:219], v[76:79], v[184:187]\n\t"
    "v_exp_f32 v87, v87\n\t"
    "v_mfma_f32_16x16x32_bf16 v[188:191], a[220:223], v[76:79], v[188:191]\n\t"
    "s_waitcnt lgkmcnt(8)\n\t"
    "v_add_f32 v44, v44, v87\n\t"
    "v_cvt_pk_bf16_f32 v75, v86, v87\n\t"
    "v_mfma_f32_16x16x32_bf16 v[80:83], a[96:99], a[0:3], v[48:51]\n\t"
    "v_exp_f32 v88, v88\n\t"
    "v_mfma_f32_16x16x32_bf16 v[80:83], a[100:103], a[4:7], v[80:83]\n\t"
    "ds_read_b64_tr_b16 a[176:177], v8 offset:9216\n\t"
    "v_add_f32 v45, v45, v88\n\t"
    "v_mfma_f32_16x16x32_bf16 v[80:83], a[104:107], a[8:11], v[80:83]\n\t"
    "v_exp_f32 v89, v89\n\t"
    "v_mfma_f32_16x16x32_bf16 v[80:83], a[108:111], a[12:15], v[80:83]\n\t"
    "v_add_f32 v45, v45, v89\n\t"
    "v_cvt_pk_bf16_f32 v76, v88, v89\n\t"
    "v_mfma_f32_16x16x32_bf16 v[84:87], a[112:115], a[0:3], v[48:51]\n\t"
    "v_exp_f32 v90, v90\n\t"
    "v_mfma_f32_16x16x32_bf16 v[84:87], a[116:119], a[4:7], v[84:87]\n\t"
    "ds_read_b64_tr_b16 a[178:179], v8 offset:13312\n\t"
    "v_add_f32 v45, v45, v90\n\t"
    "v_mfma_f32_16x16x32_bf16 v[84:87], a[120:123], a[8:11], v[84:87]\n\t"
    "v_exp_f32 v91, v91\n\t"
    "v_mfma_f32_16x16x32_bf16 v[84:87], a[124:127], a[12:15], v[84:87]\n\t"
    "v_add_f32 v45, v45, v91\n\t"
    "v_cvt_pk_bf16_f32 v77, v90, v91\n\t"
    "v_mfma_f32_16x16x32_bf16 v[192:195], a[192:195], v[72:75], v[192:195]\n\t"
    "v_exp_f32 v92, v92\n\t"
    "v_mfma_f32_16x16x32_bf16 v[196:199], a[196:199], v[72:75], v[196:199]\n\t"
    "ds_read_b64_tr_b16 a[180:181], v9 offset:9216\n\t"
    "v_add_f32 v45, v45, v92\n\t"
    "v_mfma_f32_16x16x32_bf16 v[200:203], a[200:203], v[72:75], v[200:203]\n\t"
    "v_exp_f32 v93, v93\n\t"
    "v_mfma_f32_16x16x32_bf16 v[204:207], a[204:207], v[72:75], v[204:207]\n\t"
    "v_add_f32 v45, v45, v93\n\t"
    "v_cvt_pk_bf16_f32 v78, v92, v93\n\t"
    "v_mfma_f32_16x16x32_bf16 v[208:211], a[208:211], v[72:75], v[208:211]\n\t"
    "v_exp_f32 v94, v94\n\t"
    "v_mfma_f32_16x16x32_bf16 v[212:215], a[212:215], v[72:75], v[212:215]\n\t"
    "ds_read_b64_tr_b16 a[182:183], v9 offset:13312\n\t"
    "v_add_f32 v45, v45, v94\n\t"
    "v_mfma_f32_16x16x32_bf16 v[216:219], a[216:219], v[72:75], v[216:219]\n\t"
    "v_exp_f32 v95, v95\n\t"
    "v_mfma_f32_16x16x32_bf16 v[220:223], a[220:223], v[72:75], v[220:223]\n\t"
    "v_add_f32 v45, v45, v95\n\t"
    "v_cvt_pk_bf16_f32 v79, v94, v95\n\t"
    "v_mfma_f32_16x16x32_bf16 v[88:91], a[96:99], a[16:19], v[52:55]\n\t"
    "v_exp_f32 v80, v80\n\t"
    "v_mfma_f32_16x16x32_bf16 v[88:91], a[100:103], a[20:23], v[88:91]\n\t"
    "ds_read_b64_tr_b16 a[184:185], v10 offset:9216\n\t"
    "v_add_f32 v40, v40, v80\n\t"
    "v_mfma_f32_16x16x32_bf16 v[88:91], a[104:107], a[24:27], v[88:91]\n\t"
    "v_exp_f32 v81, v81\n\t"
    "v_mfma_f32_16x16x32_bf16 v[88:91], a[108:111], a[28:31], v[88:91]\n\t"
    "v_add_f32 v40, v40, v81\n\t"
    "v_cvt_pk_bf16_f32 v72, v80, v81\n\t"
    "v_mfma_f32_16x16x32_bf16 v[92:95], a[112:115], a[16:19], v[52:55]\n\t"
    "v_exp_f32 v82, v82\n\t"
    "v_mfma_f32_16x16x32_bf16 v[92:95], a[116:119], a[20:23], v[92:95]\n\t"
    "ds_read_b64_tr_b16 a[186:187], v10 offset:13312\n\t"
    "v_add_f32 v40, v40, v82\n\t"
    "v_mfma_f32_16x16x32_bf16 v[92:95], a[120:123], a[24:27], v[92:95]\n\t"
    "v_exp_f32 v83, v83\n\t"
    "v_mfma_f32_16x16x32_bf16 v[92:95], a[124:127], a[28:31], v[92:95]\n\t"
    "v_add_f32 v40, v40, v83\n\t"
    "v_cvt_pk_bf16_f32 v73, v82, v83\n\t"
    "v_mfma_f32_16x16x32_bf16 v[224:227], a[192:195], v[76:79], v[224:227]\n\t"
    "v_exp_f32 v84, v84\n\t"
    "v_mfma_f32_16x16x32_bf16 v[228:231], a[196:199], v[76:79], v[228:231]\n\t"
    "ds_read_b64_tr_b16 a[188:189], v11 offset:9216\n\t"
    "v_add_f32 v40, v40, v84\n\t"
    "v_mfma_f32_16x16x32_bf16 v[232:235], a[200:203], v[76:79], v[232:235]\n\t"
    "v_exp_f32 v85, v85\n\t"
    "v_mfma_f32_16x16x32_bf16 v[236:239], a[204:207], v[76:79], v[236:239]\n\t"
    "v_add_f32 v40, v40, v85\n\t"
    "v_cvt_pk_bf16_f32 v74, v84, v85\n\t"
    "v_mfma_f32_16x16x32_bf16 v[240:243], a[208:211], v[76:79], v[240:243]\n\t"
    "v_exp_f32 v86, v86\n\t"
    "v_mfma_f32_16x16x32_bf16 v[244:247], a[212:215], v[76:79], v[244:247]\n\t"
    "ds_read_b64_tr_b16 a[190:191], v11 offset:13312\n\t"
    "v_add_f32 v40, v40, v86\n\t"
    "v_mfma_f32_16x16x32_bf16 v[248:251], a[216:219], v[76:79], v[248:251]\n\t"
    "v_exp_f32 v87, v87\n\t"
    "v_mfma_f32_16x16x32_bf16 v[252:255], a[220:223], v[76:79], v[252:255]\n\t"
    "s_barrier\n\t"
    "v_add_f32 v40, v40, v87\n\t"
    "v_cvt_pk_bf16_f32 v75, v86, v87\n\t"
    "v_mfma_f32_16x16x32_bf16 v[80:83], a[96:99], a[32:35], v[56:59]\n\t"
    "v_exp_f32 v88, v88\n\t"
    "v_mfma_f32_16x16x32_bf16 v[80:83], a[100:103], a[36:39], v[80:83]\n\t"
    "ds_read_b128 a[128:131], v4 offset:16384\n\t"
    "v_add_f32 v41, v41, v88\n\t"
    "v_mfma_f32_16x16x32_bf16 v[80:83], a[104:107], a[40:43], v[80:83]\n\t"
    "v_exp_f32 v89, v89\n\t"
    "v_mfma_f32_16x16x32_bf16 v[80:83], a[108:111], a[44:47], v[80:83]\n\t"
    "v_add_f32 v41, v41, v89\n\t"
    "v_cvt_pk_bf16_f32 v76, v88, v89\n\t"
    "v_mfma_f32_16x16x32_bf16 v[84:87], a[112:115], a[32:35], v[56:59]\n\t"
    "v_exp_f32 v90, v90\n\t"
    "v_mfma_f32_16x16x32_bf16 v[84:87], a[116:119], a[36:39], v[84:87]\n\t"
    "ds_read_b128 a[132:135], v5 offset:16384\n\t"
    "v_add_f32 v41, v41, v90\n\t"
    "v_mfma_f32_16x16x32_bf16 v[84:87], a[120:123], a[40:43], v[84:87]\n\t"
    "v_exp_f32 v91, v91\n\t"
    "v_mfma_f32_16x16x32_bf16 v[84:87], a[124:127], a[44:47], v[84:87]\n\t"
    "s_waitcnt lgkmcnt(2)\n\t"
    "v_add_f32 v41, v41, v91\n\t"
    "v_cvt_pk_bf16_f32 v77, v90, v91\n\t"
    "v_mfma_f32_16x16x32_bf16 a[224:227], a[160:163], v[72:75], a[224:227]\n\t"
    "v_exp_f32 v92, v92\n\t"
    "v_mfma_f32_16x16x32_bf16 a[228:231], a[164:167], v[72:75], a[228:231]\n\t"
    "ds_read_b128 a[136:139], v6 offset:16384\n\t"
    "v_add_f32 v41, v41, v92\n\t"
    "v_mfma_f32_16x16x32_bf16 a[232:235], a[168:171], v[72:75], a[232:235]\n\t"
    "v_exp_f32 v93, v93\n\t"
    "v_mfma_f32_16x16x32_bf16 a[236:239], a[172:175], v[72:75], a[236:239]\n\t"
    "v_add_f32 v41, v41, v93\n\t"
    "v_cvt_pk_bf16_f32 v78, v92, v93\n\t"
    "v_mfma_f32_16x16x32_bf16 a[240:243], a[176:179], v[72:75], a[240:243]\n\t"
    "v_exp_f32 v94, v94\n\t"
    "v_mfma_f32_16x16x32_bf16 a[244:247], a[180:183], v[72:75], a[244:247]\n\t"
    "ds_read_b128 a[140:143], v7 offset:16384\n\t"
    "v_add_f32 v41, v41, v94\n\t"
    "v_mfma_f32_16x16x32_bf16 a[248:251], a[184:187], v[72:75], a[248:251]\n\t"
    "v_exp_f32 v95, v95\n\t"
    "v_mfma_f32_16x16x32_bf16 a[252:255], a[188:191], v[72:75], a[252:255]\n\t"
    "v_add_f32 v41, v41, v95\n\t"
    "v_cvt_pk_bf16_f32 v79, v94, v95\n\t"
    "v_mfma_f32_16x16x32_bf16 v[88:91], a[96:99], a[48:51], v[60:63]\n\t"
    "v_exp_f32 v80, v80\n\t"
    "v_mfma_f32_16x16x32_bf16 v[88:91], a[100:103], a[52:55], v[88:91]\n\t"
    "ds_read_b128 a[144:147], v4 offset:20480\n\t"
    "v_add_f32 v42, v42, v80\n\t"
    "v_mfma_f32_16x16x32_bf16 v[88:91], a[104:107], a[56:59], v[88:91]\n\t"
    "v_exp_f32 v81, v81\n\t"
    "v_mfma_f32_16x16x32_bf16 v[88:91], a[108:111], a[60:63], v[88:91]\n\t"
    "v_add_f32 v42, v42, v81\n\t"
    "v_cvt_pk_bf16_f32 v72, v80, v81\n\t"
    "v_mfma_f32_16x16x32_bf16 v[92:95], a[112:115], a[48:51], v[60:63]\n\t"
    "v_exp_f32 v82, v82\n\t"
    "v_mfma_f32_16x16x32_bf16 v[92:95], a[116:119], a[52:55], v[92:95]\n\t"
    "ds_read_b128 a[148:151], v5 offset:20480\n\t"
    "v_add_f32 v42, v42, v82\n\t"
    "v_mfma_f32_16x16x32_bf16 v[92:95], a[120:123], a[56:59], v[92:95]\n\t"
    "v_exp_f32 v83, v83\n\t"
    "v_mfma_f32_16x16x32_bf16 v[92:95], a[124:127], a[60:63], v[92:95]\n\t"
    "v_add_f32 v42, v42, v83\n\t"
    "v_cvt_pk_bf16_f32 v73, v82, v83\n\t"
    "v_mfma_f32_16x16x32_bf16 v[96:99], a[160:163], v[76:79], v[96:99]\n\t"
    "v_exp_f32 v84, v84\n\t"
    "v_mfma_f32_16x16x32_bf16 v[100:103], a[164:167], v[76:79], v[100:103]\n\t"
    "ds_read_b128 a[152:155], v6 offset:20480\n\t"
    "v_add_f32 v42, v42, v84\n\t"
    "v_mfma_f32_16x16x32_bf16 v[104:107], a[168:171], v[76:79], v[104:107]\n\t"
    "v_exp_f32 v85, v85\n\t"
    "v_mfma_f32_16x16x32_bf16 v[108:111], a[172:175], v[76:79], v[108:111]\n\t"
    "v_add_f32 v42, v42, v85\n\t"
    "v_cvt_pk_bf16_f32 v74, v84, v85\n\t"
    "v_mfma_f32_16x16x32_bf16 v[112:115], a[176:179], v[76:79], v[112:115]\n\t"
    "v_exp_f32 v86, v86\n\t"
    "v_mfma_f32_16x16x32_bf16 v[116:119], a[180:183], v[76:79], v[116:119]\n\t"
    "ds_read_b128 a[156:159], v7 offset:20480\n\t"
    "v_add_f32 v42, v42, v86\n\t"
    "v_mfma_f32_16x16x32_bf16 v[120:123], a[184:187], v[76:79], v[120:123]\n\t"
    "v_exp_f32 v87, v87\n\t"
    "v_mfma_f32_16x16x32_bf16 v[124:127], a[188:191], v[76:79], v[124:127]\n\t"
    "v_add_f32 v42, v42, v87\n\t"
    "v_cvt_pk_bf16_f32 v75, v86, v87\n\t"
    "v_mfma_f32_16x16x32_bf16 v[80:83], a[96:99], a[64:67], v[64:67]\n\t"
    "v_exp_f32 v88, v88\n\t"
    "v_mfma_f32_16x16x32_bf16 v[80:83], a[100:103], a[68:71], v[80:83]\n\t"
    "ds_read_b64_tr_b16 a[192:193], v8 offset:24576\n\t"
    "v_add_f32 v43, v43, v88\n\t"
    "v_mfma_f32_16x16x32_bf16 v[80:83], a[104:107], a[72:75], v[80:83]\n\t"
    "v_exp_f32 v89, v89\n\t"
    "v_mfma_f32_16x16x32_bf16 v[80:83], a[108:111], a[76:79], v[80:83]\n\t"
    "v_add_f32 v43, v43, v89\n\t"
    "v_cvt_pk_bf16_f32 v76, v88, v89\n\t"
    "v_mfma_f32_16x16x32_bf16 v[84:87], a[112:115], a[64:67], v[64:67]\n\t"
    "v_exp_f32 v90, v90\n\t"
    "v_mfma_f32_16x16x32_bf16 v[84:87], a[116:119], a[68:71], v[84:87]\n\t"
    "ds_read_b64_tr_b16 a[194:195], v8 offset:28672\n\t"
    "v_add_f32 v43, v43, v90\n\t"
    "v_mfma_f32_16x16x32_bf16 v[84:87], a[120:123], a[72:75], v[84:87]\n\t"
    "v_exp_f32 v91, v91\n\t"
    "v_mfma_f32_16x16x32_bf16 v[84:87], a[124:127], a[76:79], v[84:87]\n\t"
    "v_add_f32 v43, v43, v91\n\t"
    "v_cvt_pk_bf16_f32 v77, v90, v91\n\t"
    "v_mfma_f32_16x16x32_bf16 v[128:131], a[160:163], v[72:75], v[128:131]\n\t"
    "v_exp_f32 v92, v92\n\t"
    "v_mfma_f32_16x16x32_bf16 v[132:135], a[164:167], v[72:75], v[132:135]\n\t"
    "ds_read_b64_tr_b16 a[196:197], v9 offset:24576\n\t"
    "v_add_f32 v43, v43, v92\n\t"
    "v_mfma_f32_16x16x32_bf16 v[136:139], a[168:171], v[72:75], v[136:139]\n\t"
    "v_exp_f32 v93, v93\n\t"
    "v_mfma_f32_16x16x32_bf16 v[140:143], a[172:175], v[72:75], v[140:143]\n\t"
    "v_add_f32 v43, v43, v93\n\t"
    "v_cvt_pk_bf16_f32 v78, v92, v93\n\t"
    "v_mfma_f32_16x16x32_bf16 v[144:147], a[176:179], v[72:75], v[144:147]\n\t"
    "v_exp_f32 v94, v94\n\t"
    "v_mfma_f32_16x16x32_bf16 v[148:151], a[180:183], v[72:75], v[148:151]\n\t"
    "ds_read_b64_tr_b16 a[198:199], v9 offset:28672\n\t"
    "v_add_f32 v43, v43, v94\n\t"
    "v_mfma_f32_16x16x32_bf16 v[152:155], a[184:187], v[72:75], v[152:155]\n\t"
    "v_exp_f32 v95, v95\n\t"
    "v_mfma_f32_16x16x32_bf16 v[156:159], a[188:191], v[72:75], v[156:159]\n\t"
    "v_add_f32 v43, v43, v95\n\t"
    "v_cvt_pk_bf16_f32 v79, v94, v95\n\t"
    "v_mfma_f32_16x16x32_bf16 v[88:91], a[96:99], a[80:83], v[68:71]\n\t"
    "v_exp_f32 v80, v80\n\t"
    "v_mfma_f32_16x16x32_bf16 v[88:91], a[100:103], a[84:87], v[88:91]\n\t"
    "ds_read_b64_tr_b16 a[200:201], v10 offset:24576\n\t"
    "v_add_f32 v44, v44, v80\n\t"
    "v_mfma_f32_16x16x32_bf16 v[88:91], a[104:107], a[88:91], v[88:91]\n\t"
    "v_exp_f32 v81, v81\n\t"
    "v_mfma_f32_16x16x32_bf16 v[88:91], a[108:111], a[92:95], v[88:91]\n\t"
    "v_add_f32 v44, v44, v81\n\t"
    "v_cvt_pk_bf16_f32 v72, v80, v81\n\t"
    "v_mfma_f32_16x16x32_bf16 v[92:95], a[112:115], a[80:83], v[68:71]\n\t"
    "v_exp_f32 v82, v82\n\t"
    "v_mfma_f32_16x16x32_bf16 v[92:95], a[116:119], a[84:87], v[92:95]\n\t"
    "ds_read_b64_tr_b16 a[202:203], v10 offset:28672\n\t"
    "v_add_f32 v44, v44, v82\n\t"
    "v_mfma_f32_16x16x32_bf16 v[92:95], a[120:123], a[88:91], v[92:95]\n\t"
    "v_exp_f32 v83, v83\n\t"
    "v_mfma_f32_16x16x32_bf16 v[92:95], a[124:127], a[92:95], v[92:95]\n\t"
    "v_add_f32 v44, v44, v83\n\t"
    "v_cvt_pk_bf16_f32 v73, v82, v83\n\t"
    "v_mfma_f32_16x16x32_bf16 v[160:163], a[160:163], v[76:79], v[160:163]\n\t"
    "v_exp_f32 v84, v84\n\t"
    "v_mfma_f32_16x16x32_bf16 v[164:167], a[164:167], v[76:79], v[164:167]\n\t"
    "ds_read_b64_tr_b16 a[204:205], v11 offset:24576\n\t"
    "v_add_f32 v44, v44, v84\n\t"
    "v_mfma_f32_16x16x32_bf16 v[168:171], a[168:171], v[76:79], v[168:171]\n\t"
    "v_exp_f32 v85, v85\n\t"
    "v_mfma_f32_16x16x32_bf16 v[172:175], a[172:175], v[76:79], v[172:175]\n\t"
    "v_add_f32 v44, v44, v85\n\t"
    "v_cvt_pk_bf16_f32 v74, v84, v85\n\t"
    "v_mfma_f32_16x16x32_bf16 v[176:179], a[176:179], v[76:79], v[176:179]\n\t"
    "v_exp_f32 v86, v86\n\t"
    "v_mfma_f32_16x16x32_bf16 v[180:183], a[180:183], v[76:79], v[180:183]\n\t"
    "ds_read_b64_tr_b16 a[206:207], v11 offset:28672\n\t"
    "v_add_f32 v44, v44, v86\n\t"
    "v_mfma_f32_16x16x32_bf16 v[184:187], a[184:187], v[76:79], v[184:187]\n\t"
    "v_exp_f32 v87, v87\n\t"
    "v_mfma_f32_16x16x32_bf16 v[188:191], a[188:191], v[76:79], v[188:191]\n\t"
    "s_waitcnt lgkmcnt(8)\n\t"
    "v_add_f32 v44, v44, v87\n\t"
    "v_cvt_pk_bf16_f32 v75, v86, v87\n\t"
    "v_mfma_f32_16x16x32_bf16 v[80:83], a[128:131], a[0:3], v[48:51]\n\t"
    "v_exp_f32 v88, v88\n\t"
    "v_mfma_f32_16x16x32_bf16 v[80:83], a[132:135], a[4:7], v[80:83]\n\t"
    "ds_read_b64_tr_b16 a[208:209], v8 offset:25600\n\t"
    "v_add_f32 v45, v45, v88\n\t"
    "v_mfma_f32_16x16x32_bf16 v[80:83], a[136:139], a[8:11], v[80:83]\n\t"
    "v_exp_f32 v89, v89\n\t"
    "v_mfma_f32_16x16x32_bf16 v[80:83], a[140:143], a[12:15], v[80:83]\n\t"
    "v_add_f32 v45, v45, v89\n\t"
    "v_cvt_pk_bf16_f32 v76, v88, v89\n\t"
    "v_mfma_f32_16x16x32_bf16 v[84:87], a[144:147], a[0:3], v[48:51]\n\t"
    "v_exp_f32 v90, v90\n\t"
    "v_mfma_f32_16x16x32_bf16 v[84:87], a[148:151], a[4:7], v[84:87]\n\t"
    "ds_read_b64_tr_b16 a[210:211], v8 offset:29696\n\t"
    "v_add_f32 v45, v45, v90\n\t"
    "v_mfma_f32_16x16x32_bf16 v[84:87], a[152:155], a[8:11], v[84:87]\n\t"
    "v_exp_f32 v91, v91\n\t"
    "v_mfma_f32_16x16x32_bf16 v[84:87], a[156:159], a[12:15], v[84:87]\n\t"
    "v_add_f32 v45, v45, v91\n\t"
    "v_cvt_pk_bf16_f32 v77, v90, v91\n\t"
    "v_mfma_f32_16x16x32_bf16 v[192:195], a[160:163], v[72:75], v[192:195]\n\t"
    "v_exp_f32 v92, v92\n\t"
    "v_mfma_f32_16x16x32_bf16 v[196:199], a[164:167], v[72:75], v[196:199]\n\t"
    "ds_read_b64_tr_b16 a[212:213], v9 offset:25600\n\t"
    "v_add_f32 v45, v45, v92\n\t"
    "v_mfma_f32_16x16x32_bf16 v[200:203], a[168:171], v[72:75], v[200:203]\n\t"
    "v_exp_f32 v93, v93\n\t"
    "v_mfma_f32_16x16x32_bf16 v[204:207], a[172:175], v[72:75], v[204:207]\n\t"
    "v_add_f32 v45, v45, v93\n\t"
    "v_cvt_pk_bf16_f32 v78, v92, v93\n\t"
    "v_mfma_f32_16x16x32_bf16 v[208:211], a[176:179], v[72:75], v[208:211]\n\t"
    "v_exp_f32 v94, v94\n\t"
    "v_mfma_f32_16x16x32_bf16 v[212:215], a[180:183], v[72:75], v[212:215]\n\t"
    "ds_read_b64_tr_b16 a[214:215], v9 offset:29696\n\t"
    "v_add_f32 v45, v45, v94\n\t"
    "v_mfma_f32_16x16x32_bf16 v[216:219], a[184:187], v[72:75], v[216:219]\n\t"
    "v_exp_f32 v95, v95\n\t"
    "v_mfma_f32_16x16x32_bf16 v[220:223], a[188:191], v[72:75], v[220:223]\n\t"
    "v_add_f32 v45, v45, v95\n\t"
    "v_cvt_pk_bf16_f32 v79, v94, v95\n\t"
    "v_mfma_f32_16x16x32_bf16 v[88:91], a[128:131], a[16:19], v[52:55]\n\t"
    "v_exp_f32 v80, v80\n\t"
    "v_mfma_f32_16x16x32_bf16 v[88:91], a[132:135], a[20:23], v[88:91]\n\t"
    "ds_read_b64_tr_b16 a[216:217], v10 offset:25600\n\t"
    "v_add_f32 v40, v40, v80\n\t"
    "v_mfma_f32_16x16x32_bf16 v[88:91], a[136:139], a[24:27], v[88:91]\n\t"
    "v_exp_f32 v81, v81\n\t"
    "v_mfma_f32_16x16x32_bf16 v[88:91], a[140:143], a[28:31], v[88:91]\n\t"
    "v_add_f32 v40, v40, v81\n\t"
    "v_cvt_pk_bf16_f32 v72, v80, v81\n\t"
    "v_mfma_f32_16x16x32_bf16 v[92:95], a[144:147], a[16:19], v[52:55]\n\t"
    "v_exp_f32 v82, v82\n\t"
    "v_mfma_f32_16x16x32_bf16 v[92:95], a[148:151], a[20:23], v[92:95]\n\t"
    "ds_read_b64_tr_b16 a[218:219], v10 offset:29696\n\t"
    "v_add_f32 v40, v40, v82\n\t"
    "v_mfma_f32_16x16x32_bf16 v[92:95], a[152:155], a[24:27], v[92:95]\n\t"
    "v_exp_f32 v83, v83\n\t"
    "v_mfma_f32_16x16x32_bf16 v[92:95], a[156:159], a[28:31], v[92:95]\n\t"
    "v_add_f32 v40, v40, v83\n\t"
    "v_cvt_pk_bf16_f32 v73, v82, v83\n\t"
    "v_mfma_f32_16x16x32_bf16 v[224:227], a[160:163], v[76:79], v[224:227]\n\t"
    "v_exp_f32 v84, v84\n\t"
    "v_mfma_f32_16x16x32_bf16 v[228:231], a[164:167], v[76:79], v[228:231]\n\t"
    "ds_read_b64_tr_b16 a[220:221], v11 offset:25600\n\t"
    "v_add_f32 v40, v40, v84\n\t"
    "v_mfma_f32_16x16x32_bf16 v[232:235], a[168:171], v[76:79], v[232:235]\n\t"
    "v_exp_f32 v85, v85\n\t"
    "v_mfma_f32_16x16x32_bf16 v[236:239], a[172:175], v[76:79], v[236:239]\n\t"
    "v_add_f32 v40, v40, v85\n\t"
    "v_cvt_pk_bf16_f32 v74, v84, v85\n\t"
    "v_mfma_f32_16x16x32_bf16 v[240:243], a[176:179], v[76:79], v[240:243]\n\t"
    "v_exp_f32 v86, v86\n\t"
    "v_mfma_f32_16x16x32_bf16 v[244:247], a[180:183], v[76:79], v[244:247]\n\t"
    "ds_read_b64_tr_b16 a[222:223], v11 offset:29696\n\t"
    "v_add_f32 v40, v40, v86\n\t"
    "v_mfma_f32_16x16x32_bf16 v[248:251], a[184:187], v[76:79], v[248:251]\n\t"
    "v_exp_f32 v87, v87\n\t"
    "v_mfma_f32_16x16x32_bf16 v[252:255], a[188:191], v[76:79], v[252:255]\n\t"
    "s_barrier\n\t"
    "v_add_f32 v40, v40, v87\n\t"
    "v_cvt_pk_bf16_f32 v75, v86, v87\n\t"
    "v_mfma_f32_16x16x32_bf16 v[80:83], a[128:131], a[32:35], v[56:59]\n\t"
    "v_exp_f32 v88, v88\n\t"
    "v_mfma_f32_16x16x32_bf16 v[80:83], a[132:135], a[36:39], v[80:83]\n\t"
    "ds_read_b128 a[96:99], v4 offset:32768\n\t"
    "v_add_f32 v41, v41, v88\n\t"
    "v_mfma_f32_16x16x32_bf16 v[80:83], a[136:139], a[40:43], v[80:83]\n\t"
    "v_exp_f32 v89, v89\n\t"
    "v_mfma_f32_16x16x32_bf16 v[80:83], a[140:143], a[44:47], v[80:83]\n\t"
    "v_add_f32 v41, v41, v89\n\t"
    "v_cvt_pk_bf16_f32 v76, v88, v89\n\t"
    "v_mfma_f32_16x16x32_bf16 v[84:87], a[144:147], a[32:35], v[56:59]\n\t"
    "v_exp_f32 v90, v90\n\t"
    "v_mfma_f32_16x16x32_bf16 v[84:87], a[148:151], a[36:39], v[84:87]\n\t"
    "ds_read_b128 a[100:103], v5 offset:32768\n\t"
    "v_add_f32 v41, v41, v90\n\t"
    "v_mfma_f32_16x16x32_bf16 v[84:87], a[152:155], a[40:43], v[84:87]\n\t"
    "v_exp_f32 v91, v91\n\t"
    "v_mfma_f32_16x16x32_bf16 v[84:87], a[156:159], a[44:47], v[84:87]\n\t"
    "s_waitcnt lgkmcnt(2)\n\t"
    "v_add_f32 v41, v41, v91\n\t"
    "v_cvt_pk_bf16_f32 v77, v90, v91\n\t"
    "v_mfma_f32_16x16x32_bf16 a[224:227], a[192:195], v[72:75], a[224:227]\n\t"
    "v_exp_f32 v92, v92\n\t"
    "v_mfma_f32_16x16x32_bf16 a[228:231], a[196:199], v[72:75], a[228:231]\n\t"
    "ds_read_b128 a[104:107], v6 offset:32768\n\t"
    "v_add_f32 v41, v41, v92\n\t"
    "v_mfma_f32_16x16x32_bf16 a[232:235], a[200:203], v[72:75], a[232:235]\n\t"
    "v_exp_f32 v93, v93\n\t"
    "v_mfma_f32_16x16x32_bf16 a[236:239], a[204:207], v[72:75], a[236:239]\n\t"
    "v_add_f32 v41, v41, v93\n\t"
    "v_cvt_pk_bf16_f32 v78, v92, v93\n\t"
    "v_mfma_f32_16x16x32_bf16 a[240:243], a[208:211], v[72:75], a[240:243]\n\t"
    "v_exp_f32 v94, v94\n\t"
    "v_mfma_f32_16x16x32_bf16 a[244:247], a[212:215], v[72:75], a[244:247]\n\t"
    "ds_read_b128 a[108:111], v7 offset:32768\n\t"
    "v_add_f32 v41, v41, v94\n\t"
    "v_mfma_f32_16x16x32_bf16 a[248:251], a[216:219], v[72:75], a[248:251]\n\t"
    "v_exp_f32 v95, v95\n\t"
    "v_mfma_f32_16x16x32_bf16 a[252:255], a[220:223], v[72:75], a[252:255]\n\t"
    "v_add_f32 v41, v41, v95\n\t"
    "v_cvt_pk_bf16_f32 v79, v94, v95\n\t"
    "v_mfma_f32_16x16x32_bf16 v[88:91], a[128:131], a[48:51], v[60:63]\n\t"
    "v_exp_f32 v80, v80\n\t"
    "v_mfma_f32_16x16x32_bf16 v[88:91], a[132:135], a[52:55], v[88:91]\n\t"
    "ds_read_b128 a[112:115], v4 offset:36864\n\t"
    "v_add_f32 v42, v42, v80\n\t"
    "v_mfma_f32_16x16x32_bf16 v[88:91], a[136:139], a[56:59], v[88:91]\n\t"
    "v_exp_f32 v81, v81\n\t"
    "v_mfma_f32_16x16x32_bf16 v[88:91], a[140:143], a[60:63], v[88:91]\n\t"
    "v_add_f32 v42, v42, v81\n\t"
    "v_cvt_pk_bf16_f32 v72, v80, v81\n\t"
    "v_mfma_f32_16x16x32_bf16 v[92:95], a[144:147], a[48:51], v[60:63]\n\t"
    "v_exp_f32 v82, v82\n\t"
    "v_mfma_f32_16x16x32_bf16 v[92:95], a[148:151], a[52:55], v[92:95]\n\t"
    "ds_read_b128 a[116:119], v5 offset:36864\n\t"
    "v_add_f32 v42, v42, v82\n\t"
    "v_mfma_f32_16x16x32_bf16 v[92:95], a[152:155], a[56:59], v[92:95]\n\t"
    "v_exp_f32 v83, v83\n\t"
    "v_mfma_f32_16x16x32_bf16 v[92:95], a[156:159], a[60:63], v[92:95]\n\t"
    "v_add_f32 v42, v42, v83\n\t"
    "v_cvt_pk_bf16_f32 v73, v82, v83\n\t"
    "v_mfma_f32_16x16x32_bf16 v[96:99], a[192:195], v[76:79], v[96:99]\n\t"
    "v_exp_f32 v84, v84\n\t"
    "v_mfma_f32_16x16x32_bf16 v[100:103], a[196:199], v[76:79], v[100:103]\n\t"
    "ds_read_b128 a[120:123], v6 offset:36864\n\t"
    "v_add_f32 v42, v42, v84\n\t"
    "v_mfma_f32_16x16x32_bf16 v[104:107], a[200:203], v[76:79], v[104:107]\n\t"
    "v_exp_f32 v85, v85\n\t"
    "v_mfma_f32_16x16x32_bf16 v[108:111], a[204:207], v[76:79], v[108:111]\n\t"
    "v_add_f32 v42, v42, v85\n\t"
    "v_cvt_pk_bf16_f32 v74, v84, v85\n\t"
    "v_mfma_f32_16x16x32_bf16 v[112:115], a[208:211], v[76:79], v[112:115]\n\t"
    "v_exp_f32 v86, v86\n\t"
    "v_mfma_f32_16x16x32_bf16 v[116:119], a[212:215], v[76:79], v[116:119]\n\t"
    "ds_read_b128 a[124:127], v7 offset:36864\n\t"
    "v_add_f32 v42, v42, v86\n\t"
    "v_mfma_f32_16x16x32_bf16 v[120:123], a[216:219], v[76:79], v[120:123]\n\t"
    "v_exp_f32 v87, v87\n\t"
    "v_mfma_f32_16x16x32_bf16 v[124:127], a[220:223], v[76:79], v[124:127]\n\t"
    "v_add_f32 v42, v42, v87\n\t"
    "v_cvt_pk_bf16_f32 v75, v86, v87\n\t"
    "v_mfma_f32_16x16x32_bf16 v[80:83], a[128:131], a[64:67], v[64:67]\n\t"
    "v_exp_f32 v88, v88\n\t"
    "v_mfma_f32_16x16x32_bf16 v[80:83], a[132:135], a[68:71], v[80:83]\n\t"
    "ds_read_b64_tr_b16 a[160:161], v8 offset:40960\n\t"
    "v_add_f32 v43, v43, v88\n\t"
    "v_mfma_f32_16x16x32_bf16 v[80:83], a[136:139], a[72:75], v[80:83]\n\t"
    "v_exp_f32 v89, v89\n\t"
    "v_mfma_f32_16x16x32_bf16 v[80:83], a[140:143], a[76:79], v[80:83]\n\t"
    "v_add_f32 v43, v43, v89\n\t"
    "v_cvt_pk_bf16_f32 v76, v88, v89\n\t"
    "v_mfma_f32_16x16x32_bf16 v[84:87], a[144:147], a[64:67], v[64:67]\n\t"
    "v_exp_f32 v90, v90\n\t"
    "v_mfma_f32_16x16x32_bf16 v[84:87], a[148:151], a[68:71], v[84:87]\n\t"
    "ds_read_b64_tr_b16 a[162:163], v8 offset:45056\n\t"
    "v_add_f32 v43, v43, v90\n\t"
    "v_mfma_f32_16x16x32_bf16 v[84:87], a[152:155], a[72:75], v[84:87]\n\t"
    "v_exp_f32 v91, v91\n\t"
    "v_mfma_f32_16x16x32_bf16 v[84:87], a[156:159], a[76:79], v[84:87]\n\t"
    "v_add_f32 v43, v43, v91\n\t"
    "v_cvt_pk_bf16_f32 v77, v90, v91\n\t"
    "v_mfma_f32_16x16x32_bf16 v[128:131], a[192:195], v[72:75], v[128:131]\n\t"
    "v_exp_f32 v92, v92\n\t"
    "v_mfma_f32_16x16x32_bf16 v[132:135], a[196:199], v[72:75], v[132:135]\n\t"
    "ds_read_b64_tr_b16 a[164:165], v9 offset:40960\n\t"
    "v_add_f32 v43, v43, v92\n\t"
    "v_mfma_f32_16x16x32_bf16 v[136:139], a[200:203], v[72:75], v[136:139]\n\t"
    "v_exp_f32 v93, v93\n\t"
    "v_mfma_f32_16x16x32_bf16 v[140:143], a[204:207], v[72:75], v[140:143]\n\t"
    "v_add_f32 v43, v43, v93\n\t"
    "v_cvt_pk_bf16_f32 v78, v92, v93\n\t"
    "v_mfma_f32_16x16x32_bf16 v[144:147], a[208:211], v[72:75], v[144:147]\n\t"
    "v_exp_f32 v94, v94\n\t"
    "v_mfma_f32_16x16x32_bf16 v[148:151], a[212:215], v[72:75], v[148:151]\n\t"
    "ds_read_b64_tr_b16 a[166:167], v9 offset:45056\n\t"
    "v_add_f32 v43, v43, v94\n\t"
    "v_mfma_f32_16x16x32_bf16 v[152:155], a[216:219], v[72:75], v[152:155]\n\t"
    "v_exp_f32 v95, v95\n\t"
    "v_mfma_f32_16x16x32_bf16 v[156:159], a[220:223], v[72:75], v[156:159]\n\t"
    "v_add_f32 v43, v43, v95\n\t"
    "v_cvt_pk_bf16_f32 v79, v94, v95\n\t"
    "v_mfma_f32_16x16x32_bf16 v[88:91], a[128:131], a[80:83], v[68:71]\n\t"
    "v_exp_f32 v80, v80\n\t"
    "v_mfma_f32_16x16x32_bf16 v[88:91], a[132:135], a[84:87], v[88:91]\n\t"
    "ds_read_b64_tr_b16 a[168:169], v10 offset:40960\n\t"
    "v_add_f32 v44, v44, v80\n\t"
    "v_mfma_f32_16x16x32_bf16 v[88:91], a[136:139], a[88:91], v[88:91]\n\t"
    "v_exp_f32 v81, v81\n\t"
    "v_mfma_f32_16x16x32_bf16 v[88:91], a[140:143], a[92:95], v[88:91]\n\t"
    "v_add_f32 v44, v44, v81\n\t"
    "v_cvt_pk_bf16_f32 v72, v80, v81\n\t"
    "v_mfma_f32_16x16x32_bf16 v[92:95], a[144:147], a[80:83], v[68:71]\n\t"
    "v_exp_f32 v82, v82\n\t"
    "v_mfma_f32_16x16x32_bf16 v[92:95], a[148:151], a[84:87], v[92:95]\n\t"
    "ds_read_b64_tr_b16 a[170:171], v10 offset:45056\n\t"
    "v_add_f32 v44, v44, v82\n\t"
    "v_mfma_f32_16x16x32_bf16 v[92:95], a[152:155], a[88:91], v[92:95]\n\t"
    "v_exp_f32 v83, v83\n\t"
    "v_mfma_f32_16x16x32_bf16 v[92:95], a[156:159], a[92:95], v[92:95]\n\t"
    "v_add_f32 v44, v44, v83\n\t"
    "v_cvt_pk_bf16_f32 v73, v82, v83\n\t"
    "v_mfma_f32_16x16x32_bf16 v[160:163], a[192:195], v[76:79], v[160:163]\n\t"
    "ds_read_b128 a[128:131], v4 offset:49152\n\t"
    "v_exp_f32 v84, v84\n\t"
    "v_mfma_f32_16x16x32_bf16 v[164:167], a[196:199], v[76:79], v[164:167]\n\t"
    "ds_read_b64_tr_b16 a[172:173], v11 offset:40960\n\t"
    "v_add_f32 v44, v44, v84\n\t"
    "v_mfma_f32_16x16x32_bf16 v[168:171], a[200:203], v[76:79], v[168:171]\n\t"
    "ds_read_b128 a[132:135], v5 offset:49152\n\t"
    "v_exp_f32 v85, v85\n\t"
    "v_mfma_f32_16x16x32_bf16 v[172:175], a[204:207], v[76:79], v[172:175]\n\t"
    "v_add_f32 v44, v44, v85\n\t"
    "v_cvt_pk_bf16_f32 v74, v84, v85\n\t"
    "v_mfma_f32_16x16x32_bf16 v[176:179], a[208:211], v[76:79], v[176:179]\n\t"
    "ds_read_b128 a[136:139], v6 offset:49152\n\t"
    "v_exp_f32 v86, v86\n\t"
    "v_mfma_f32_16x16x32_bf16 v[180:183], a[212:215], v[76:79], v[180:183]\n\t"
    "ds_read_b64_tr_b16 a[174:175], v11 offset:45056\n\t"
    "v_add_f32 v44, v44, v86\n\t"
    "v_mfma_f32_16x16x32_bf16 v[184:187], a[216:219], v[76:79], v[184:187]\n\t"
    "ds_read_b128 a[140:143], v7 offset:49152\n\t"
    "v_exp_f32 v87, v87\n\t"
    "v_mfma_f32_16x16x32_bf16 v[188:191], a[220:223], v[76:79], v[188:191]\n\t"
    "s_waitcnt lgkmcnt(12)\n\t"
    "v_add_f32 v44, v44, v87\n\t"
    "v_cvt_pk_bf16_f32 v75, v86, v87\n\t"
    "v_mfma_f32_16x16x32_bf16 v[80:83], a[96:99], a[0:3], v[48:51]\n\t"
    "ds_read_b128 a[144:147], v4 offset:53248\n\t"
    "v_exp_f32 v88, v88\n\t"
    "v_mfma_f32_16x16x32_bf16 v[80:83], a[100:103], a[4:7], v[80:83]\n\t"
    "ds_read_b64_tr_b16 a[176:177], v8 offset:41984\n\t"
    "v_add_f32 v45, v45, v88\n\t"
    "v_mfma_f32_16x16x32_bf16 v[80:83], a[104:107], a[8:11], v[80:83]\n\t"
    "ds_read_b128 a[148:151], v5 offset:53248\n\t"
    "v_exp_f32 v89, v89\n\t"
    "v_mfma_f32_16x16x32_bf16 v[80:83], a[108:111], a[12:15], v[80:83]\n\t"
    "v_add_f32 v45, v45, v89\n\t"
    "v_cvt_pk_bf16_f32 v76, v88, v89\n\t"
    "v_mfma_f32_16x16x32_bf16 v[84:87], a[112:115], a[0:3], v[48:51]\n\t"
    "ds_read_b128 a[152:155], v6 offset:53248\n\t"
    "v_exp_f32 v90, v90\n\t"
    "v_mfma_f32_16x16x32_bf16 v[84:87], a[116:119], a[4:7], v[84:87]\n\t"
    "ds_read_b64_tr_b16 a[178:179], v8 offset:46080\n\t"
    "v_add_f32 v45, v45, v90\n\t"
    "v_mfma_f32_16x16x32_bf16 v[84:87], a[120:123], a[8:11], v[84:87]\n\t"
    "ds_read_b128 a[156:159], v7 offset:53248\n\t"
    "v_exp_f32 v91, v91\n\t"
    "v_mfma_f32_16x16x32_bf16 v[84:87], a[124:127], a[12:15], v[84:87]\n\t"
    "v_add_f32 v45, v45, v91\n\t"
    "v_cvt_pk_bf16_f32 v77, v90, v91\n\t"
    "v_mfma_f32_16x16x32_bf16 v[192:195], a[192:195], v[72:75], v[192:195]\n\t"
    "v_exp_f32 v92, v92\n\t"
    "v_mfma_f32_16x16x32_bf16 v[196:199], a[196:199], v[72:75], v[196:199]\n\t"
    "ds_read_b64_tr_b16 a[180:181], v9 offset:41984\n\t"
    "v_add_f32 v45, v45, v92\n\t"
    "v_mfma_f32_16x16x32_bf16 v[200:203], a[200:203], v[72:75], v[200:203]\n\t"
    "v_exp_f32 v93, v93\n\t"
    "v_mfma_f32_16x16x32_bf16 v[204:207], a[204:207], v[72:75], v[204:207]\n\t"
    "v_add_f32 v45, v45, v93\n\t"
    "v_cvt_pk_bf16_f32 v78, v92, v93\n\t"
    "v_mfma_f32_16x16x32_bf16 v[208:211], a[208:211], v[72:75], v[208:211]\n\t"
    "v_exp_f32 v94, v94\n\t"
    "v_mfma_f32_16x16x32_bf16 v[212:215], a[212:215], v[72:75], v[212:215]\n\t"
    "ds_read_b64_tr_b16 a[182:183], v9 offset:46080\n\t"
    "v_add_f32 v45, v45, v94\n\t"
    "v_mfma_f32_16x16x32_bf16 v[216:219], a[216:219], v[72:75], v[216:219]\n\t"
    "v_exp_f32 v95, v95\n\t"
    "v_mfma_f32_16x16x32_bf16 v[220:223], a[220:223], v[72:75], v[220:223]\n\t"
    "s_waitcnt lgkmcnt(2)\n\t"
    "v_add_f32 v45, v45, v95\n\t"
    "v_cvt_pk_bf16_f32 v79, v94, v95\n\t"
    "v_mfma_f32_16x16x32_bf16 v[88:91], a[128:131], a[0:3], v[48:51]\n\t"
    "v_exp_f32 v80, v80\n\t"
    "s_lshl_b32 s69, s60, 14\n\t"
    "v_mfma_f32_16x16x32_bf16 v[88:91], a[132:135], a[4:7], v[88:91]\n\t"
    "ds_read_b64_tr_b16 a[184:185], v10 offset:41984\n\t"
    "v_add_f32 v40, v40, v80\n\t"
    "s_add_u32 s69, s69, s72\n\t"
    "v_mfma_f32_16x16x32_bf16 v[88:91], a[136:139], a[8:11], v[88:91]\n\t"
    "v_exp_f32 v81, v81\n\t"
    "v_and_b32 v2, 15, v0\n\t"
    "v_mfma_f32_16x16x32_bf16 v[88:91], a[140:143], a[12:15], v[88:91]\n\t"
    "v_add_f32 v40, v40, v81\n\t"
    "v_cvt_pk_bf16_f32 v72, v80, v81\n\t"
    "v_lshrrev_b32 v3, 4, v0\n\t"
    "v_mfma_f32_16x16x32_bf16 v[92:95], a[144:147], a[0:3], v[48:51]\n\t"
    "v_exp_f32 v82, v82\n\t"
    "v_mul_u32_u24 v20, 272, v2\n\t"
    "v_mfma_f32_16x16x32_bf16 v[92:95], a[148:151], a[4:7], v[92:95]\n\t"
    "ds_read_b64_tr_b16 a[186:187], v10 offset:46080\n\t"
    "v_add_f32 v40, v40, v82\n\t"
    "v_mfma_f32_16x16x32_bf16 v[92:95], a[152:155], a[8:11], v[92:95]\n\t"
    "v_exp_f32 v83, v83\n\t"
    "v_lshl_add_u32 v20, v3, 3, v20\n\t"
    "v_mfma_f32_16x16x32_bf16 v[92:95], a[156:159], a[12:15], v[92:95]\n\t"
    "v_add_f32 v40, v40, v83\n\t"
    "v_cvt_pk_bf16_f32 v73, v82, v83\n\t"
    "v_and_b32 v22, 8, v0\n\t"
    "v_mfma_f32_16x16x32_bf16 v[224:227], a[192:195], v[76:79], v[224:227]\n\t"
    "v_exp_f32 v84, v84\n\t"
    "v_xor_b32 v20, v22, v20\n\t"
    "v_mfma_f32_16x16x32_bf16 v[228:231], a[196:199], v[76:79], v[228:231]\n\t"
    "ds_read_b64_tr_b16 a[188:189], v11 offset:41984\n\t"
    "v_add_f32 v40, v40, v84\n\t"
    "v_add_u32 v20, s69, v20\n\t"
    "v_mfma_f32_16x16x32_bf16 v[232:235], a[200:203], v[76:79], v[232:235]\n\t"
    "v_exp_f32 v85, v85\n\t"
    "v_mfma_f32_16x16x32_bf16 v[236:239], a[204:207], v[76:79], v[236:239]\n\t"
    "v_add_f32 v40, v40, v85\n\t"
    "v_cvt_pk_bf16_f32 v74, v84, v85\n\t"
    "v_mul_u32_u24 v21, 272, v3\n\t"
    "v_mfma_f32_16x16x32_bf16 v[240:243], a[208:211], v[76:79], v[240:243]\n\t"
    "v_exp_f32 v86, v86\n\t"
    "v_lshl_add_u32 v21, v2, 4, v21\n\t"
    "v_mfma_f32_16x16x32_bf16 v[244:247], a[212:215], v[76:79], v[244:247]\n\t"
    "ds_read_b64_tr_b16 a[190:191], v11 offset:46080\n\t"
    "v_add_f32 v40, v40, v86\n\t"
    "v_add_u32 v21, s69, v21\n\t"
    "v_mfma_f32_16x16x32_bf16 v[248:251], a[216:219], v[76:79], v[248:251]\n\t"
    "v_exp_f32 v87, v87\n\t"
    "v_lshlrev_b32 v22, 4, v0\n\t"
    "v_mfma_f32_16x16x32_bf16 v[252:255], a[220:223], v[76:79], v[252:255]\n\t"
    "s_barrier\n\t"
    "v_add_f32 v40, v40, v87\n\t"
    "v_cvt_pk_bf16_f32 v75, v86, v87\n\t"
    "v_mfma_f32_16x16x32_bf16 v[80:83], a[96:99], a[16:19], v[52:55]\n\t"
    "ds_read_b64_tr_b16 a[192:193], v8 offset:57344\n\t"
    "v_exp_f32 v88, v88\n\t"
    "v_mfma_f32_16x16x32_bf16 v[80:83], a[100:103], a[20:23], v[80:83]\n\t"
    "ds_read_b64_tr_b16 a[194:195], v8 offset:61440\n\t"
    "v_add_f32 v40, v40, v88\n\t"
    "v_mfma_f32_16x16x32_bf16 v[80:83], a[104:107], a[24:27], v[80:83]\n\t"
    "ds_read_b64_tr_b16 a[196:197], v9 offset:57344\n\t"
    "v_exp_f32 v89, v89\n\t"
    "v_mfma_f32_16x16x32_bf16 v[80:83], a[108:111], a[28:31], v[80:83]\n\t"
    "ds_read_b64_tr_b16 a[198:199], v9 offset:61440\n\t"
    "v_add_f32 v40, v40, v89\n\t"
    "v_cvt_pk_bf16_f32 v76, v88, v89\n\t"
    "v_mfma_f32_16x16x32_bf16 v[84:87], a[112:115], a[16:19], v[52:55]\n\t"
    "ds_read_b64_tr_b16 a[200:201], v10 offset:57344\n\t"
    "v_exp_f32 v90, v90\n\t"
    "v_mfma_f32_16x16x32_bf16 v[84:87], a[116:119], a[20:23], v[84:87]\n\t"
    "ds_read_b64_tr_b16 a[202:203], v10 offset:61440\n\t"
    "v_add_f32 v40, v40, v90\n\t"
    "v_mfma_f32_16x16x32_bf16 v[84:87], a[120:123], a[24:27], v[84:87]\n\t"
    "ds_read_b64_tr_b16 a[204:205], v11 offset:57344\n\t"
    "v_exp_f32 v91, v91\n\t"
    "v_mfma_f32_16x16x32_bf16 v[84:87], a[124:127], a[28:31], v[84:87]\n\t"
    "s_waitcnt lgkmcnt(7)\n\t"
    "ds_read_b64_tr_b16 a[206:207], v11 offset:61440\n\t"
    "v_add_f32 v40, v40, v91\n\t"
    "v_cvt_pk_bf16_f32 v77, v90, v91\n\t"
    "v_mfma_f32_16x16x32_bf16 a[224:227], a[160:163], v[72:75], a[224:227]\n\t"
    "ds_read_b64_tr_b16 a[208:209], v8 offset:58368\n\t"
    "v_exp_f32 v92, v92\n\t"
    "v_mfma_f32_16x16x32_bf16 a[228:231], a[164:167], v[72:75], a[228:231]\n\t"
    "ds_read_b64_tr_b16 a[210:211], v8 offset:62464\n\t"
    "v_add_f32 v40, v40, v92\n\t"
    "v_mfma_f32_16x16x32_bf16 a[232:235], a[168:171], v[72:75], a[232:235]\n\t"
    "ds_read_b64_tr_b16 a[212:213], v9 offset:58368\n\t"
    "v_exp_f32 v93, v93\n\t"
    "v_mfma_f32_16x16x32_bf16 a[236:239], a[172:175], v[72:75], a[236:239]\n\t"
    "ds_read_b64_tr_b16 a[214:215], v9 offset:62464\n\t"
    "v_add_f32 v40, v40, v93\n\t"
    "v_cvt_pk_bf16_f32 v78, v92, v93\n\t"
    "v_mfma_f32_16x16x32_bf16 a[240:243], a[176:179], v[72:75], a[240:243]\n\t"
    "ds_read_b64_tr_b16 a[216:217], v10 offset:58368\n\t"
    "v_exp_f32 v94, v94\n\t"
    "v_mfma_f32_16x16x32_bf16 a[244:247], a[180:183], v[72:75], a[244:247]\n\t"
    "ds_read_b64_tr_b16 a[218:219], v10 offset:62464\n\t"
    "v_add_f32 v40, v40, v94\n\t"
    "v_mfma_f32_16x16x32_bf16 a[248:251], a[184:187], v[72:75], a[248:251]\n\t"
    "ds_read_b64_tr_b16 a[220:221], v11 offset:58368\n\t"
    "v_exp_f32 v95, v95\n\t"
    "v_mfma_f32_16x16x32_bf16 a[252:255], a[188:191], v[72:75], a[252:255]\n\t"
    "ds_read_b64_tr_b16 a[222:223], v11 offset:62464\n\t"
    "v_add_f32 v40, v40, v95\n\t"
    "v_cvt_pk_bf16_f32 v79, v94, v95\n\t"
    "v_mfma_f32_16x16x32_bf16 v[88:91], a[128:131], a[16:19], v[52:55]\n\t"
    "v_exp_f32 v80, v80\n\t"
    "v_mov_b32 v2, v40\n\t"
    "v_mfma_f32_16x16x32_bf16 v[88:91], a[132:135], a[20:23], v[88:91]\n\t"
    "v_add_f32 v41, v41, v80\n\t"
    "v_permlane16_swap_b32 v40, v2\n\t"
    "v_mfma_f32_16x16x32_bf16 v[88:91], a[136:139], a[24:27], v[88:91]\n\t"
    "v_exp_f32 v81, v81\n\t"
    "v_add_f32 v40, v40, v2\n\t"
    "v_mfma_f32_16x16x32_bf16 v[88:91], a[140:143], a[28:31], v[88:91]\n\t"
    "v_add_f32 v41, v41, v81\n\t"
    "v_cvt_pk_bf16_f32 v72, v80, v81\n\t"
    "v_mov_b32 v2, v40\n\t"
    "v_mfma_f32_16x16x32_bf16 v[92:95], a[144:147], a[16:19], v[52:55]\n\t"
    "v_exp_f32 v82, v82\n\t"
    "v_mfma_f32_16x16x32_bf16 v[92:95], a[148:151], a[20:23], v[92:95]\n\t"
    "v_add_f32 v41, v41, v82\n\t"
    "v_permlane32_swap_b32 v40, v2\n\t"
    "v_mfma_f32_16x16x32_bf16 v[92:95], a[152:155], a[24:27], v[92:95]\n\t"
    "v_exp_f32 v83, v83\n\t"
    "v_add_f32 v40, v40, v2\n\t"
    "v_mfma_f32_16x16x32_bf16 v[92:95], a[156:159], a[28:31], v[92:95]\n\t"
    "s_waitcnt lgkmcnt(0)\n\t"
    "v_add_f32 v41, v41, v83\n\t"
    "v_cvt_pk_bf16_f32 v73, v82, v83\n\t"
    "v_cmp_ngt_f32 vcc, s71, v40\n\t"
    "v_mfma_f32_16x16x32_bf16 a[224:227], a[192:195], v[76:79], a[224:227]\n\t"
    "v_exp_f32 v84, v84\n\t"
    "v_rcp_f32 v23, v40\n\t"
    "v_mfma_f32_16x16x32_bf16 a[228:231], a[196:199], v[76:79], a[228:231]\n\t"
    "v_add_f32 v41, v41, v84\n\t"
    "v_mfma_f32_16x16x32_bf16 a[232:235], a[200:203], v[76:79], a[232:235]\n\t"
    "v_exp_f32 v85, v85\n\t"
    "s_cmp_lg_u64 vcc, 0\n\t"
    "v_mfma_f32_16x16x32_bf16 a[236:239], a[204:207], v[76:79], a[236:239]\n\t"
    "v_add_f32 v41, v41, v85\n\t"
    "v_cvt_pk_bf16_f32 v74, v84, v85\n\t"
    "s_cselect_b32 s70, 1, 0\n\t"
    "v_mfma_f32_16x16x32_bf16 a[240:243], a[208:211], v[76:79], a[240:243]\n\t"
    "v_exp_f32 v86, v86\n\t"
    "s_or_b32 s68, s68, s70\n\t"
    "v_mfma_f32_16x16x32_bf16 a[244:247], a[212:215], v[76:79], a[244:247]\n\t"
    "v_add_f32 v41, v41, v86\n\t"
    "v_mfma_f32_16x16x32_bf16 a[248:251], a[216:219], v[76:79], a[248:251]\n\t"
    "v_exp_f32 v87, v87\n\t"
    "v_mfma_f32_16x16x32_bf16 a[252:255], a[220:223], v[76:79], a[252:255]\n\t"
    "v_add_f32 v41, v41, v87\n\t"
    "v_cvt_pk_bf16_f32 v75, v86, v87\n\t"
    "v_mov_b32 v46, v23\n\t"
    "v_accvgpr_read_b32 v24, a224\n\t"
    "v_accvgpr_read_b32 v25, a225\n\t"
    "v_accvgpr_read_b32 v26, a226\n\t"
    "v_accvgpr_read_b32 v27, a227\n\t"
    "v_accvgpr_read_b32 v28, a228\n\t"
    "v_accvgpr_read_b32 v29, a229\n\t"
    "v_accvgpr_read_b32 v30, a230\n\t"
    "v_accvgpr_read_b32 v31, a231\n\t"
    "v_accvgpr_read_b32 v32, a232\n\t"
    "v_accvgpr_read_b32 v33, a233\n\t"
    "v_accvgpr_read_b32 v34, a234\n\t"
    "v_accvgpr_read_b32 v35, a235\n\t"
    "v_accvgpr_read_b32 v36, a236\n\t"
    "v_accvgpr_read_b32 v37, a237\n\t"
    "v_accvgpr_read_b32 v38, a238\n\t"
    "v_accvgpr_read_b32 v39, a239\n\t"
    "v_pk_mul_f32 v[24:25], v[24:25], v[46:47] op_sel_hi:[1,0]\n\t"
    "v_pk_mul_f32 v[26:27], v[26:27], v[46:47] op_sel_hi:[1,0]\n\t"
    "v_pk_mul_f32 v[28:29], v[28:29], v[46:47] op_sel_hi:[1,0]\n\t"
    "v_pk_mul_f32 v[30:31], v[30:31], v[46:47] op_sel_hi:[1,0]\n\t"
    "v_pk_mul_f32 v[32:33], v[32:33], v[46:47] op_sel_hi:[1,0]\n\t"
    "v_pk_mul_f32 v[34:35], v[34:35], v[46:47] op_sel_hi:[1,0]\n\t"
    "v_cvt_pk_bf16_f32 v24, v24, v25\n\t"
    "v_cvt_pk_bf16_f32 v25, v26, v27\n\t"
    "v_pk_mul_f32 v[36:37], v[36:37], v[46:47] op_sel_hi:[1,0]\n\t"
    "v_pk_mul_f32 v[38:39], v[38:39], v[46:47] op_sel_hi:[1,0]\n\t"
    "v_cvt_pk_bf16_f32 v28, v28, v29\n\t"
    "v_cvt_pk_bf16_f32 v29, v30, v31\n\t"
    "ds_write_b64 v20, v[24:25] offset:0\n\t"
    "v_cvt_pk_bf16_f32 v32, v32, v33\n\t"
    "v_cvt_pk_bf16_f32 v33, v34, v35\n\t"
    "ds_write_b64 v20, v[28:29] offset:32\n\t"
    "v_cvt_pk_bf16_f32 v36, v36, v37\n\t"
    "v_cvt_pk_bf16_f32 v37, v38, v39\n\t"
    "ds_write_b64 v20, v[32:33] offset:64\n\t"
    "ds_write_b64 v20, v[36:37] offset:96\n\t"
    "v_accvgpr_read_b32 v24, a240\n\t"
    "v_accvgpr_read_b32 v25, a241\n\t"
    "v_accvgpr_read_b32 v26, a242\n\t"
    "v_accvgpr_read_b32 v27, a243\n\t"
    "v_accvgpr_read_b32 v28, a244\n\t"
    "v_accvgpr_read_b32 v29, a245\n\t"
    "v_accvgpr_read_b32 v30, a246\n\t"
    "v_accvgpr_read_b32 v31, a247\n\t"
    "v_accvgpr_read_b32 v32, a248\n\t"
    "v_accvgpr_read_b32 v33, a249\n\t"
    "v_accvgpr_read_b32 v34, a250\n\t"
    "v_accvgpr_read_b32 v35, a251\n\t"
    "v_accvgpr_read_b32 v36, a252\n\t"
    "v_accvgpr_read_b32 v37, a253\n\t"
    "v_accvgpr_read_b32 v38, a254\n\t"
    "v_accvgpr_read_b32 v39, a255\n\t"
    "v_pk_mul_f32 v[24:25], v[24:25], v[46:47] op_sel_hi:[1,0]\n\t"
    "v_pk_mul_f32 v[26:27], v[26:27], v[46:47] op_sel_hi:[1,0]\n\t"
    "v_pk_mul_f32 v[28:29], v[28:29], v[46:47] op_sel_hi:[1,0]\n\t"
    "v_pk_mul_f32 v[30:31], v[30:31], v[46:47] op_sel_hi:[1,0]\n\t"
    "v_pk_mul_f32 v[32:33], v[32:33], v[46:47] op_sel_hi:[1,0]\n\t"
    "v_pk_mul_f32 v[34:35], v[34:35], v[46:47] op_sel_hi:[1,0]\n\t"
    "v_cvt_pk_bf16_f32 v24, v24, v25\n\t"
    "v_cvt_pk_bf16_f32 v25, v26, v27\n\t"
    "v_pk_mul_f32 v[36:37], v[36:37], v[46:47] op_sel_hi:[1,0]\n\t"
    "v_pk_mul_f32 v[38:39], v[38:39], v[46:47] op_sel_hi:[1,0]\n\t"
    "v_cvt_pk_bf16_f32 v28, v28, v29\n\t"
    "v_cvt_pk_bf16_f32 v29, v30, v31\n\t"
    "ds_write_b64 v20, v[24:25] offset:128\n\t"
    "v_cvt_pk_bf16_f32 v32, v32, v33\n\t"
    "v_cvt_pk_bf16_f32 v33, v34, v35\n\t"
    "ds_write_b64 v20, v[28:29] offset:160\n\t"
    "v_cvt_pk_bf16_f32 v36, v36, v37\n\t"
    "v_cvt_pk_bf16_f32 v37, v38, v39\n\t"
    "ds_write_b64 v20, v[32:33] offset:192\n\t"
    "ds_write_b64 v20, v[36:37] offset:224\n\t"
    "v_mfma_f32_16x16x32_bf16 v[80:83], a[96:99], a[32:35], v[56:59]\n\t"
    "v_exp_f32 v88, v88\n\t"
    "v_mfma_f32_16x16x32_bf16 v[80:83], a[100:103], a[36:39], v[80:83]\n\t"
    "v_add_f32 v41, v41, v88\n\t"
    "v_mfma_f32_16x16x32_bf16 v[80:83], a[104:107], a[40:43], v[80:83]\n\t"
    "v_exp_f32 v89, v89\n\t"
    "v_mfma_f32_16x16x32_bf16 v[80:83], a[108:111], a[44:47], v[80:83]\n\t"
    "v_add_f32 v41, v41, v89\n\t"
    "v_cvt_pk_bf16_f32 v76, v88, v89\n\t"
    "v_mfma_f32_16x16x32_bf16 v[84:87], a[112:115], a[32:35], v[56:59]\n\t"
    "s_waitcnt lgkmcnt(0)\n\t"
    "v_exp_f32 v90, v90\n\t"
    "v_mfma_f32_16x16x32_bf16 v[84:87], a[116:119], a[36:39], v[84:87]\n\t"
    "ds_read_b128 v[12:15], v21 offset:0\n\t"
    "v_add_f32 v41, v41, v90\n\t"
    "v_mfma_f32_16x16x32_bf16 v[84:87], a[120:123], a[40:43], v[84:87]\n\t"
    "ds_read_b128 v[48:51], v21 offset:1088\n\t"
    "v_exp_f32 v91, v91\n\t"
    "v_mfma_f32_16x16x32_bf16 v[84:87], a[124:127], a[44:47], v[84:87]\n\t"
    "v_add_u32 v47, 2176, v21\n\t"
    "v_add_f32 v41, v41, v91\n\t"
    "v_cvt_pk_bf16_f32 v77, v90, v91\n\t"
    "v_mfma_f32_16x16x32_bf16 v[96:99], a[160:163], v[72:75], v[96:99]\n\t"
    "ds_read2_b64 v[24:27], v47 offset0:1 offset1:0\n\t"
    "v_exp_f32 v92, v92\n\t"
    "v_mfma_f32_16x16x32_bf16 v[100:103], a[164:167], v[72:75], v[100:103]\n\t"
    "ds_read2_b64 v[28:31], v47 offset0:137 offset1:136\n\t"
    "s_add_u32 s73, s66, 0\n\t"
    "v_add_f32 v41, v41, v92\n\t"
    "v_mfma_f32_16x16x32_bf16 v[104:107], a[168:171], v[72:75], v[104:107]\n\t"
    "v_exp_f32 v93, v93\n\t"
    "v_mfma_f32_16x16x32_bf16 v[108:111], a[172:175], v[72:75], v[108:111]\n\t"
    "s_waitcnt lgkmcnt(3)\n\t"
    "buffer_store_dwordx4 v[12:15], v22, s[52:55], s73 offen offset:0 nt\n\t"
    "v_add_f32 v41, v41, v93\n\t"
    "v_cvt_pk_bf16_f32 v78, v92, v93\n\t"
    "v_mfma_f32_16x16x32_bf16 v[112:115], a[176:179], v[72:75], v[112:115]\n\t"
    "v_exp_f32 v94, v94\n\t"
    "v_mfma_f32_16x16x32_bf16 v[116:119], a[180:183], v[72:75], v[116:119]\n\t"
    "s_waitcnt lgkmcnt(2)\n\t"
    "buffer_store_dwordx4 v[48:51], v22, s[52:55], s73 offen offset:1024 nt\n\t"
    "v_add_f32 v41, v41, v94\n\t"
    "v_mfma_f32_16x16x32_bf16 v[120:123], a[184:187], v[72:75], v[120:123]\n\t"
    "s_waitcnt lgkmcnt(1)\n\t"
    "buffer_store_dwordx4 v[24:27], v22, s[52:55], s73 offen offset:2048 nt\n\t"
    "v_exp_f32 v95, v95\n\t"
    "v_mfma_f32_16x16x32_bf16 v[124:127], a[188:191], v[72:75], v[124:127]\n\t"
    "s_waitcnt lgkmcnt(0)\n\t"
    "buffer_store_dwordx4 v[28:31], v22, s[52:55], s73 offen offset:3072 nt\n\t"
    "v_add_f32 v41, v41, v95\n\t"
    "v_cvt_pk_bf16_f32 v79, v94, v95\n\t"
    "v_mfma_f32_16x16x32_bf16 v[88:91], a[128:131], a[32:35], v[56:59]\n\t"
    "v_exp_f32 v80, v80\n\t"
    "v_mov_b32 v2, v41\n\t"
    "v_mfma_f32_16x16x32_bf16 v[88:91], a[132:135], a[36:39], v[88:91]\n\t"
    "v_add_f32 v42, v42, v80\n\t"
    "v_permlane16_swap_b32 v41, v2\n\t"
    "v_mfma_f32_16x16x32_bf16 v[88:91], a[136:139], a[40:43], v[88:91]\n\t"
    "v_exp_f32 v81, v81\n\t"
    "v_add_f32 v41, v41, v2\n\t"
    "v_mfma_f32_16x16x32_bf16 v[88:91], a[140:143], a[44:47], v[88:91]\n\t"
    "v_add_f32 v42, v42, v81\n\t"
    "v_cvt_pk_bf16_f32 v72, v80, v81\n\t"
    "v_mov_b32 v2, v41\n\t"
    "v_mfma_f32_16x16x32_bf16 v[92:95], a[144:147], a[32:35], v[56:59]\n\t"
    "v_exp_f32 v82, v82\n\t"
    "v_mfma_f32_16x16x32_bf16 v[92:95], a[148:151], a[36:39], v[92:95]\n\t"
    "v_add_f32 v42, v42, v82\n\t"
    "v_permlane32_swap_b32 v41, v2\n\t"
    "v_mfma_f32_16x16x32_bf16 v[92:95], a[152:155], a[40:43], v[92:95]\n\t"
    "v_exp_f32 v83, v83\n\t"
    "v_add_f32 v41, v41, v2\n\t"
    "v_mfma_f32_16x16x32_bf16 v[92:95], a[156:159], a[44:47], v[92:95]\n\t"
    "v_add_f32 v42, v42, v83\n\t"
    "v_cvt_pk_bf16_f32 v73, v82, v83\n\t"
    "v_cmp_ngt_f32 vcc, s71, v41\n\t"
    "v_mfma_f32_16x16x32_bf16 v[96:99], a[192:195], v[76:79], v[96:99]\n\t"
    "v_exp_f32 v84, v84\n\t"
    "v_rcp_f32 v3, v41\n\t"
    "v_mfma_f32_16x16x32_bf16 v[100:103], a[196:199], v[76:79], v[100:103]\n\t"
    "v_add_f32 v42, v42, v84\n\t"
    "v_mfma_f32_16x16x32_bf16 v[104:107], a[200:203], v[76:79], v[104:107]\n\t"
    "v_exp_f32 v85, v85\n\t"
    "s_cmp_lg_u64 vcc, 0\n\t"
    "v_mfma_f32_16x16x32_bf16 v[108:111], a[204:207], v[76:79], v[108:111]\n\t"
    "v_add_f32 v42, v42, v85\n\t"
    "v_cvt_pk_bf16_f32 v74, v84, v85\n\t"
    "s_cselect_b32 s70, 2, 0\n\t"
    "v_mfma_f32_16x16x32_bf16 v[112:115], a[208:211], v[76:79], v[112:115]\n\t"
    "v_exp_f32 v86, v86\n\t"
    "s_or_b32 s68, s68, s70\n\t"
    "v_mfma_f32_16x16x32_bf16 v[116:119], a[212:215], v[76:79], v[116:119]\n\t"
    "v_add_f32 v42, v42, v86\n\t"
    "v_mfma_f32_16x16x32_bf16 v[120:123], a[216:219], v[76:79], v[120:123]\n\t"
    "v_exp_f32 v87, v87\n\t"
    "v_mfma_f32_16x16x32_bf16 v[124:127], a[220:223], v[76:79], v[124:127]\n\t"
    "v_add_f32 v42, v42, v87\n\t"
    "v_cvt_pk_bf16_f32 v75, v86, v87\n\t"
    "v_mov_b32 v46, v3\n\t"
    "v_pk_mul_f32 v[96:97], v[96:97], v[46:47] op_sel_hi:[1,0]\n\t"
    "v_pk_mul_f32 v[98:99], v[98:99], v[46:47] op_sel_hi:[1,0]\n\t"
    "v_pk_mul_f32 v[100:101], v[100:101], v[46:47] op_sel_hi:[1,0]\n\t"
    "v_pk_mul_f32 v[102:103], v[102:103], v[46:47] op_sel_hi:[1,0]\n\t"
    "v_pk_mul_f32 v[104:105], v[104:105], v[46:47] op_sel_hi:[1,0]\n\t"
    "v_pk_mul_f32 v[106:107], v[106:107], v[46:47] op_sel_hi:[1,0]\n\t"
    "v_cvt_pk_bf16_f32 v96, v96, v97\n\t"
    "v_cvt_pk_bf16_f32 v97, v98, v99\n\t"
    "v_pk_mul_f32 v[108:109], v[108:109], v[46:47] op_sel_hi:[1,0]\n\t"
    "v_pk_mul_f32 v[110:111], v[110:111], v[46:47] op_sel_hi:[1,0]\n\t"
    "v_cvt_pk_bf16_f32 v100, v100, v101\n\t"
    "v_cvt_pk_bf16_f32 v101, v102, v103\n\t"
    "ds_write_b64 v20, v[96:97] offset:4352\n\t"
    "v_pk_mul_f32 v[112:113], v[112:113], v[46:47] op_sel_hi:[1,0]\n\t"
    "v_pk_mul_f32 v[114:115], v[114:115], v[46:47] op_sel_hi:[1,0]\n\t"
    "v_cvt_pk_bf16_f32 v104, v104, v105\n\t"
    "v_cvt_pk_bf16_f32 v105, v106, v107\n\t"
    "ds_write_b64 v20, v[100:101] offset:4384\n\t"
    "v_pk_mul_f32 v[116:117], v[116:117], v[46:47] op_sel_hi:[1,0]\n\t"
    "v_pk_mul_f32 v[118:119], v[118:119], v[46:47] op_sel_hi:[1,0]\n\t"
    "v_cvt_pk_bf16_f32 v108, v108, v109\n\t"
    "v_cvt_pk_bf16_f32 v109, v110, v111\n\t"
    "ds_write_b64 v20, v[104:105] offset:4416\n\t"
    "v_pk_mul_f32 v[120:121], v[120:121], v[46:47] op_sel_hi:[1,0]\n\t"
    "v_pk_mul_f32 v[122:123], v[122:123], v[46:47] op_sel_hi:[1,0]\n\t"
    "v_cvt_pk_bf16_f32 v112, v112, v113\n\t"
    "v_cvt_pk_bf16_f32 v113, v114, v115\n\t"
    "ds_write_b64 v20, v[108:109] offset:4448\n\t"
    "v_pk_mul_f32 v[124:125], v[124:125], v[46:47] op_sel_hi:[1,0]\n\t"
    "v_pk_mul_f32 v[126:127], v[126:127], v[46:47] op_sel_hi:[1,0]\n\t"
    "v_cvt_pk_bf16_f32 v116, v116, v117\n\t"
    "v_cvt_pk_bf16_f32 v117, v118, v119\n\t"
    "ds_write_b64 v20, v[112:113] offset:4480\n\t"
    "v_cvt_pk_bf16_f32 v120, v120, v121\n\t"
    "v_cvt_pk_bf16_f32 v121, v122, v123\n\t"
    "ds_write_b64 v20, v[116:117] offset:4512\n\t"
    "v_cvt_pk_bf16_f32 v124, v124, v125\n\t"
    "v_cvt_pk_bf16_f32 v125, v126, v127\n\t"
    "ds_write_b64 v20, v[120:121] offset:4544\n\t"
    "ds_write_b64 v20, v[124:125] offset:4576\n\t"
    "v_mfma_f32_16x16x32_bf16 v[80:83], a[96:99], a[48:51], v[60:63]\n\t"
    "v_exp_f32 v88, v88\n\t"
    "v_mfma_f32_16x16x32_bf16 v[80:83], a[100:103], a[52:55], v[80:83]\n\t"
    "v_add_f32 v42, v42, v88\n\t"
    "v_mfma_f32_16x16x32_bf16 v[80:83], a[104:107], a[56:59], v[80:83]\n\t"
    "v_exp_f32 v89, v89\n\t"
    "v_mfma_f32_16x16x32_bf16 v[80:83], a[108:111], a[60:63], v[80:83]\n\t"
    "v_add_f32 v42, v42, v89\n\t"
    "v_cvt_pk_bf16_f32 v76, v88, v89\n\t"
    "v_mfma_f32_16x16x32_bf16 v[84:87], a[112:115], a[48:51], v[60:63]\n\t"
    "s_waitcnt lgkmcnt(0)\n\t"
    "v_exp_f32 v90, v90\n\t"
    "v_mfma_f32_16x16x32_bf16 v[84:87], a[116:119], a[52:55], v[84:87]\n\t"
    "ds_read_b128 v[32:35], v21 offset:4352\n\t"
    "v_add_f32 v42, v42, v90\n\t"
    "v_mfma_f32_16x16x32_bf16 v[84:87], a[120:123], a[56:59], v[84:87]\n\t"
    "ds_read_b128 v[36:39], v21 offset:5440\n\t"
    "v_exp_f32 v91, v91\n\t"
    "v_mfma_f32_16x16x32_bf16 v[84:87], a[124:127], a[60:63], v[84:87]\n\t"
    "v_add_u32 v47, 6528, v21\n\t"
    "v_add_f32 v42, v42, v91\n\t"
    "v_cvt_pk_bf16_f32 v77, v90, v91\n\t"
    "v_mfma_f32_16x16x32_bf16 v[128:131], a[160:163], v[72:75], v[128:131]\n\t"
    "ds_read2_b64 v[52:55], v47 offset0:1 offset1:0\n\t"
    "v_exp_f32 v92, v92\n\t"
    "v_mfma_f32_16x16x32_bf16 v[132:135], a[164:167], v[72:75], v[132:135]\n\t"
    "ds_read2_b64 v[48:51], v47 offset0:137 offset1:136\n\t"
    "s_add_u32 s73, s66, 16384\n\t"
    "v_add_f32 v42, v42, v92\n\t"
    "v_mfma_f32_16x16x32_bf16 v[136:139], a[168:171], v[72:75], v[136:139]\n\t"
    "v_exp_f32 v93, v93\n\t"
    "v_mfma_f32_16x16x32_bf16 v[140:143], a[172:175], v[72:75], v[140:143]\n\t"
    "s_waitcnt lgkmcnt(3)\n\t"
    "buffer_store_dwordx4 v[32:35], v22, s[52:55], s73 offen offset:0 nt\n\t"
    "v_add_f32 v42, v42, v93\n\t"
    "v_cvt_pk_bf16_f32 v78, v92, v93\n\t"
    "v_mfma_f32_16x16x32_bf16 v[144:147], a[176:179], v[72:75], v[144:147]\n\t"
    "v_exp_f32 v94, v94\n\t"
    "v_mfma_f32_16x16x32_bf16 v[148:151], a[180:183], v[72:75], v[148:151]\n\t"
    "s_waitcnt lgkmcnt(2)\n\t"
    "buffer_store_dwordx4 v[36:39], v22, s[52:55], s73 offen offset:1024 nt\n\t"
    "v_add_f32 v42, v42, v94\n\t"
    "v_mfma_f32_16x16x32_bf16 v[152:155], a[184:187], v[72:75], v[152:155]\n\t"
    "s_waitcnt lgkmcnt(1)\n\t"
    "buffer_store_dwordx4 v[52:55], v22, s[52:55], s73 offen offset:2048 nt\n\t"
    "v_exp_f32 v95, v95\n\t"
    "v_mfma_f32_16x16x32_bf16 v[156:159], a[188:191], v[72:75], v[156:159]\n\t"
    "s_waitcnt lgkmcnt(0)\n\t"
    "buffer_store_dwordx4 v[48:51], v22, s[52:55], s73 offen offset:3072 nt\n\t"
    "v_add_f32 v42, v42, v95\n\t"
    "v_cvt_pk_bf16_f32 v79, v94, v95\n\t"
    "v_mfma_f32_16x16x32_bf16 v[88:91], a[128:131], a[48:51], v[60:63]\n\t"
    "v_exp_f32 v80, v80\n\t"
    "v_mov_b32 v2, v42\n\t"
    "v_mfma_f32_16x16x32_bf16 v[88:91], a[132:135], a[52:55], v[88:91]\n\t"
    "v_add_f32 v43, v43, v80\n\t"
    "v_permlane16_swap_b32 v42, v2\n\t"
    "v_mfma_f32_16x16x32_bf16 v[88:91], a[136:139], a[56:59], v[88:91]\n\t"
    "v_exp_f32 v81, v81\n\t"
    "v_add_f32 v42, v42, v2\n\t"
    "v_mfma_f32_16x16x32_bf16 v[88:91], a[140:143], a[60:63], v[88:91]\n\t"
    "v_add_f32 v43, v43, v81\n\t"
    "v_cvt_pk_bf16_f32 v72, v80, v81\n\t"
    "v_mov_b32 v2, v42\n\t"
    "v_mfma_f32_16x16x32_bf16 v[92:95], a[144:147], a[48:51], v[60:63]\n\t"
    "v_exp_f32 v82, v82\n\t"
    "v_mfma_f32_16x16x32_bf16 v[92:95], a[148:151], a[52:55], v[92:95]\n\t"
    "v_add_f32 v43, v43, v82\n\t"
    "v_permlane32_swap_b32 v42, v2\n\t"
    "v_mfma_f32_16x16x32_bf16 v[92:95], a[152:155], a[56:59], v[92:95]\n\t"
    "v_exp_f32 v83, v83\n\t"
    "v_add_f32 v42, v42, v2\n\t"
    "v_mfma_f32_16x16x32_bf16 v[92:95], a[156:159], a[60:63], v[92:95]\n\t"
    "v_add_f32 v43, v43, v83\n\t"
    "v_cvt_pk_bf16_f32 v73, v82, v83\n\t"
    "v_cmp_ngt_f32 vcc, s71, v42\n\t"
    "v_mfma_f32_16x16x32_bf16 v[128:131], a[192:195], v[76:79], v[128:131]\n\t"
    "v_exp_f32 v84, v84\n\t"
    "v_rcp_f32 v23, v42\n\t"
    "v_mfma_f32_16x16x32_bf16 v[132:135], a[196:199], v[76:79], v[132:135]\n\t"
    "v_add_f32 v43, v43, v84\n\t"
    "v_mfma_f32_16x16x32_bf16 v[136:139], a[200:203], v[76:79], v[136:139]\n\t"
    "v_exp_f32 v85, v85\n\t"
    "s_cmp_lg_u64 vcc, 0\n\t"
    "v_mfma_f32_16x16x32_bf16 v[140:143], a[204:207], v[76:79], v[140:143]\n\t"
    "v_add_f32 v43, v43, v85\n\t"
    "v_cvt_pk_bf16_f32 v74, v84, v85\n\t"
    "s_cselect_b32 s70, 4, 0\n\t"
    "v_mfma_f32_16x16x32_bf16 v[144:147], a[208:211], v[76:79], v[144:147]\n\t"
    "v_exp_f32 v86, v86\n\t"
    "s_or_b32 s68, s68, s70\n\t"
    "v_mfma_f32_16x16x32_bf16 v[148:151], a[212:215], v[76:79], v[148:151]\n\t"
    "v_add_f32 v43, v43, v86\n\t"
    "v_mfma_f32_16x16x32_bf16 v[152:155], a[216:219], v[76:79], v[152:155]\n\t"
    "v_exp_f32 v87, v87\n\t"
    "v_mfma_f32_16x16x32_bf16 v[156:159], a[220:223], v[76:79], v[156:159]\n\t"
    "v_add_f32 v43, v43, v87\n\t"
    "v_cvt_pk_bf16_f32 v75, v86, v87\n\t"
    "v_mov_b32 v46, v23\n\t"
    "v_pk_mul_f32 v[128:129], v[128:129], v[46:47] op_sel_hi:[1,0]\n\t"
    "v_pk_mul_f32 v[130:131], v[130:131], v[46:47] op_sel_hi:[1,0]\n\t"
    "v_pk_mul_f32 v[132:133], v[132:133], v[46:47] op_sel_hi:[1,0]\n\t"
    "v_pk_mul_f32 v[134:135], v[134:135], v[46:47] op_sel_hi:[1,0]\n\t"
    "v_pk_mul_f32 v[136:137], v[136:137], v[46:47] op_sel_hi:[1,0]\n\t"
    "v_pk_mul_f32 v[138:139], v[138:139], v[46:47] op_sel_hi:[1,0]\n\t"
    "v_cvt_pk_bf16_f32 v128, v128, v129\n\t"
    "v_cvt_pk_bf16_f32 v129, v130, v131\n\t"
    "v_pk_mul_f32 v[140:141], v[140:141], v[46:47] op_sel_hi:[1,0]\n\t"
    "v_pk_mul_f32 v[142:143], v[142:143], v[46:47] op_sel_hi:[1,0]\n\t"
    "v_cvt_pk_bf16_f32 v132, v132, v133\n\t"
    "v_cvt_pk_bf16_f32 v133, v134, v135\n\t"
    "ds_write_b64 v20, v[128:129] offset:8704\n\t"
    "v_pk_mul_f32 v[144:145], v[144:145], v[46:47] op_sel_hi:[1,0]\n\t"
    "v_pk_mul_f32 v[146:147], v[146:147], v[46:47] op_sel_hi:[1,0]\n\t"
    "v_cvt_pk_bf16_f32 v136, v136, v137\n\t"
    "v_cvt_pk_bf16_f32 v137, v138, v139\n\t"
    "ds_write_b64 v20, v[132:133] offset:8736\n\t"
    "v_pk_mul_f32 v[148:149], v[148:149], v[46:47] op_sel_hi:[1,0]\n\t"
    "v_pk_mul_f32 v[150:151], v[150:151], v[46:47] op_sel_hi:[1,0]\n\t"
    "v_cvt_pk_bf16_f32 v140, v140, v141\n\t"
    "v_cvt_pk_bf16_f32 v141, v142, v143\n\t"
    "ds_write_b64 v20, v[136:137] offset:8768\n\t"
    "v_pk_mul_f32 v[152:153], v[152:153], v[46:47] op_sel_hi:[1,0]\n\t"
    "v_pk_mul_f32 v[154:155], v[154:155], v[46:47] op_sel_hi:[1,0]\n\t"
    "v_cvt_pk_bf16_f32 v144, v144, v145\n\t"
    "v_cvt_pk_bf16_f32 v145, v146, v147\n\t"
    "ds_write_b64 v20, v[140:141] offset:8800\n\t"
    "v_pk_mul_f32 v[156:157], v[156:157], v[46:47] op_sel_hi:[1,0]\n\t"
    "v_pk_mul_f32 v[158:159], v[158:159], v[46:47] op_sel_hi:[1,0]\n\t"
    "v_cvt_pk_bf16_f32 v148, v148, v149\n\t"
    "v_cvt_pk_bf16_f32 v149, v150, v151\n\t"
    "ds_write_b64 v20, v[144:145] offset:8832\n\t"
    "v_cvt_pk_bf16_f32 v152, v152, v153\n\t"
    "v_cvt_pk_bf16_f32 v153, v154, v155\n\t"
    "ds_write_b64 v20, v[148:149] offset:8864\n\t"
    "v_cvt_pk_bf16_f32 v156, v156, v157\n\t"
    "v_cvt_pk_bf16_f32 v157, v158, v159\n\t"
    "ds_write_b64 v20, v[152:153] offset:8896\n\t"
    "ds_write_b64 v20, v[156:157] offset:8928\n\t"
    "v_mfma_f32_16x16x32_bf16 v[80:83], a[96:99], a[64:67], v[64:67]\n\t"
    "v_exp_f32 v88, v88\n\t"
    "v_mfma_f32_16x16x32_bf16 v[80:83], a[100:103], a[68:71], v[80:83]\n\t"
    "v_add_f32 v43, v43, v88\n\t"
    "v_mfma_f32_16x16x32_bf16 v[80:83], a[104:107], a[72:75], v[80:83]\n\t"
    "v_exp_f32 v89, v89\n\t"
    "v_mfma_f32_16x16x32_bf16 v[80:83], a[108:111], a[76:79], v[80:83]\n\t"
    "v_add_f32 v43, v43, v89\n\t"
    "v_cvt_pk_bf16_f32 v76, v88, v89\n\t"
    "v_mfma_f32_16x16x32_bf16 v[84:87], a[112:115], a[64:67], v[64:67]\n\t"
    "s_waitcnt lgkmcnt(0)\n\t"
    "v_exp_f32 v90, v90\n\t"
    "v_mfma_f32_16x16x32_bf16 v[84:87], a[116:119], a[68:71], v[84:87]\n\t"
    "ds_read_b128 v[32:35], v21 offset:8704\n\t"
    "v_add_f32 v43, v43, v90\n\t"
    "v_mfma_f32_16x16x32_bf16 v[84:87], a[120:123], a[72:75], v[84:87]\n\t"
    "ds_read_b128 v[36:39], v21 offset:9792\n\t"
    "v_exp_f32 v91, v91\n\t"
    "v_mfma_f32_16x16x32_bf16 v[84:87], a[124:127], a[76:79], v[84:87]\n\t"
    "v_add_u32 v47, 10880, v21\n\t"
    "v_add_f32 v43, v43, v91\n\t"
    "v_cvt_pk_bf16_f32 v77, v90, v91\n\t"
    "v_mfma_f32_16x16x32_bf16 v[160:163], a[160:163], v[72:75], v[160:163]\n\t"
    "ds_read2_b64 v[56:59], v47 offset0:1 offset1:0\n\t"
    "v_exp_f32 v92, v92\n\t"
    "v_mfma_f32_16x16x32_bf16 v[164:167], a[164:167], v[72:75], v[164:167]\n\t"
    "ds_read2_b64 v[52:55], v47 offset0:137 offset1:136\n\t"
    "s_add_u32 s73, s66, 32768\n\t"
    "v_add_f32 v43, v43, v92\n\t"
    "v_mfma_f32_16x16x32_bf16 v[168:171], a[168:171], v[72:75], v[168:171]\n\t"
    "v_exp_f32 v93, v93\n\t"
    "v_mfma_f32_16x16x32_bf16 v[172:175], a[172:175], v[72:75], v[172:175]\n\t"
    "s_waitcnt lgkmcnt(3)\n\t"
    "buffer_store_dwordx4 v[32:35], v22, s[52:55], s73 offen offset:0 nt\n\t"
    "v_add_f32 v43, v43, v93\n\t"
    "v_cvt_pk_bf16_f32 v78, v92, v93\n\t"
    "v_mfma_f32_16x16x32_bf16 v[176:179], a[176:179], v[72:75], v[176:179]\n\t"
    "v_exp_f32 v94, v94\n\t"
    "v_mfma_f32_16x16x32_bf16 v[180:183], a[180:183], v[72:75], v[180:183]\n\t"
    "s_waitcnt lgkmcnt(2)\n\t"
    "buffer_store_dwordx4 v[36:39], v22, s[52:55], s73 offen offset:1024 nt\n\t"
    "v_add_f32 v43, v43, v94\n\t"
    "v_mfma_f32_16x16x32_bf16 v[184:187], a[184:187], v[72:75], v[184:187]\n\t"
    "s_waitcnt lgkmcnt(1)\n\t"
    "buffer_store_dwordx4 v[56:59], v22, s[52:55], s73 offen offset:2048 nt\n\t"
    "v_exp_f32 v95, v95\n\t"
    "v_mfma_f32_16x16x32_bf16 v[188:191], a[188:191], v[72:75], v[188:191]\n\t"
    "s_waitcnt lgkmcnt(0)\n\t"
    "buffer_store_dwordx4 v[52:55], v22, s[52:55], s73 offen offset:3072 nt\n\t"
    "v_add_f32 v43, v43, v95\n\t"
    "v_cvt_pk_bf16_f32 v79, v94, v95\n\t"
    "v_mfma_f32_16x16x32_bf16 v[88:91], a[128:131], a[64:67], v[64:67]\n\t"
    "v_exp_f32 v80, v80\n\t"
    "v_mov_b32 v2, v43\n\t"
    "v_mfma_f32_16x16x32_bf16 v[88:91], a[132:135], a[68:71], v[88:91]\n\t"
    "v_add_f32 v44, v44, v80\n\t"
    "v_permlane16_swap_b32 v43, v2\n\t"
    "v_mfma_f32_16x16x32_bf16 v[88:91], a[136:139], a[72:75], v[88:91]\n\t"
    "v_exp_f32 v81, v81\n\t"
    "v_add_f32 v43, v43, v2\n\t"
    "v_mfma_f32_16x16x32_bf16 v[88:91], a[140:143], a[76:79], v[88:91]\n\t"
    "v_add_f32 v44, v44, v81\n\t"
    "v_cvt_pk_bf16_f32 v72, v80, v81\n\t"
    "v_mov_b32 v2, v43\n\t"
    "v_mfma_f32_16x16x32_bf16 v[92:95], a[144:147], a[64:67], v[64:67]\n\t"
    "v_exp_f32 v82, v82\n\t"
    "v_mfma_f32_16x16x32_bf16 v[92:95], a[148:151], a[68:71], v[92:95]\n\t"
    "v_add_f32 v44, v44, v82\n\t"
    "v_permlane32_swap_b32 v43, v2\n\t"
    "v_mfma_f32_16x16x32_bf16 v[92:95], a[152:155], a[72:75], v[92:95]\n\t"
    "v_exp_f32 v83, v83\n\t"
    "v_add_f32 v43, v43, v2\n\t"
    "v_mfma_f32_16x16x32_bf16 v[92:95], a[156:159], a[76:79], v[92:95]\n\t"
    "v_add_f32 v44, v44, v83\n\t"
    "v_cvt_pk_bf16_f32 v73, v82, v83\n\t"
    "v_cmp_ngt_f32 vcc, s71, v43\n\t"
    "v_mfma_f32_16x16x32_bf16 v[160:163], a[192:195], v[76:79], v[160:163]\n\t"
    "v_exp_f32 v84, v84\n\t"
    "v_rcp_f32 v3, v43\n\t"
    "v_mfma_f32_16x16x32_bf16 v[164:167], a[196:199], v[76:79], v[164:167]\n\t"
    "v_add_f32 v44, v44, v84\n\t"
    "v_mfma_f32_16x16x32_bf16 v[168:171], a[200:203], v[76:79], v[168:171]\n\t"
    "v_exp_f32 v85, v85\n\t"
    "s_cmp_lg_u64 vcc, 0\n\t"
    "v_mfma_f32_16x16x32_bf16 v[172:175], a[204:207], v[76:79], v[172:175]\n\t"
    "v_add_f32 v44, v44, v85\n\t"
    "v_cvt_pk_bf16_f32 v74, v84, v85\n\t"
    "s_cselect_b32 s70, 8, 0\n\t"
    "v_mfma_f32_16x16x32_bf16 v[176:179], a[208:211], v[76:79], v[176:179]\n\t"
    "v_exp_f32 v86, v86\n\t"
    "s_or_b32 s68, s68, s70\n\t"
    "v_mfma_f32_16x16x32_bf16 v[180:183], a[212:215], v[76:79], v[180:183]\n\t"
    "v_add_f32 v44, v44, v86\n\t"
    "v_mfma_f32_16x16x32_bf16 v[184:187], a[216:219], v[76:79], v[184:187]\n\t"
    "v_exp_f32 v87, v87\n\t"
    "v_mfma_f32_16x16x32_bf16 v[188:191], a[220:223], v[76:79], v[188:191]\n\t"
    "v_add_f32 v44, v44, v87\n\t"
    "v_cvt_pk_bf16_f32 v75, v86, v87\n\t"
    "v_mov_b32 v46, v3\n\t"
    "v_pk_mul_f32 v[160:161], v[160:161], v[46:47] op_sel_hi:[1,0]\n\t"
    "v_pk_mul_f32 v[162:163], v[162:163], v[46:47] op_sel_hi:[1,0]\n\t"
    "v_pk_mul_f32 v[164:165], v[164:165], v[46:47] op_sel_hi:[1,0]\n\t"
    "v_pk_mul_f32 v[166:167], v[166:167], v[46:47] op_sel_hi:[1,0]\n\t"
    "v_pk_mul_f32 v[168:169], v[168:169], v[46:47] op_sel_hi:[1,0]\n\t"
    "v_pk_mul_f32 v[170:171], v[170:171], v[46:47] op_sel_hi:[1,0]\n\t"
    "v_cvt_pk_bf16_f32 v160, v160, v161\n\t"
    "v_cvt_pk_bf16_f32 v161, v162, v163\n\t"
    "v_pk_mul_f32 v[172:173], v[172:173], v[46:47] op_sel_hi:[1,0]\n\t"
    "v_pk_mul_f32 v[174:175], v[174:175], v[46:47] op_sel_hi:[1,0]\n\t"
    "v_cvt_pk_bf16_f32 v164, v164, v165\n\t"
    "v_cvt_pk_bf16_f32 v165, v166, v167\n\t"
    "ds_write_b64 v20, v[160:161] offset:0\n\t"
    "v_pk_mul_f32 v[176:177], v[176:177], v[46:47] op_sel_hi:[1,0]\n\t"
    "v_pk_mul_f32 v[178:179], v[178:179], v[46:47] op_sel_hi:[1,0]\n\t"
    "v_cvt_pk_bf16_f32 v168, v168, v169\n\t"
    "v_cvt_pk_bf16_f32 v169, v170, v171\n\t"
    "ds_write_b64 v20, v[164:165] offset:32\n\t"
    "v_pk_mul_f32 v[180:181], v[180:181], v[46:47] op_sel_hi:[1,0]\n\t"
    "v_pk_mul_f32 v[182:183], v[182:183], v[46:47] op_sel_hi:[1,0]\n\t"
    "v_cvt_pk_bf16_f32 v172, v172, v173\n\t"
    "v_cvt_pk_bf16_f32 v173, v174, v175\n\t"
    "ds_write_b64 v20, v[168:169] offset:64\n\t"
    "v_pk_mul_f32 v[184:185], v[184:185], v[46:47] op_sel_hi:[1,0]\n\t"
    "v_pk_mul_f32 v[186:187], v[186:187], v[46:47] op_sel_hi:[1,0]\n\t"
    "v_cvt_pk_bf16_f32 v176, v176, v177\n\t"
    "v_cvt_pk_bf16_f32 v177, v178, v179\n\t"
    "ds_write_b64 v20, v[172:173] offset:96\n\t"
    "v_pk_mul_f32 v[188:189], v[188:189], v[46:47] op_sel_hi:[1,0]\n\t"
    "v_pk_mul_f32 v[190:191], v[190:191], v[46:47] op_sel_hi:[1,0]\n\t"
    "v_cvt_pk_bf16_f32 v180, v180, v181\n\t"
    "v_cvt_pk_bf16_f32 v181, v182, v183\n\t"
    "ds_write_b64 v20, v[176:177] offset:128\n\t"
    "v_cvt_pk_bf16_f32 v184, v184, v185\n\t"
    "v_cvt_pk_bf16_f32 v185, v186, v187\n\t"
    "ds_write_b64 v20, v[180:181] offset:160\n\t"
    "v_cvt_pk_bf16_f32 v188, v188, v189\n\t"
    "v_cvt_pk_bf16_f32 v189, v190, v191\n\t"
    "ds_write_b64 v20, v[184:185] offset:192\n\t"
    "ds_write_b64 v20, v[188:189] offset:224\n\t"
    "v_mfma_f32_16x16x32_bf16 v[80:83], a[96:99], a[80:83], v[68:71]\n\t"
    "v_exp_f32 v88, v88\n\t"
    "v_mfma_f32_16x16x32_bf16 v[80:83], a[100:103], a[84:87], v[80:83]\n\t"
    "v_add_f32 v44, v44, v88\n\t"
    "v_mfma_f32_16x16x32_bf16 v[80:83], a[104:107], a[88:91], v[80:83]\n\t"
    "v_exp_f32 v89, v89\n\t"
    "v_mfma_f32_16x16x32_bf16 v[80:83], a[108:111], a[92:95], v[80:83]\n\t"
    "v_add_f32 v44, v44, v89\n\t"
    "v_cvt_pk_bf16_f32 v76, v88, v89\n\t"
    "v_mfma_f32_16x16x32_bf16 v[84:87], a[112:115], a[80:83], v[68:71]\n\t"
    "s_waitcnt lgkmcnt(0)\n\t"
    "v_exp_f32 v90, v90\n\t"
    "v_mfma_f32_16x16x32_bf16 v[84:87], a[116:119], a[84:87], v[84:87]\n\t"
    "ds_read_b128 v[32:35], v21 offset:0\n\t"
    "v_add_f32 v44, v44, v90\n\t"
    "v_mfma_f32_16x16x32_bf16 v[84:87], a[120:123], a[88:91], v[84:87]\n\t"
    "ds_read_b128 v[36:39], v21 offset:1088\n\t"
    "v_exp_f32 v91, v91\n\t"
    "v_mfma_f32_16x16x32_bf16 v[84:87], a[124:127], a[92:95], v[84:87]\n\t"
    "v_add_u32 v47, 2176, v21\n\t"
    "v_add_f32 v44, v44, v91\n\t"
    "v_cvt_pk_bf16_f32 v77, v90, v91\n\t"
    "v_mfma_f32_16x16x32_bf16 v[192:195], a[160:163], v[72:75], v[192:195]\n\t"
    "ds_read2_b64 v[60:63], v47 offset0:1 offset1:0\n\t"
    "v_exp_f32 v92, v92\n\t"
    "v_mfma_f32_16x16x32_bf16 v[196:199], a[164:167], v[72:75], v[196:199]\n\t"
    "ds_read2_b64 v[56:59], v47 offset0:137 offset1:136\n\t"
    "s_add_u32 s73, s66, 49152\n\t"
    "v_add_f32 v44, v44, v92\n\t"
    "v_mfma_f32_16x16x32_bf16 v[200:203], a[168:171], v[72:75], v[200:203]\n\t"
    "v_exp_f32 v93, v93\n\t"
    "v_mfma_f32_16x16x32_bf16 v[204:207], a[172:175], v[72:75], v[204:207]\n\t"
    "s_waitcnt lgkmcnt(3)\n\t"
    "buffer_store_dwordx4 v[32:35], v22, s[52:55], s73 offen offset:0 nt\n\t"
    "v_add_f32 v44, v44, v93\n\t"
    "v_cvt_pk_bf16_f32 v78, v92, v93\n\t"
    "v_mfma_f32_16x16x32_bf16 v[208:211], a[176:179], v[72:75], v[208:211]\n\t"
    "v_exp_f32 v94, v94\n\t"
    "v_mfma_f32_16x16x32_bf16 v[212:215], a[180:183], v[72:75], v[212:215]\n\t"
    "s_waitcnt lgkmcnt(2)\n\t"
    "buffer_store_dwordx4 v[36:39], v22, s[52:55], s73 offen offset:1024 nt\n\t"
    "v_add_f32 v44, v44, v94\n\t"
    "v_mfma_f32_16x16x32_bf16 v[216:219], a[184:187], v[72:75], v[216:219]\n\t"
    "s_waitcnt lgkmcnt(1)\n\t"
    "buffer_store_dwordx4 v[60:63], v22, s[52:55], s73 offen offset:2048 nt\n\t"
    "v_exp_f32 v95, v95\n\t"
    "v_mfma_f32_16x16x32_bf16 v[220:223], a[188:191], v[72:75], v[220:223]\n\t"
    "s_waitcnt lgkmcnt(0)\n\t"
    "buffer_store_dwordx4 v[56:59], v22, s[52:55], s73 offen offset:3072 nt\n\t"
    "v_add_f32 v44, v44, v95\n\t"
    "v_cvt_pk_bf16_f32 v79, v94, v95\n\t"
    "v_mfma_f32_16x16x32_bf16 v[88:91], a[128:131], a[80:83], v[68:71]\n\t"
    "v_exp_f32 v80, v80\n\t"
    "v_mov_b32 v2, v44\n\t"
    "v_mfma_f32_16x16x32_bf16 v[88:91], a[132:135], a[84:87], v[88:91]\n\t"
    "v_add_f32 v45, v45, v80\n\t"
    "v_permlane16_swap_b32 v44, v2\n\t"
    "v_mfma_f32_16x16x32_bf16 v[88:91], a[136:139], a[88:91], v[88:91]\n\t"
    "v_exp_f32 v81, v81\n\t"
    "v_add_f32 v44, v44, v2\n\t"
    "v_mfma_f32_16x16x32_bf16 v[88:91], a[140:143], a[92:95], v[88:91]\n\t"
    "v_add_f32 v45, v45, v81\n\t"
    "v_cvt_pk_bf16_f32 v72, v80, v81\n\t"
    "v_mov_b32 v2, v44\n\t"
    "v_mfma_f32_16x16x32_bf16 v[92:95], a[144:147], a[80:83], v[68:71]\n\t"
    "v_exp_f32 v82, v82\n\t"
    "v_mfma_f32_16x16x32_bf16 v[92:95], a[148:151], a[84:87], v[92:95]\n\t"
    "v_add_f32 v45, v45, v82\n\t"
    "v_permlane32_swap_b32 v44, v2\n\t"
    "v_mfma_f32_16x16x32_bf16 v[92:95], a[152:155], a[88:91], v[92:95]\n\t"
    "v_exp_f32 v83, v83\n\t"
    "v_add_f32 v44, v44, v2\n\t"
    "v_mfma_f32_16x16x32_bf16 v[92:95], a[156:159], a[92:95], v[92:95]\n\t"
    "v_add_f32 v45, v45, v83\n\t"
    "v_cvt_pk_bf16_f32 v73, v82, v83\n\t"
    "v_cmp_ngt_f32 vcc, s71, v44\n\t"
    "v_mfma_f32_16x16x32_bf16 v[192:195], a[192:195], v[76:79], v[192:195]\n\t"
    "v_exp_f32 v84, v84\n\t"
    "v_rcp_f32 v23, v44\n\t"
    "v_mfma_f32_16x16x32_bf16 v[196:199], a[196:199], v[76:79], v[196:199]\n\t"
    "v_add_f32 v45, v45, v84\n\t"
    "v_mfma_f32_16x16x32_bf16 v[200:203], a[200:203], v[76:79], v[200:203]\n\t"
    "v_exp_f32 v85, v85\n\t"
    "s_cmp_lg_u64 vcc, 0\n\t"
    "v_mfma_f32_16x16x32_bf16 v[204:207], a[204:207], v[76:79], v[204:207]\n\t"
    "v_add_f32 v45, v45, v85\n\t"
    "v_cvt_pk_bf16_f32 v74, v84, v85\n\t"
    "s_cselect_b32 s70, 16, 0\n\t"
    "v_mfma_f32_16x16x32_bf16 v[208:211], a[208:211], v[76:79], v[208:211]\n\t"
    "v_exp_f32 v86, v86\n\t"
    "s_or_b32 s68, s68, s70\n\t"
    "v_mfma_f32_16x16x32_bf16 v[212:215], a[212:215], v[76:79], v[212:215]\n\t"
    "v_add_f32 v45, v45, v86\n\t"
    "v_mfma_f32_16x16x32_bf16 v[216:219], a[216:219], v[76:79], v[216:219]\n\t"
    "v_exp_f32 v87, v87\n\t"
    "v_mfma_f32_16x16x32_bf16 v[220:223], a[220:223], v[76:79], v[220:223]\n\t"
    "v_add_f32 v45, v45, v87\n\t"
    "v_cvt_pk_bf16_f32 v75, v86, v87\n\t"
    "v_mov_b32 v46, v23\n\t"
    "v_pk_mul_f32 v[192:193], v[192:193], v[46:47] op_sel_hi:[1,0]\n\t"
    "v_pk_mul_f32 v[194:195], v[194:195], v[46:47] op_sel_hi:[1,0]\n\t"
    "v_pk_mul_f32 v[196:197], v[196:197], v[46:47] op_sel_hi:[1,0]\n\t"
    "v_pk_mul_f32 v[198:199], v[198:199], v[46:47] op_sel_hi:[1,0]\n\t"
    "v_pk_mul_f32 v[200:201], v[200:201], v[46:47] op_sel_hi:[1,0]\n\t"
    "v_pk_mul_f32 v[202:203], v[202:203], v[46:47] op_sel_hi:[1,0]\n\t"
    "v_cvt_pk_bf16_f32 v192, v192, v193\n\t"
    "v_cvt_pk_bf16_f32 v193, v194, v195\n\t"
    "v_pk_mul_f32 v[204:205], v[204:205], v[46:47] op_sel_hi:[1,0]\n\t"
    "v_pk_mul_f32 v[206:207], v[206:207], v[46:47] op_sel_hi:[1,0]\n\t"
    "v_cvt_pk_bf16_f32 v196, v196, v197\n\t"
    "v_cvt_pk_bf16_f32 v197, v198, v199\n\t"
    "ds_write_b64 v20, v[192:193] offset:4352\n\t"
    "v_pk_mul_f32 v[208:209], v[208:209], v[46:47] op_sel_hi:[1,0]\n\t"
    "v_pk_mul_f32 v[210:211], v[210:211], v[46:47] op_sel_hi:[1,0]\n\t"
    "v_cvt_pk_bf16_f32 v200, v200, v201\n\t"
    "v_cvt_pk_bf16_f32 v201, v202, v203\n\t"
    "ds_write_b64 v20, v[196:197] offset:4384\n\t"
    "v_pk_mul_f32 v[212:213], v[212:213], v[46:47] op_sel_hi:[1,0]\n\t"
    "v_pk_mul_f32 v[214:215], v[214:215], v[46:47] op_sel_hi:[1,0]\n\t"
    "v_cvt_pk_bf16_f32 v204, v204, v205\n\t"
    "v_cvt_pk_bf16_f32 v205, v206, v207\n\t"
    "ds_write_b64 v20, v[200:201] offset:4416\n\t"
    "v_pk_mul_f32 v[216:217], v[216:217], v[46:47] op_sel_hi:[1,0]\n\t"
    "v_pk_mul_f32 v[218:219], v[218:219], v[46:47] op_sel_hi:[1,0]\n\t"
    "v_cvt_pk_bf16_f32 v208, v208, v209\n\t"
    "v_cvt_pk_bf16_f32 v209, v210, v211\n\t"
    "ds_write_b64 v20, v[204:205] offset:4448\n\t"
    "v_pk_mul_f32 v[220:221], v[220:221], v[46:47] op_sel_hi:[1,0]\n\t"
    "v_pk_mul_f32 v[222:223], v[222:223], v[46:47] op_sel_hi:[1,0]\n\t"
    "v_cvt_pk_bf16_f32 v212, v212, v213\n\t"
    "v_cvt_pk_bf16_f32 v213, v214, v215\n\t"
    "ds_write_b64 v20, v[208:209] offset:4480\n\t"
    "v_cvt_pk_bf16_f32 v216, v216, v217\n\t"
    "v_cvt_pk_bf16_f32 v217, v218, v219\n\t"
    "ds_write_b64 v20, v[212:213] offset:4512\n\t"
    "v_cvt_pk_bf16_f32 v220, v220, v221\n\t"
    "v_cvt_pk_bf16_f32 v221, v222, v223\n\t"
    "ds_write_b64 v20, v[216:217] offset:4544\n\t"
    "ds_write_b64 v20, v[220:221] offset:4576\n\t"
    "v_exp_f32 v88, v88\n\t"
    "s_nop 0\n\t"
    "v_add_f32 v45, v45, v88\n\t"
    "v_exp_f32 v89, v89\n\t"
    "s_nop 0\n\t"
    "v_add_f32 v45, v45, v89\n\t"
    "v_cvt_pk_bf16_f32 v76, v88, v89\n\t"
    "s_waitcnt lgkmcnt(0)\n\t"
    "v_exp_f32 v90, v90\n\t"
    "ds_read_b128 v[32:35], v21 offset:4352\n\t"
    "v_add_f32 v45, v45, v90\n\t"
    "ds_read_b128 v[36:39], v21 offset:5440\n\t"
    "v_exp_f32 v91, v91\n\t"
    "v_add_u32 v47, 6528, v21\n\t"
    "v_add_f32 v45, v45, v91\n\t"
    "v_cvt_pk_bf16_f32 v77, v90, v91\n\t"
    "v_mfma_f32_16x16x32_bf16 v[224:227], a[160:163], v[72:75], v[224:227]\n\t"
    "ds_read2_b64 v[64:67], v47 offset0:1 offset1:0\n\t"
    "v_exp_f32 v92, v92\n\t"
    "v_mfma_f32_16x16x32_bf16 v[228:231], a[164:167], v[72:75], v[228:231]\n\t"
    "ds_read2_b64 v[60:63], v47 offset0:137 offset1:136\n\t"
    "s_add_u32 s73, s66, 65536\n\t"
    "v_add_f32 v45, v45, v92\n\t"
    "v_mfma_f32_16x16x32_bf16 v[232:235], a[168:171], v[72:75], v[232:235]\n\t"
    "v_exp_f32 v93, v93\n\t"
    "v_mfma_f32_16x16x32_bf16 v[236:239], a[172:175], v[72:75], v[236:239]\n\t"
    "s_waitcnt lgkmcnt(3)\n\t"
    "buffer_store_dwordx4 v[32:35], v22, s[52:55], s73 offen offset:0 nt\n\t"
    "v_add_f32 v45, v45, v93\n\t"
    "v_cvt_pk_bf16_f32 v78, v92, v93\n\t"
    "v_mfma_f32_16x16x32_bf16 v[240:243], a[176:179], v[72:75], v[240:243]\n\t"
    "v_exp_f32 v94, v94\n\t"
    "v_mfma_f32_16x16x32_bf16 v[244:247], a[180:183], v[72:75], v[244:247]\n\t"
    "s_waitcnt lgkmcnt(2)\n\t"
    "buffer_store_dwordx4 v[36:39], v22, s[52:55], s73 offen offset:1024 nt\n\t"
    "v_add_f32 v45, v45, v94\n\t"
    "v_mfma_f32_16x16x32_bf16 v[248:251], a[184:187], v[72:75], v[248:251]\n\t"
    "s_waitcnt lgkmcnt(1)\n\t"
    "buffer_store_dwordx4 v[64:67], v22, s[52:55], s73 offen offset:2048 nt\n\t"
    "v_exp_f32 v95, v95\n\t"
    "v_mfma_f32_16x16x32_bf16 v[252:255], a[188:191], v[72:75], v[252:255]\n\t"
    "s_waitcnt lgkmcnt(0)\n\t"
    "buffer_store_dwordx4 v[60:63], v22, s[52:55], s73 offen offset:3072 nt\n\t"
    "v_add_f32 v45, v45, v95\n\t"
    "v_cvt_pk_bf16_f32 v79, v94, v95\n\t"
    "v_mov_b32 v2, v45\n\t"
    "s_nop 1\n\t"
    "v_permlane16_swap_b32 v45, v2\n\t"
    "v_add_f32 v45, v45, v2\n\t"
    "v_mov_b32 v2, v45\n\t"
    "s_nop 1\n\t"
    "v_permlane32_swap_b32 v45, v2\n\t"
    "v_add_f32 v45, v45, v2\n\t"
    "v_cmp_ngt_f32 vcc, s71, v45\n\t"
    "v_mfma_f32_16x16x32_bf16 v[224:227], a[192:195], v[76:79], v[224:227]\n\t"
    "v_rcp_f32 v3, v45\n\t"
    "v_mfma_f32_16x16x32_bf16 v[228:231], a[196:199], v[76:79], v[228:231]\n\t"
    "v_mfma_f32_16x16x32_bf16 v[232:235], a[200:203], v[76:79], v[232:235]\n\t"
    "s_cmp_lg_u64 vcc, 0\n\t"
    "v_mfma_f32_16x16x32_bf16 v[236:239], a[204:207], v[76:79], v[236:239]\n\t"
    "s_cselect_b32 s70, 32, 0\n\t"
    "v_mfma_f32_16x16x32_bf16 v[240:243], a[208:211], v[76:79], v[240:243]\n\t"
    "s_or_b32 s68, s68, s70\n\t"
    "v_mfma_f32_16x16x32_bf16 v[244:247], a[212:215], v[76:79], v[244:247]\n\t"
    "v_mfma_f32_16x16x32_bf16 v[248:251], a[216:219], v[76:79], v[248:251]\n\t"
    "v_mfma_f32_16x16x32_bf16 v[252:255], a[220:223], v[76:79], v[252:255]\n\t"
    "v_mov_b32 v46, v3\n\t"
    "v_pk_mul_f32 v[224:225], v[224:225], v[46:47] op_sel_hi:[1,0]\n\t"
    "v_pk_mul_f32 v[226:227], v[226:227], v[46:47] op_sel_hi:[1,0]\n\t"
    "v_pk_mul_f32 v[228:229], v[228:229], v[46:47] op_sel_hi:[1,0]\n\t"
    "v_pk_mul_f32 v[230:231], v[230:231], v[46:47] op_sel_hi:[1,0]\n\t"
    "v_pk_mul_f32 v[232:233], v[232:233], v[46:47] op_sel_hi:[1,0]\n\t"
    "v_pk_mul_f32 v[234:235], v[234:235], v[46:47] op_sel_hi:[1,0]\n\t"
    "v_cvt_pk_bf16_f32 v224, v224, v225\n\t"
    "v_cvt_pk_bf16_f32 v225, v226, v227\n\t"
    "v_pk_mul_f32 v[236:237], v[236:237], v[46:47] op_sel_hi:[1,0]\n\t"
    "v_pk_mul_f32 v[238:239], v[238:239], v[46:47] op_sel_hi:[1,0]\n\t"
    "v_cvt_pk_bf16_f32 v228, v228, v229\n\t"
    "v_cvt_pk_bf16_f32 v229, v230, v231\n\t"
    "ds_write_b64 v20, v[224:225] offset:8704\n\t"
    "v_pk_mul_f32 v[240:241], v[240:241], v[46:47] op_sel_hi:[1,0]\n\t"
    "v_pk_mul_f32 v[242:243], v[242:243], v[46:47] op_sel_hi:[1,0]\n\t"
    "v_cvt_pk_bf16_f32 v232, v232, v233\n\t"
    "v_cvt_pk_bf16_f32 v233, v234, v235\n\t"
    "ds_write_b64 v20, v[228:229] offset:8736\n\t"
    "v_pk_mul_f32 v[244:245], v[244:245], v[46:47] op_sel_hi:[1,0]\n\t"
    "v_pk_mul_f32 v[246:247], v[246:247], v[46:47] op_sel_hi:[1,0]\n\t"
    "v_cvt_pk_bf16_f32 v236, v236, v237\n\t"
    "v_cvt_pk_bf16_f32 v237, v238, v239\n\t"
    "ds_write_b64 v20, v[232:233] offset:8768\n\t"
    "v_pk_mul_f32 v[248:249], v[248:249], v[46:47] op_sel_hi:[1,0]\n\t"
    "v_pk_mul_f32 v[250:251], v[250:251], v[46:47] op_sel_hi:[1,0]\n\t"
    "v_cvt_pk_bf16_f32 v240, v240, v241\n\t"
    "v_cvt_pk_bf16_f32 v241, v242, v243\n\t"
    "ds_write_b64 v20, v[236:237] offset:8800\n\t"
    "v_pk_mul_f32 v[252:253], v[252:253], v[46:47] op_sel_hi:[1,0]\n\t"
    "v_pk_mul_f32 v[254:255], v[254:255], v[46:47] op_sel_hi:[1,0]\n\t"
    "v_cvt_pk_bf16_f32 v244, v244, v245\n\t"
    "v_cvt_pk_bf16_f32 v245, v246, v247\n\t"
    "ds_write_b64 v20, v[240:241] offset:8832\n\t"
    "v_cvt_pk_bf16_f32 v248, v248, v249\n\t"
    "v_cvt_pk_bf16_f32 v249, v250, v251\n\t"
    "ds_write_b64 v20, v[244:245] offset:8864\n\t"
    "v_cvt_pk_bf16_f32 v252, v252, v253\n\t"
    "v_cvt_pk_bf16_f32 v253, v254, v255\n\t"
    "ds_write_b64 v20, v[248:249] offset:8896\n\t"
    "ds_write_b64 v20, v[252:253] offset:8928\n\t"
    "s_waitcnt lgkmcnt(0)\n\t"
    "ds_read_b128 v[32:35], v21 offset:8704\n\t"
    "ds_read_b128 v[36:39], v21 offset:9792\n\t"
    "v_add_u32 v47, 10880, v21\n\t"
    "ds_read2_b64 v[68:71], v47 offset0:1 offset1:0\n\t"
    "ds_read2_b64 v[64:67], v47 offset0:137 offset1:136\n\t"
    "s_add_u32 s73, s66, 81920\n\t"
    "s_waitcnt lgkmcnt(3)\n\t"
    "buffer_store_dwordx4 v[32:35], v22, s[52:55], s73 offen offset:0 nt\n\t"
    "s_waitcnt lgkmcnt(2)\n\t"
    "buffer_store_dwordx4 v[36:39], v22, s[52:55], s73 offen offset:1024 nt\n\t"
    "s_waitcnt lgkmcnt(1)\n\t"
    "buffer_store_dwordx4 v[68:71], v22, s[52:55], s73 offen offset:2048 nt\n\t"
    "s_waitcnt lgkmcnt(0)\n\t"
    "buffer_store_dwordx4 v[64:67], v22, s[52:55], s73 offen offset:3072 nt\n\t"
    "s_branch 4f\n\t"
    "3:\n\t"
    "v_mfma_f32_16x16x32_bf16 v[88:91], a[128:131], a[0:3], v[48:51]\n\t"
    "s_waitcnt vmcnt(8)\n\t"
    "ds_read_b128 v[24:27], v20 offset:16384\n\t"
    "ds_read_b128 v[28:31], v21 offset:16384\n\t"
    "ds_read_b128 v[32:35], v22 offset:16384\n\t"
    "ds_read_b128 v[36:39], v23 offset:16384\n\t"
    "ds_read_b64_tr_b16 a[192:193], v8 offset:24576\n\t"
    "v_add_f32 v80, v48, v80\n\t"
    "v_add_f32 v81, v48, v81\n\t"
    "v_mfma_f32_16x16x32_bf16 v[88:91], a[132:135], a[4:7], v[88:91]\n\t"
    "ds_read_b64_tr_b16 a[194:195], v8 offset:28672\n\t"
    "v_add_f32 v82, v48, v82\n\t"
    "v_add_f32 v83, v48, v83\n\t"
    "v_mfma_f32_16x16x32_bf16 v[88:91], a[136:139], a[8:11], v[88:91]\n\t"
    "s_add_u32 m0, s65, 65536\n\t"
    "ds_read_b64_tr_b16 a[196:197], v9 offset:24576\n\t"
    "v_add_f32 v84, v48, v84\n\t"
    "v_add_f32 v85, v48, v85\n\t"
    "v_mfma_f32_16x16x32_bf16 v[88:91], a[140:143], a[12:15], v[88:91]\n\t"
    "s_add_u32 s58, s66, 65536\n\t"
    "ds_read_b64_tr_b16 a[198:199], v9 offset:28672\n\t"
    "v_add_f32 v86, v48, v86\n\t"
    "s_waitcnt lgkmcnt(7)\n\t"
    "v_lshlrev_b32 v2, 16, v24\n\t"
    "v_and_b32 v3, 0xffff0000, v24\n\t"
    "v_mul_f32 v2, s56, v2\n\t"
    "v_mul_f32 v3, s56, v3\n\t"
    "v_cvt_pk_bf16_f32 v24, v2, v3\n\t"
    "v_accvgpr_write_b32 a16, v24\n\t"
    "v_lshlrev_b32 v46, 16, v25\n\t"
    "v_mfma_f32_16x16x32_bf16 v[92:95], a[144:147], a[0:3], v[48:51]\n\t"
    "buffer_load_dwordx4 v12, s[40:43], s58 offen nt lds\n\t"
    "ds_read_b64_tr_b16 a[200:201], v10 offset:24576\n\t"
    "v_add_f32 v87, v48, v87\n\t"
    "v_exp_f32 v80, v80\n\t"
    "v_and_b32 v47, 0xffff0000, v25\n\t"
    "v_mul_f32 v46, s56, v46\n\t"
    "v_mul_f32 v47, s56, v47\n\t"
    "v_cvt_pk_bf16_f32 v25, v46, v47\n\t"
    "v_accvgpr_write_b32 a17, v25\n\t"
    "v_lshlrev_b32 v2, 16, v26\n\t"
    "v_and_b32 v3, 0xffff0000, v26\n\t"
    "v_mul_f32 v2, s56, v2\n\t"
    "v_mfma_f32_16x16x32_bf16 v[92:95], a[148:151], a[4:7], v[92:95]\n\t"
    "buffer_load_dwordx4 v13, s[40:43], s58 offen offset:1024 nt lds\n\t"
    "ds_read_b64_tr_b16 a[202:203], v10 offset:28672\n\t"
    "v_exp_f32 v81, v81\n\t"
    "v_mov_b32 v40, v80\n\t"
    "v_mul_f32 v3, s56, v3\n\t"
    "v_cvt_pk_bf16_f32 v26, v2, v3\n\t"
    "v_accvgpr_write_b32 a18, v26\n\t"
    "v_lshlrev_b32 v46, 16, v27\n\t"
    "v_and_b32 v47, 0xffff0000, v27\n\t"
    "v_mul_f32 v46, s56, v46\n\t"
    "v_mul_f32 v47, s56, v47\n\t"
    "v_cvt_pk_bf16_f32 v27, v46, v47\n\t"
    "v_mfma_f32_16x16x32_bf16 v[92:95], a[152:155], a[8:11], v[92:95]\n\t"
    "buffer_load_dwordx4 v14, s[40:43], s58 offen offset:2048 nt lds\n\t"
    "ds_read_b64_tr_b16 a[204:205], v11 offset:24576\n\t"
    "v_exp_f32 v82, v82\n\t"
    "v_add_f32 v40, v40, v81\n\t"
    "v_accvgpr_write_b32 a19, v27\n\t"
    "s_waitcnt lgkmcnt(9)\n\t"
    "v_lshlrev_b32 v2, 16, v28\n\t"
    "v_and_b32 v3, 0xffff0000, v28\n\t"
    "v_mul_f32 v2, s56, v2\n\t"
    "v_mul_f32 v3, s56, v3\n\t"
    "v_cvt_pk_bf16_f32 v28, v2, v3\n\t"
    "v_mfma_f32_16x16x32_bf16 v[92:95], a[156:159], a[12:15], v[92:95]\n\t"
    "buffer_load_dwordx4 v15, s[40:43], s58 offen offset:3072 nt lds\n\t"
    "ds_read_b64_tr_b16 a[206:207], v11 offset:28672\n\t"
    "v_cvt_pk_bf16_f32 v72, v80, v81\n\t"
    "v_accvgpr_write_b32 a20, v28\n\t"
    "v_lshlrev_b32 v46, 16, v29\n\t"
    "v_and_b32 v47, 0xffff0000, v29\n\t"
    "v_mul_f32 v46, s56, v46\n\t"
    "v_mul_f32 v47, s56, v47\n\t"
    "v_cvt_pk_bf16_f32 v29, v46, v47\n\t"
    "v_accvgpr_write_b32 a21, v29\n\t"
    "v_lshlrev_b32 v2, 16, v30\n\t"
    "ds_read_b64_tr_b16 a[208:209], v8 offset:25600\n\t"
    "v_exp_f32 v83, v83\n\t"
    "v_add_f32 v40, v40, v82\n\t"
    "v_and_b32 v3, 0xffff0000, v30\n\t"
    "v_mul_f32 v2, s56, v2\n\t"
    "v_mul_f32 v3, s56, v3\n\t"
    "v_cvt_pk_bf16_f32 v30, v2, v3\n\t"
    "v_accvgpr_write_b32 a22, v30\n\t"
    "v_lshlrev_b32 v46, 16, v31\n\t"
    "v_and_b32 v47, 0xffff0000, v31\n\t"
    "v_mul_f32 v46, s56, v46\n\t"
    "ds_read_b64_tr_b16 a[210:211], v8 offset:29696\n\t"
    "v_exp_f32 v84, v84\n\t"
    "v_add_f32 v40, v40, v83\n\t"
    "v_mul_f32 v47, s56, v47\n\t"
    "v_cvt_pk_bf16_f32 v31, v46, v47\n\t"
    "v_accvgpr_write_b32 a23, v31\n\t"
    "s_waitcnt lgkmcnt(11)\n\t"
    "v_lshlrev_b32 v2, 16, v32\n\t"
    "v_and_b32 v3, 0xffff0000, v32\n\t"
    "v_mul_f32 v2, s56, v2\n\t"
    "ds_read_b64_tr_b16 a[212:213], v9 offset:25600\n\t"
    "v_cvt_pk_bf16_f32 v73, v82, v83\n\t"
    "v_exp_f32 v85, v85\n\t"
    "v_mul_f32 v3, s56, v3\n\t"
    "v_cvt_pk_bf16_f32 v32, v2, v3\n\t"
    "v_accvgpr_write_b32 a24, v32\n\t"
    "v_lshlrev_b32 v46, 16, v33\n\t"
    "v_and_b32 v47, 0xffff0000, v33\n\t"
    "v_mul_f32 v46, s56, v46\n\t"
    "v_mul_f32 v47, s56, v47\n\t"
    "v_cvt_pk_bf16_f32 v33, v46, v47\n\t"
    "ds_read_b64_tr_b16 a[214:215], v9 offset:29696\n\t"
    "v_add_f32 v40, v40, v84\n\t"
    "v_accvgpr_write_b32 a25, v33\n\t"
    "v_lshlrev_b32 v2, 16, v34\n\t"
    "v_and_b32 v3, 0xffff0000, v34\n\t"
    "v_mul_f32 v2, s56, v2\n\t"
    "v_mul_f32 v3, s56, v3\n\t"
    "v_cvt_pk_bf16_f32 v34, v2, v3\n\t"
    "v_accvgpr_write_b32 a26, v34\n\t"
    "v_lshlrev_b32 v46, 16, v35\n\t"
    "ds_read_b64_tr_b16 a[216:217], v10 offset:25600\n\t"
    "v_exp_f32 v86, v86\n\t"
    "v_add_f32 v40, v40, v85\n\t"
    "v_and_b32 v47, 0xffff0000, v35\n\t"
    "v_mul_f32 v46, s56, v46\n\t"
    "v_mul_f32 v47, s56, v47\n\t"
    "v_cvt_pk_bf16_f32 v35, v46, v47\n\t"
    "v_accvgpr_write_b32 a27, v35\n\t"
    "s_waitcnt lgkmcnt(13)\n\t"
    "v_lshlrev_b32 v2, 16, v36\n\t"
    "ds_read_b64_tr_b16 a[218:219], v10 offset:29696\n\t"
    "v_cvt_pk_bf16_f32 v74, v84, v85\n\t"
    "v_exp_f32 v87, v87\n\t"
    "v_and_b32 v3, 0xffff0000, v36\n\t"
    "v_mul_f32 v2, s56, v2\n\t"
    "v_mul_f32 v3, s56, v3\n\t"
    "v_cvt_pk_bf16_f32 v36, v2, v3\n\t"
    "v_accvgpr_write_b32 a28, v36\n\t"
    "v_lshlrev_b32 v46, 16, v37\n\t"
    "v_and_b32 v47, 0xffff0000, v37\n\t"
    "v_mul_f32 v46, s56, v46\n\t"
    "ds_read_b64_tr_b16 a[220:221], v11 offset:25600\n\t"
    "v_add_f32 v40, v40, v86\n\t"
    "v_add_f32 v40, v40, v87\n\t"
    "v_mul_f32 v47, s56, v47\n\t"
    "v_cvt_pk_bf16_f32 v37, v46, v47\n\t"
    "v_accvgpr_write_b32 a29, v37\n\t"
    "v_lshlrev_b32 v2, 16, v38\n\t"
    "v_and_b32 v3, 0xffff0000, v38\n\t"
    "v_mul_f32 v2, s56, v2\n\t"
    "v_mul_f32 v3, s56, v3\n\t"
    "v_cvt_pk_bf16_f32 v38, v2, v3\n\t"
    "ds_read_b64_tr_b16 a[222:223], v11 offset:29696\n\t"
    "v_cvt_pk_bf16_f32 v75, v86, v87\n\t"
    "v_accvgpr_write_b32 a30, v38\n\t"
    "v_lshlrev_b32 v46, 16, v39\n\t"
    "v_and_b32 v47, 0xffff0000, v39\n\t"
    "v_mul_f32 v46, s56, v46\n\t"
    "v_mul_f32 v47, s56, v47\n\t"
    "v_cvt_pk_bf16_f32 v39, v46, v47\n\t"
    "v_accvgpr_write_b32 a31, v39\n\t"
    "v_mfma_f32_16x16x32_bf16 v[80:83], a[96:99], a[16:19], 0\n\t"
    "v_exp_f32 v88, v88\n\t"
    "v_exp_f32 v89, v89\n\t"
    "v_mfma_f32_16x16x32_bf16 v[80:83], a[100:103], a[20:23], v[80:83]\n\t"
    "v_add_f32 v40, v40, v88\n\t"
    "v_mfma_f32_16x16x32_bf16 v[80:83], a[104:107], a[24:27], v[80:83]\n\t"
    "v_exp_f32 v90, v90\n\t"
    "v_mfma_f32_16x16x32_bf16 v[80:83], a[108:111], a[28:31], v[80:83]\n\t"
    "v_add_f32 v40, v40, v89\n\t"
    "v_mfma_f32_16x16x32_bf16 v[84:87], a[112:115], a[16:19], 0\n\t"
    "v_cvt_pk_bf16_f32 v76, v88, v89\n\t"
    "v_exp_f32 v91, v91\n\t"
    "v_mfma_f32_16x16x32_bf16 v[84:87], a[116:119], a[20:23], v[84:87]\n\t"
    "v_add_f32 v40, v40, v90\n\t"
    "v_mfma_f32_16x16x32_bf16 v[84:87], a[120:123], a[24:27], v[84:87]\n\t"
    "v_exp_f32 v92, v92\n\t"
    "v_mfma_f32_16x16x32_bf16 v[84:87], a[124:127], a[28:31], v[84:87]\n\t"
    "v_add_f32 v40, v40, v91\n\t"
    "v_mfma_f32_16x16x32_bf16 a[224:227], a[160:163], v[72:75], 0\n\t"
    "v_cvt_pk_bf16_f32 v77, v90, v91\n\t"
    "v_exp_f32 v93, v93\n\t"
    "v_mfma_f32_16x16x32_bf16 a[228:231], a[164:167], v[72:75], 0\n\t"
    "v_add_f32 v40, v40, v92\n\t"
    "v_mfma_f32_16x16x32_bf16 a[232:235], a[168:171], v[72:75], 0\n\t"
    "v_exp_f32 v94, v94\n\t"
    "v_max3_f32 v53, v80, v81, v82\n\t"
    "v_max3_f32 v53, v53, v83, v84\n\t"
    "v_max3_f32 v53, v53, v85, v86\n\t"
    "v_mfma_f32_16x16x32_bf16 a[236:239], a[172:175], v[72:75], 0\n\t"
    "v_add_f32 v40, v40, v93\n\t"
    "v_max_f32 v53, v53, v87\n\t"
    "v_mov_b32 v54, v53\n\t"
    "v_mfma_f32_16x16x32_bf16 a[240:243], a[176:179], v[72:75], 0\n\t"
    "v_cvt_pk_bf16_f32 v78, v92, v93\n\t"
    "v_exp_f32 v95, v95\n\t"
    "v_permlane16_swap_b32 v53, v54\n\t"
    "v_max_f32 v53, v53, v54\n\t"
    "v_mfma_f32_16x16x32_bf16 a[244:247], a[180:183], v[72:75], 0\n\t"
    "v_add_f32 v40, v40, v94\n\t"
    "v_mov_b32 v54, v53\n\t"
    "s_nop 1\n\t"
    "v_permlane32_swap_b32 v53, v54\n\t"
    "v_max_f32 v53, v53, v54\n\t"
    "v_mfma_f32_16x16x32_bf16 a[248:251], a[184:187], v[72:75], 0\n\t"
    "v_add_f32 v40, v40, v95\n\t"
    "v_sub_f32 v52, 0, v53\n\t"
    "v_mov_b32 v53, v52\n\t"
    "v_mfma_f32_16x16x32_bf16 a[252:255], a[188:191], v[72:75], 0\n\t"
    "v_cvt_pk_bf16_f32 v79, v94, v95\n\t"
    "v_mov_b32 v54, v52\n\t"
    "v_mov_b32 v55, v52\n\t"
    "s_nop 1\n\t"
    "v_mfma_f32_16x16x32_bf16 v[88:91], a[128:131], a[16:19], v[52:55]\n\t"
    "s_waitcnt vmcnt(8)\n\t"
    "ds_read_b128 v[24:27], v20 offset:32768\n\t"
    "ds_read_b128 v[28:31], v21 offset:32768\n\t"
    "ds_read_b128 v[32:35], v22 offset:32768\n\t"
    "ds_read_b128 v[36:39], v23 offset:32768\n\t"
    "v_add_f32 v80, v52, v80\n\t"
    "v_add_f32 v81, v52, v81\n\t"
    "v_mfma_f32_16x16x32_bf16 v[88:91], a[132:135], a[20:23], v[88:91]\n\t"
    "v_add_f32 v82, v52, v82\n\t"
    "v_add_f32 v83, v52, v83\n\t"
    "v_mfma_f32_16x16x32_bf16 v[88:91], a[136:139], a[24:27], v[88:91]\n\t"
    "s_add_u32 m0, s61, 32768\n\t"
    "v_add_f32 v84, v52, v84\n\t"
    "v_add_f32 v85, v52, v85\n\t"
    "v_mfma_f32_16x16x32_bf16 v[88:91], a[140:143], a[28:31], v[88:91]\n\t"
    "s_add_u32 s58, s62, 16384\n\t"
    "v_add_f32 v86, v52, v86\n\t"
    "s_waitcnt lgkmcnt(3)\n\t"
    "v_lshlrev_b32 v2, 16, v24\n\t"
    "v_and_b32 v3, 0xffff0000, v24\n\t"
    "v_mul_f32 v2, s56, v2\n\t"
    "v_mul_f32 v3, s56, v3\n\t"
    "v_cvt_pk_bf16_f32 v24, v2, v3\n\t"
    "v_accvgpr_write_b32 a32, v24\n\t"
    "v_lshlrev_b32 v46, 16, v25\n\t"
    "v_mfma_f32_16x16x32_bf16 v[92:95], a[144:147], a[16:19], v[52:55]\n\t"
    "buffer_load_dwordx4 v16, s[44:47], s58 offen nt lds\n\t"
    "v_add_f32 v87, v52, v87\n\t"
    "v_exp_f32 v80, v80\n\t"
    "v_and_b32 v47, 0xffff0000, v25\n\t"
    "v_mul_f32 v46, s56, v46\n\t"
    "v_mul_f32 v47, s56, v47\n\t"
    "v_cvt_pk_bf16_f32 v25, v46, v47\n\t"
    "v_accvgpr_write_b32 a33, v25\n\t"
    "v_lshlrev_b32 v2, 16, v26\n\t"
    "v_and_b32 v3, 0xffff0000, v26\n\t"
    "v_mul_f32 v2, s56, v2\n\t"
    "v_mfma_f32_16x16x32_bf16 v[92:95], a[148:151], a[20:23], v[92:95]\n\t"
    "buffer_load_dwordx4 v17, s[44:47], s58 offen offset:1024 nt lds\n\t"
    "v_exp_f32 v81, v81\n\t"
    "v_mov_b32 v41, v80\n\t"
    "v_mul_f32 v3, s56, v3\n\t"
    "v_cvt_pk_bf16_f32 v26, v2, v3\n\t"
    "v_accvgpr_write_b32 a34, v26\n\t"
    "v_lshlrev_b32 v46, 16, v27\n\t"
    "v_and_b32 v47, 0xffff0000, v27\n\t"
    "v_mul_f32 v46, s56, v46\n\t"
    "v_mul_f32 v47, s56, v47\n\t"
    "v_cvt_pk_bf16_f32 v27, v46, v47\n\t"
    "v_mfma_f32_16x16x32_bf16 v[92:95], a[152:155], a[24:27], v[92:95]\n\t"
    "s_add_u32 m0, s63, 32768\n\t"
    "v_exp_f32 v82, v82\n\t"
    "v_add_f32 v41, v41, v81\n\t"
    "v_accvgpr_write_b32 a35, v27\n\t"
    "s_waitcnt lgkmcnt(2)\n\t"
    "v_lshlrev_b32 v2, 16, v28\n\t"
    "v_and_b32 v3, 0xffff0000, v28\n\t"
    "v_mul_f32 v2, s56, v2\n\t"
    "v_mul_f32 v3, s56, v3\n\t"
    "v_cvt_pk_bf16_f32 v28, v2, v3\n\t"
    "v_mfma_f32_16x16x32_bf16 v[92:95], a[156:159], a[28:31], v[92:95]\n\t"
    "s_add_u32 s59, s64, 16384\n\t"
    "v_cvt_pk_bf16_f32 v72, v80, v81\n\t"
    "v_accvgpr_write_b32 a36, v28\n\t"
    "v_lshlrev_b32 v46, 16, v29\n\t"
    "v_and_b32 v47, 0xffff0000, v29\n\t"
    "v_mul_f32 v46, s56, v46\n\t"
    "v_mul_f32 v47, s56, v47\n\t"
    "v_cvt_pk_bf16_f32 v29, v46, v47\n\t"
    "v_accvgpr_write_b32 a37, v29\n\t"
    "v_lshlrev_b32 v2, 16, v30\n\t"
    "v_mfma_f32_16x16x32_bf16 a[224:227], a[192:195], v[76:79], a[224:227]\n\t"
    "buffer_load_dwordx4 v18, s[48:51], s59 offen nt lds\n\t"
    "v_exp_f32 v83, v83\n\t"
    "v_add_f32 v41, v41, v82\n\t"
    "v_and_b32 v3, 0xffff0000, v30\n\t"
    "v_mul_f32 v2, s56, v2\n\t"
    "v_mul_f32 v3, s56, v3\n\t"
    "v_cvt_pk_bf16_f32 v30, v2, v3\n\t"
    "v_accvgpr_write_b32 a38, v30\n\t"
    "v_lshlrev_b32 v46, 16, v31\n\t"
    "v_and_b32 v47, 0xffff0000, v31\n\t"
    "v_mul_f32 v46, s56, v46\n\t"
    "v_mfma_f32_16x16x32_bf16 a[228:231], a[196:199], v[76:79], a[228:231]\n\t"
    "buffer_load_dwordx4 v18, s[48:51], s59 offen offset:2048 nt lds\n\t"
    "v_exp_f32 v84, v84\n\t"
    "v_add_f32 v41, v41, v83\n\t"
    "v_mul_f32 v47, s56, v47\n\t"
    "v_cvt_pk_bf16_f32 v31, v46, v47\n\t"
    "v_accvgpr_write_b32 a39, v31\n\t"
    "s_waitcnt lgkmcnt(1)\n\t"
    "v_lshlrev_b32 v2, 16, v32\n\t"
    "v_and_b32 v3, 0xffff0000, v32\n\t"
    "v_mul_f32 v2, s56, v2\n\t"
    "v_mfma_f32_16x16x32_bf16 a[232:235], a[200:203], v[76:79], a[232:235]\n\t"
    "v_cvt_pk_bf16_f32 v73, v82, v83\n\t"
    "v_exp_f32 v85, v85\n\t"
    "v_mul_f32 v3, s56, v3\n\t"
    "v_cvt_pk_bf16_f32 v32, v2, v3\n\t"
    "v_accvgpr_write_b32 a40, v32\n\t"
    "v_lshlrev_b32 v46, 16, v33\n\t"
    "v_and_b32 v47, 0xffff0000, v33\n\t"
    "v_mul_f32 v46, s56, v46\n\t"
    "v_mul_f32 v47, s56, v47\n\t"
    "v_cvt_pk_bf16_f32 v33, v46, v47\n\t"
    "v_mfma_f32_16x16x32_bf16 a[236:239], a[204:207], v[76:79], a[236:239]\n\t"
    "v_add_f32 v41, v41, v84\n\t"
    "v_accvgpr_write_b32 a41, v33\n\t"
    "v_lshlrev_b32 v2, 16, v34\n\t"
    "v_and_b32 v3, 0xffff0000, v34\n\t"
    "v_mul_f32 v2, s56, v2\n\t"
    "v_mul_f32 v3, s56, v3\n\t"
    "v_cvt_pk_bf16_f32 v34, v2, v3\n\t"
    "v_accvgpr_write_b32 a42, v34\n\t"
    "v_lshlrev_b32 v46, 16, v35\n\t"
    "v_mfma_f32_16x16x32_bf16 a[240:243], a[208:211], v[76:79], a[240:243]\n\t"
    "v_exp_f32 v86, v86\n\t"
    "v_add_f32 v41, v41, v85\n\t"
    "v_and_b32 v47, 0xffff0000, v35\n\t"
    "v_mul_f32 v46, s56, v46\n\t"
    "v_mul_f32 v47, s56, v47\n\t"
    "v_cvt_pk_bf16_f32 v35, v46, v47\n\t"
    "v_accvgpr_write_b32 a43, v35\n\t"
    "s_waitcnt lgkmcnt(0)\n\t"
    "v_lshlrev_b32 v2, 16, v36\n\t"
    "v_mfma_f32_16x16x32_bf16 a[244:247], a[212:215], v[76:79], a[244:247]\n\t"
    "v_cvt_pk_bf16_f32 v74, v84, v85\n\t"
    "v_exp_f32 v87, v87\n\t"
    "v_and_b32 v3, 0xffff0000, v36\n\t"
    "v_mul_f32 v2, s56, v2\n\t"
    "v_mul_f32 v3, s56, v3\n\t"
    "v_cvt_pk_bf16_f32 v36, v2, v3\n\t"
    "v_accvgpr_write_b32 a44, v36\n\t"
    "v_lshlrev_b32 v46, 16, v37\n\t"
    "v_and_b32 v47, 0xffff0000, v37\n\t"
    "v_mul_f32 v46, s56, v46\n\t"
    "v_mfma_f32_16x16x32_bf16 a[248:251], a[216:219], v[76:79], a[248:251]\n\t"
    "v_add_f32 v41, v41, v86\n\t"
    "v_add_f32 v41, v41, v87\n\t"
    "v_mul_f32 v47, s56, v47\n\t"
    "v_cvt_pk_bf16_f32 v37, v46, v47\n\t"
    "v_accvgpr_write_b32 a45, v37\n\t"
    "v_lshlrev_b32 v2, 16, v38\n\t"
    "v_and_b32 v3, 0xffff0000, v38\n\t"
    "v_mul_f32 v2, s56, v2\n\t"
    "v_mul_f32 v3, s56, v3\n\t"
    "v_cvt_pk_bf16_f32 v38, v2, v3\n\t"
    "v_mfma_f32_16x16x32_bf16 a[252:255], a[220:223], v[76:79], a[252:255]\n\t"
    "v_cvt_pk_bf16_f32 v75, v86, v87\n\t"
    "v_accvgpr_write_b32 a46, v38\n\t"
    "v_lshlrev_b32 v46, 16, v39\n\t"
    "v_and_b32 v47, 0xffff0000, v39\n\t"
    "v_mul_f32 v46, s56, v46\n\t"
    "v_mul_f32 v47, s56, v47\n\t"
    "v_cvt_pk_bf16_f32 v39, v46, v47\n\t"
    "v_accvgpr_write_b32 a47, v39\n\t"
    "v_mfma_f32_16x16x32_bf16 v[80:83], a[96:99], a[32:35], 0\n\t"
    "v_exp_f32 v88, v88\n\t"
    "v_exp_f32 v89, v89\n\t"
    "v_mfma_f32_16x16x32_bf16 v[80:83], a[100:103], a[36:39], v[80:83]\n\t"
    "v_add_f32 v41, v41, v88\n\t"
    "v_mfma_f32_16x16x32_bf16 v[80:83], a[104:107], a[40:43], v[80:83]\n\t"
    "v_exp_f32 v90, v90\n\t"
    "v_mfma_f32_16x16x32_bf16 v[80:83], a[108:111], a[44:47], v[80:83]\n\t"
    "v_add_f32 v41, v41, v89\n\t"
    "v_mfma_f32_16x16x32_bf16 v[84:87], a[112:115], a[32:35], 0\n\t"
    "v_cvt_pk_bf16_f32 v76, v88, v89\n\t"
    "v_exp_f32 v91, v91\n\t"
    "v_mfma_f32_16x16x32_bf16 v[84:87], a[116:119], a[36:39], v[84:87]\n\t"
    "v_add_f32 v41, v41, v90\n\t"
    "v_mfma_f32_16x16x32_bf16 v[84:87], a[120:123], a[40:43], v[84:87]\n\t"
    "v_exp_f32 v92, v92\n\t"
    "v_mfma_f32_16x16x32_bf16 v[84:87], a[124:127], a[44:47], v[84:87]\n\t"
    "v_add_f32 v41, v41, v91\n\t"
    "v_mfma_f32_16x16x32_bf16 v[96:99], a[160:163], v[72:75], 0\n\t"
    "v_cvt_pk_bf16_f32 v77, v90, v91\n\t"
    "v_exp_f32 v93, v93\n\t"
    "v_mfma_f32_16x16x32_bf16 v[100:103], a[164:167], v[72:75], 0\n\t"
    "v_add_f32 v41, v41, v92\n\t"
    "v_mfma_f32_16x16x32_bf16 v[104:107], a[168:171], v[72:75], 0\n\t"
    "v_exp_f32 v94, v94\n\t"
    "v_max3_f32 v57, v80, v81, v82\n\t"
    "v_max3_f32 v57, v57, v83, v84\n\t"
    "v_max3_f32 v57, v57, v85, v86\n\t"
    "v_mfma_f32_16x16x32_bf16 v[108:111], a[172:175], v[72:75], 0\n\t"
    "v_add_f32 v41, v41, v93\n\t"
    "v_max_f32 v57, v57, v87\n\t"
    "v_mov_b32 v58, v57\n\t"
    "v_mfma_f32_16x16x32_bf16 v[112:115], a[176:179], v[72:75], 0\n\t"
    "v_cvt_pk_bf16_f32 v78, v92, v93\n\t"
    "v_exp_f32 v95, v95\n\t"
    "v_permlane16_swap_b32 v57, v58\n\t"
    "v_max_f32 v57, v57, v58\n\t"
    "v_mfma_f32_16x16x32_bf16 v[116:119], a[180:183], v[72:75], 0\n\t"
    "v_add_f32 v41, v41, v94\n\t"
    "v_mov_b32 v58, v57\n\t"
    "s_nop 1\n\t"
    "v_permlane32_swap_b32 v57, v58\n\t"
    "v_max_f32 v57, v57, v58\n\t"
    "v_mfma_f32_16x16x32_bf16 v[120:123], a[184:187], v[72:75], 0\n\t"
    "v_add_f32 v41, v41, v95\n\t"
    "v_sub_f32 v56, 0, v57\n\t"
    "v_mov_b32 v57, v56\n\t"
    "v_mfma_f32_16x16x32_bf16 v[124:127], a[188:191], v[72:75], 0\n\t"
    "v_cvt_pk_bf16_f32 v79, v94, v95\n\t"
    "v_mov_b32 v58, v56\n\t"
    "v_mov_b32 v59, v56\n\t"
    "s_nop 1\n\t"
    "v_mfma_f32_16x16x32_bf16 v[88:91], a[128:131], a[32:35], v[56:59]\n\t"
    "s_waitcnt vmcnt(8)\n\t"
    "ds_read_b128 v[24:27], v20 offset:49152\n\t"
    "ds_read_b128 v[28:31], v21 offset:49152\n\t"
    "ds_read_b128 v[32:35], v22 offset:49152\n\t"
    "ds_read_b128 v[36:39], v23 offset:49152\n\t"
    "v_add_f32 v80, v56, v80\n\t"
    "v_add_f32 v81, v56, v81\n\t"
    "v_mfma_f32_16x16x32_bf16 v[88:91], a[132:135], a[36:39], v[88:91]\n\t"
    "v_add_f32 v82, v56, v82\n\t"
    "v_add_f32 v83, v56, v83\n\t"
    "v_mfma_f32_16x16x32_bf16 v[88:91], a[136:139], a[40:43], v[88:91]\n\t"
    "v_add_f32 v84, v56, v84\n\t"
    "v_add_f32 v85, v56, v85\n\t"
    "v_mfma_f32_16x16x32_bf16 v[88:91], a[140:143], a[44:47], v[88:91]\n\t"
    "v_add_f32 v86, v56, v86\n\t"
    "s_waitcnt lgkmcnt(3)\n\t"
    "v_lshlrev_b32 v2, 16, v24\n\t"
    "v_and_b32 v3, 0xffff0000, v24\n\t"
    "v_mul_f32 v2, s56, v2\n\t"
    "v_mul_f32 v3, s56, v3\n\t"
    "v_cvt_pk_bf16_f32 v24, v2, v3\n\t"
    "v_accvgpr_write_b32 a48, v24\n\t"
    "v_lshlrev_b32 v46, 16, v25\n\t"
    "v_mfma_f32_16x16x32_bf16 v[92:95], a[144:147], a[32:35], v[56:59]\n\t"
    "v_add_f32 v87, v56, v87\n\t"
    "v_exp_f32 v80, v80\n\t"
    "v_and_b32 v47, 0xffff0000, v25\n\t"
    "v_mul_f32 v46, s56, v46\n\t"
    "v_mul_f32 v47, s56, v47\n\t"
    "v_cvt_pk_bf16_f32 v25, v46, v47\n\t"
    "v_accvgpr_write_b32 a49, v25\n\t"
    "v_lshlrev_b32 v2, 16, v26\n\t"
    "v_and_b32 v3, 0xffff0000, v26\n\t"
    "v_mul_f32 v2, s56, v2\n\t"
    "v_mfma_f32_16x16x32_bf16 v[92:95], a[148:151], a[36:39], v[92:95]\n\t"
    "v_exp_f32 v81, v81\n\t"
    "v_mov_b32 v42, v80\n\t"
    "v_mul_f32 v3, s56, v3\n\t"
    "v_cvt_pk_bf16_f32 v26, v2, v3\n\t"
    "v_accvgpr_write_b32 a50, v26\n\t"
    "v_lshlrev_b32 v46, 16, v27\n\t"
    "v_and_b32 v47, 0xffff0000, v27\n\t"
    "v_mul_f32 v46, s56, v46\n\t"
    "v_mul_f32 v47, s56, v47\n\t"
    "v_cvt_pk_bf16_f32 v27, v46, v47\n\t"
    "v_mfma_f32_16x16x32_bf16 v[92:95], a[152:155], a[40:43], v[92:95]\n\t"
    "v_exp_f32 v82, v82\n\t"
    "v_add_f32 v42, v42, v81\n\t"
    "v_accvgpr_write_b32 a51, v27\n\t"
    "s_waitcnt lgkmcnt(2)\n\t"
    "v_lshlrev_b32 v2, 16, v28\n\t"
    "v_and_b32 v3, 0xffff0000, v28\n\t"
    "v_mul_f32 v2, s56, v2\n\t"
    "v_mul_f32 v3, s56, v3\n\t"
    "v_cvt_pk_bf16_f32 v28, v2, v3\n\t"
    "v_mfma_f32_16x16x32_bf16 v[92:95], a[156:159], a[44:47], v[92:95]\n\t"
    "v_cvt_pk_bf16_f32 v72, v80, v81\n\t"
    "v_accvgpr_write_b32 a52, v28\n\t"
    "v_lshlrev_b32 v46, 16, v29\n\t"
    "v_and_b32 v47, 0xffff0000, v29\n\t"
    "v_mul_f32 v46, s56, v46\n\t"
    "v_mul_f32 v47, s56, v47\n\t"
    "v_cvt_pk_bf16_f32 v29, v46, v47\n\t"
    "v_accvgpr_write_b32 a53, v29\n\t"
    "v_lshlrev_b32 v2, 16, v30\n\t"
    "v_mfma_f32_16x16x32_bf16 v[96:99], a[192:195], v[76:79], v[96:99]\n\t"
    "v_exp_f32 v83, v83\n\t"
    "v_add_f32 v42, v42, v82\n\t"
    "v_and_b32 v3, 0xffff0000, v30\n\t"
    "v_mul_f32 v2, s56, v2\n\t"
    "v_mul_f32 v3, s56, v3\n\t"
    "v_cvt_pk_bf16_f32 v30, v2, v3\n\t"
    "v_accvgpr_write_b32 a54, v30\n\t"
    "v_lshlrev_b32 v46, 16, v31\n\t"
    "v_and_b32 v47, 0xffff0000, v31\n\t"
    "v_mul_f32 v46, s56, v46\n\t"
    "v_mfma_f32_16x16x32_bf16 v[100:103], a[196:199], v[76:79], v[100:103]\n\t"
    "v_exp_f32 v84, v84\n\t"
    "v_add_f32 v42, v42, v83\n\t"
    "v_mul_f32 v47, s56, v47\n\t"
    "v_cvt_pk_bf16_f32 v31, v46, v47\n\t"
    "v_accvgpr_write_b32 a55, v31\n\t"
    "s_waitcnt lgkmcnt(1)\n\t"
    "v_lshlrev_b32 v2, 16, v32\n\t"
    "v_and_b32 v3, 0xffff0000, v32\n\t"
    "v_mul_f32 v2, s56, v2\n\t"
    "v_mfma_f32_16x16x32_bf16 v[104:107], a[200:203], v[76:79], v[104:107]\n\t"
    "v_cvt_pk_bf16_f32 v73, v82, v83\n\t"
    "v_exp_f32 v85, v85\n\t"
    "v_mul_f32 v3, s56, v3\n\t"
    "v_cvt_pk_bf16_f32 v32, v2, v3\n\t"
    "v_accvgpr_write_b32 a56, v32\n\t"
    "v_lshlrev_b32 v46, 16, v33\n\t"
    "v_and_b32 v47, 0xffff0000, v33\n\t"
    "v_mul_f32 v46, s56, v46\n\t"
    "v_mul_f32 v47, s56, v47\n\t"
    "v_cvt_pk_bf16_f32 v33, v46, v47\n\t"
    "v_mfma_f32_16x16x32_bf16 v[108:111], a[204:207], v[76:79], v[108:111]\n\t"
    "v_add_f32 v42, v42, v84\n\t"
    "v_accvgpr_write_b32 a57, v33\n\t"
    "v_lshlrev_b32 v2, 16, v34\n\t"
    "v_and_b32 v3, 0xffff0000, v34\n\t"
    "v_mul_f32 v2, s56, v2\n\t"
    "v_mul_f32 v3, s56, v3\n\t"
    "v_cvt_pk_bf16_f32 v34, v2, v3\n\t"
    "v_accvgpr_write_b32 a58, v34\n\t"
    "v_lshlrev_b32 v46, 16, v35\n\t"
    "v_mfma_f32_16x16x32_bf16 v[112:115], a[208:211], v[76:79], v[112:115]\n\t"
    "v_exp_f32 v86, v86\n\t"
    "v_add_f32 v42, v42, v85\n\t"
    "v_and_b32 v47, 0xffff0000, v35\n\t"
    "v_mul_f32 v46, s56, v46\n\t"
    "v_mul_f32 v47, s56, v47\n\t"
    "v_cvt_pk_bf16_f32 v35, v46, v47\n\t"
    "v_accvgpr_write_b32 a59, v35\n\t"
    "s_waitcnt lgkmcnt(0)\n\t"
    "v_lshlrev_b32 v2, 16, v36\n\t"
    "v_mfma_f32_16x16x32_bf16 v[116:119], a[212:215], v[76:79], v[116:119]\n\t"
    "v_cvt_pk_bf16_f32 v74, v84, v85\n\t"
    "v_exp_f32 v87, v87\n\t"
    "v_and_b32 v3, 0xffff0000, v36\n\t"
    "v_mul_f32 v2, s56, v2\n\t"
    "v_mul_f32 v3, s56, v3\n\t"
    "v_cvt_pk_bf16_f32 v36, v2, v3\n\t"
    "v_accvgpr_write_b32 a60, v36\n\t"
    "v_lshlrev_b32 v46, 16, v37\n\t"
    "v_and_b32 v47, 0xffff0000, v37\n\t"
    "v_mul_f32 v46, s56, v46\n\t"
    "v_mfma_f32_16x16x32_bf16 v[120:123], a[216:219], v[76:79], v[120:123]\n\t"
    "v_add_f32 v42, v42, v86\n\t"
    "v_add_f32 v42, v42, v87\n\t"
    "v_mul_f32 v47, s56, v47\n\t"
    "v_cvt_pk_bf16_f32 v37, v46, v47\n\t"
    "v_accvgpr_write_b32 a61, v37\n\t"
    "v_lshlrev_b32 v2, 16, v38\n\t"
    "v_and_b32 v3, 0xffff0000, v38\n\t"
    "v_mul_f32 v2, s56, v2\n\t"
    "v_mul_f32 v3, s56, v3\n\t"
    "v_cvt_pk_bf16_f32 v38, v2, v3\n\t"
    "v_mfma_f32_16x16x32_bf16 v[124:127], a[220:223], v[76:79], v[124:127]\n\t"
    "v_cvt_pk_bf16_f32 v75, v86, v87\n\t"
    "v_accvgpr_write_b32 a62, v38\n\t"
    "v_lshlrev_b32 v46, 16, v39\n\t"
    "v_and_b32 v47, 0xffff0000, v39\n\t"
    "v_mul_f32 v46, s56, v46\n\t"
    "v_mul_f32 v47, s56, v47\n\t"
    "v_cvt_pk_bf16_f32 v39, v46, v47\n\t"
    "v_accvgpr_write_b32 a63, v39\n\t"
    "v_mfma_f32_16x16x32_bf16 v[80:83], a[96:99], a[48:51], 0\n\t"
    "v_exp_f32 v88, v88\n\t"
    "v_exp_f32 v89, v89\n\t"
    "v_mfma_f32_16x16x32_bf16 v[80:83], a[100:103], a[52:55], v[80:83]\n\t"
    "v_add_f32 v42, v42, v88\n\t"
    "v_mfma_f32_16x16x32_bf16 v[80:83], a[104:107], a[56:59], v[80:83]\n\t"
    "v_exp_f32 v90, v90\n\t"
    "v_mfma_f32_16x16x32_bf16 v[80:83], a[108:111], a[60:63], v[80:83]\n\t"
    "v_add_f32 v42, v42, v89\n\t"
    "v_mfma_f32_16x16x32_bf16 v[84:87], a[112:115], a[48:51], 0\n\t"
    "v_cvt_pk_bf16_f32 v76, v88, v89\n\t"
    "v_exp_f32 v91, v91\n\t"
    "v_mfma_f32_16x16x32_bf16 v[84:87], a[116:119], a[52:55], v[84:87]\n\t"
    "v_add_f32 v42, v42, v90\n\t"
    "v_mfma_f32_16x16x32_bf16 v[84:87], a[120:123], a[56:59], v[84:87]\n\t"
    "v_exp_f32 v92, v92\n\t"
    "v_mfma_f32_16x16x32_bf16 v[84:87], a[124:127], a[60:63], v[84:87]\n\t"
    "v_add_f32 v42, v42, v91\n\t"
    "v_mfma_f32_16x16x32_bf16 v[128:131], a[160:163], v[72:75], 0\n\t"
    "v_cvt_pk_bf16_f32 v77, v90, v91\n\t"
    "v_exp_f32 v93, v93\n\t"
    "v_mfma_f32_16x16x32_bf16 v[132:135], a[164:167], v[72:75], 0\n\t"
    "v_add_f32 v42, v42, v92\n\t"
    "v_mfma_f32_16x16x32_bf16 v[136:139], a[168:171], v[72:75], 0\n\t"
    "v_exp_f32 v94, v94\n\t"
    "v_max3_f32 v61, v80, v81, v82\n\t"
    "v_max3_f32 v61, v61, v83, v84\n\t"
    "v_max3_f32 v61, v61, v85, v86\n\t"
    "v_mfma_f32_16x16x32_bf16 v[140:143], a[172:175], v[72:75], 0\n\t"
    "v_add_f32 v42, v42, v93\n\t"
    "v_max_f32 v61, v61, v87\n\t"
    "v_mov_b32 v62, v61\n\t"
    "v_mfma_f32_16x16x32_bf16 v[144:147], a[176:179], v[72:75], 0\n\t"
    "v_cvt_pk_bf16_f32 v78, v92, v93\n\t"
    "v_exp_f32 v95, v95\n\t"
    "v_permlane16_swap_b32 v61, v62\n\t"
    "v_max_f32 v61, v61, v62\n\t"
    "v_mfma_f32_16x16x32_bf16 v[148:151], a[180:183], v[72:75], 0\n\t"
    "v_add_f32 v42, v42, v94\n\t"
    "v_mov_b32 v62, v61\n\t"
    "s_nop 1\n\t"
    "v_permlane32_swap_b32 v61, v62\n\t"
    "v_max_f32 v61, v61, v62\n\t"
    "v_mfma_f32_16x16x32_bf16 v[152:155], a[184:187], v[72:75], 0\n\t"
    "v_add_f32 v42, v42, v95\n\t"
    "v_sub_f32 v60, 0, v61\n\t"
    "v_mov_b32 v61, v60\n\t"
    "v_mfma_f32_16x16x32_bf16 v[156:159], a[188:191], v[72:75], 0\n\t"
    "v_cvt_pk_bf16_f32 v79, v94, v95\n\t"
    "v_mov_b32 v62, v60\n\t"
    "v_mov_b32 v63, v60\n\t"
    "s_nop 1\n\t"
    "v_mfma_f32_16x16x32_bf16 v[88:91], a[128:131], a[48:51], v[60:63]\n\t"
    "s_waitcnt vmcnt(4)\n\t"
    "v_add_u32 v20, 0x10000, v20\n\t"
    "v_add_u32 v21, 0x10000, v21\n\t"
    "v_add_u32 v22, 0x10000, v22\n\t"
    "v_add_u32 v23, 0x10000, v23\n\t"
    "ds_read_b128 v[24:27], v20 offset:0\n\t"
    "s_add_u32 m0, s61, 49152\n\t"
    "v_add_f32 v80, v60, v80\n\t"
    "v_add_f32 v81, v60, v81\n\t"
    "v_mfma_f32_16x16x32_bf16 v[88:91], a[132:135], a[52:55], v[88:91]\n\t"
    "ds_read_b128 v[28:31], v21 offset:0\n\t"
    "ds_read_b128 v[32:35], v22 offset:0\n\t"
    "ds_read_b128 v[36:39], v23 offset:0\n\t"
    "s_add_u32 s58, s62, 24576\n\t"
    "v_add_f32 v82, v60, v82\n\t"
    "v_add_f32 v83, v60, v83\n\t"
    "v_mfma_f32_16x16x32_bf16 v[88:91], a[136:139], a[56:59], v[88:91]\n\t"
    "buffer_load_dwordx4 v16, s[44:47], s58 offen nt lds\n\t"
    "v_add_f32 v84, v60, v84\n\t"
    "v_add_f32 v85, v60, v85\n\t"
    "v_mfma_f32_16x16x32_bf16 v[88:91], a[140:143], a[60:63], v[88:91]\n\t"
    "buffer_load_dwordx4 v17, s[44:47], s58 offen offset:1024 nt lds\n\t"
    "v_add_f32 v86, v60, v86\n\t"
    "s_waitcnt lgkmcnt(3)\n\t"
    "v_lshlrev_b32 v2, 16, v24\n\t"
    "v_and_b32 v3, 0xffff0000, v24\n\t"
    "v_mul_f32 v2, s56, v2\n\t"
    "v_mul_f32 v3, s56, v3\n\t"
    "v_cvt_pk_bf16_f32 v24, v2, v3\n\t"
    "v_accvgpr_write_b32 a64, v24\n\t"
    "v_lshlrev_b32 v46, 16, v25\n\t"
    "v_mfma_f32_16x16x32_bf16 v[92:95], a[144:147], a[48:51], v[60:63]\n\t"
    "s_add_u32 m0, s63, 49152\n\t"
    "v_add_f32 v87, v60, v87\n\t"
    "v_exp_f32 v80, v80\n\t"
    "v_and_b32 v47, 0xffff0000, v25\n\t"
    "v_mul_f32 v46, s56, v46\n\t"
    "v_mul_f32 v47, s56, v47\n\t"
    "v_cvt_pk_bf16_f32 v25, v46, v47\n\t"
    "v_accvgpr_write_b32 a65, v25\n\t"
    "v_lshlrev_b32 v2, 16, v26\n\t"
    "v_and_b32 v3, 0xffff0000, v26\n\t"
    "v_mul_f32 v2, s56, v2\n\t"
    "v_mfma_f32_16x16x32_bf16 v[92:95], a[148:151], a[52:55], v[92:95]\n\t"
    "s_add_u32 s59, s64, 24576\n\t"
    "v_exp_f32 v81, v81\n\t"
    "v_mov_b32 v43, v80\n\t"
    "v_mul_f32 v3, s56, v3\n\t"
    "v_cvt_pk_bf16_f32 v26, v2, v3\n\t"
    "v_accvgpr_write_b32 a66, v26\n\t"
    "v_lshlrev_b32 v46, 16, v27\n\t"
    "v_and_b32 v47, 0xffff0000, v27\n\t"
    "v_mul_f32 v46, s56, v46\n\t"
    "v_mul_f32 v47, s56, v47\n\t"
    "v_cvt_pk_bf16_f32 v27, v46, v47\n\t"
    "v_mfma_f32_16x16x32_bf16 v[92:95], a[152:155], a[56:59], v[92:95]\n\t"
    "buffer_load_dwordx4 v18, s[48:51], s59 offen nt lds\n\t"
    "v_exp_f32 v82, v82\n\t"
    "v_add_f32 v43, v43, v81\n\t"
    "v_accvgpr_write_b32 a67, v27\n\t"
    "s_waitcnt lgkmcnt(2)\n\t"
    "v_lshlrev_b32 v2, 16, v28\n\t"
    "v_and_b32 v3, 0xffff0000, v28\n\t"
    "v_mul_f32 v2, s56, v2\n\t"
    "v_mul_f32 v3, s56, v3\n\t"
    "v_cvt_pk_bf16_f32 v28, v2, v3\n\t"
    "v_mfma_f32_16x16x32_bf16 v[92:95], a[156:159], a[60:63], v[92:95]\n\t"
    "buffer_load_dwordx4 v18, s[48:51], s59 offen offset:2048 nt lds\n\t"
    "v_cvt_pk_bf16_f32 v72, v80, v81\n\t"
    "v_accvgpr_write_b32 a68, v28\n\t"
    "v_lshlrev_b32 v46, 16, v29\n\t"
    "v_and_b32 v47, 0xffff0000, v29\n\t"
    "v_mul_f32 v46, s56, v46\n\t"
    "v_mul_f32 v47, s56, v47\n\t"
    "v_cvt_pk_bf16_f32 v29, v46, v47\n\t"
    "v_accvgpr_write_b32 a69, v29\n\t"
    "v_lshlrev_b32 v2, 16, v30\n\t"
    "v_mfma_f32_16x16x32_bf16 v[128:131], a[192:195], v[76:79], v[128:131]\n\t"
    "v_exp_f32 v83, v83\n\t"
    "v_add_f32 v43, v43, v82\n\t"
    "v_and_b32 v3, 0xffff0000, v30\n\t"
    "v_mul_f32 v2, s56, v2\n\t"
    "v_mul_f32 v3, s56, v3\n\t"
    "v_cvt_pk_bf16_f32 v30, v2, v3\n\t"
    "v_accvgpr_write_b32 a70, v30\n\t"
    "v_lshlrev_b32 v46, 16, v31\n\t"
    "v_and_b32 v47, 0xffff0000, v31\n\t"
    "v_mul_f32 v46, s56, v46\n\t"
    "v_mfma_f32_16x16x32_bf16 v[132:135], a[196:199], v[76:79], v[132:135]\n\t"
    "v_exp_f32 v84, v84\n\t"
    "v_add_f32 v43, v43, v83\n\t"
    "v_mul_f32 v47, s56, v47\n\t"
    "v_cvt_pk_bf16_f32 v31, v46, v47\n\t"
    "v_accvgpr_write_b32 a71, v31\n\t"
    "s_waitcnt lgkmcnt(1)\n\t"
    "v_lshlrev_b32 v2, 16, v32\n\t"
    "v_and_b32 v3, 0xffff0000, v32\n\t"
    "v_mul_f32 v2, s56, v2\n\t"
    "v_mfma_f32_16x16x32_bf16 v[136:139], a[200:203], v[76:79], v[136:139]\n\t"
    "v_cvt_pk_bf16_f32 v73, v82, v83\n\t"
    "v_exp_f32 v85, v85\n\t"
    "v_mul_f32 v3, s56, v3\n\t"
    "v_cvt_pk_bf16_f32 v32, v2, v3\n\t"
    "v_accvgpr_write_b32 a72, v32\n\t"
    "v_lshlrev_b32 v46, 16, v33\n\t"
    "v_and_b32 v47, 0xffff0000, v33\n\t"
    "v_mul_f32 v46, s56, v46\n\t"
    "v_mul_f32 v47, s56, v47\n\t"
    "v_cvt_pk_bf16_f32 v33, v46, v47\n\t"
    "v_mfma_f32_16x16x32_bf16 v[140:143], a[204:207], v[76:79], v[140:143]\n\t"
    "v_add_f32 v43, v43, v84\n\t"
    "v_accvgpr_write_b32 a73, v33\n\t"
    "v_lshlrev_b32 v2, 16, v34\n\t"
    "v_and_b32 v3, 0xffff0000, v34\n\t"
    "v_mul_f32 v2, s56, v2\n\t"
    "v_mul_f32 v3, s56, v3\n\t"
    "v_cvt_pk_bf16_f32 v34, v2, v3\n\t"
    "v_accvgpr_write_b32 a74, v34\n\t"
    "v_lshlrev_b32 v46, 16, v35\n\t"
    "v_mfma_f32_16x16x32_bf16 v[144:147], a[208:211], v[76:79], v[144:147]\n\t"
    "v_exp_f32 v86, v86\n\t"
    "v_add_f32 v43, v43, v85\n\t"
    "v_and_b32 v47, 0xffff0000, v35\n\t"
    "v_mul_f32 v46, s56, v46\n\t"
    "v_mul_f32 v47, s56, v47\n\t"
    "v_cvt_pk_bf16_f32 v35, v46, v47\n\t"
    "v_accvgpr_write_b32 a75, v35\n\t"
    "s_waitcnt lgkmcnt(0)\n\t"
    "v_lshlrev_b32 v2, 16, v36\n\t"
    "v_mfma_f32_16x16x32_bf16 v[148:151], a[212:215], v[76:79], v[148:151]\n\t"
    "v_cvt_pk_bf16_f32 v74, v84, v85\n\t"
    "v_exp_f32 v87, v87\n\t"
    "v_and_b32 v3, 0xffff0000, v36\n\t"
    "v_mul_f32 v2, s56, v2\n\t"
    "v_mul_f32 v3, s56, v3\n\t"
    "v_cvt_pk_bf16_f32 v36, v2, v3\n\t"
    "v_accvgpr_write_b32 a76, v36\n\t"
    "v_lshlrev_b32 v46, 16, v37\n\t"
    "v_and_b32 v47, 0xffff0000, v37\n\t"
    "v_mul_f32 v46, s56, v46\n\t"
    "v_mfma_f32_16x16x32_bf16 v[152:155], a[216:219], v[76:79], v[152:155]\n\t"
    "v_add_f32 v43, v43, v86\n\t"
    "v_add_f32 v43, v43, v87\n\t"
    "v_mul_f32 v47, s56, v47\n\t"
    "v_cvt_pk_bf16_f32 v37, v46, v47\n\t"
    "v_accvgpr_write_b32 a77, v37\n\t"
    "v_lshlrev_b32 v2, 16, v38\n\t"
    "v_and_b32 v3, 0xffff0000, v38\n\t"
    "v_mul_f32 v2, s56, v2\n\t"
    "v_mul_f32 v3, s56, v3\n\t"
    "v_cvt_pk_bf16_f32 v38, v2, v3\n\t"
    "v_mfma_f32_16x16x32_bf16 v[156:159], a[220:223], v[76:79], v[156:159]\n\t"
    "v_cvt_pk_bf16_f32 v75, v86, v87\n\t"
    "v_accvgpr_write_b32 a78, v38\n\t"
    "v_lshlrev_b32 v46, 16, v39\n\t"
    "v_and_b32 v47, 0xffff0000, v39\n\t"
    "v_mul_f32 v46, s56, v46\n\t"
    "v_mul_f32 v47, s56, v47\n\t"
    "v_cvt_pk_bf16_f32 v39, v46, v47\n\t"
    "v_accvgpr_write_b32 a79, v39\n\t"
    "v_mfma_f32_16x16x32_bf16 v[80:83], a[96:99], a[64:67], 0\n\t"
    "v_exp_f32 v88, v88\n\t"
    "v_exp_f32 v89, v89\n\t"
    "v_mfma_f32_16x16x32_bf16 v[80:83], a[100:103], a[68:71], v[80:83]\n\t"
    "v_add_f32 v43, v43, v88\n\t"
    "v_mfma_f32_16x16x32_bf16 v[80:83], a[104:107], a[72:75], v[80:83]\n\t"
    "v_exp_f32 v90, v90\n\t"
    "v_mfma_f32_16x16x32_bf16 v[80:83], a[108:111], a[76:79], v[80:83]\n\t"
    "v_add_f32 v43, v43, v89\n\t"
    "v_mfma_f32_16x16x32_bf16 v[84:87], a[112:115], a[64:67], 0\n\t"
    "v_cvt_pk_bf16_f32 v76, v88, v89\n\t"
    "v_exp_f32 v91, v91\n\t"
    "v_mfma_f32_16x16x32_bf16 v[84:87], a[116:119], a[68:71], v[84:87]\n\t"
    "v_add_f32 v43, v43, v90\n\t"
    "v_mfma_f32_16x16x32_bf16 v[84:87], a[120:123], a[72:75], v[84:87]\n\t"
    "v_exp_f32 v92, v92\n\t"
    "v_mfma_f32_16x16x32_bf16 v[84:87], a[124:127], a[76:79], v[84:87]\n\t"
    "s_waitcnt vmcnt(4)\n\t"
    "s_barrier\n\t"
    "v_add_f32 v43, v43, v91\n\t"
    "v_mfma_f32_16x16x32_bf16 v[160:163], a[160:163], v[72:75], 0\n\t"
    "ds_read_b128 a[96:99], v4 offset:32768\n\t"
    "v_cvt_pk_bf16_f32 v77, v90, v91\n\t"
    "v_exp_f32 v93, v93\n\t"
    "v_mfma_f32_16x16x32_bf16 v[164:167], a[164:167], v[72:75], 0\n\t"
    "ds_read_b128 a[100:103], v5 offset:32768\n\t"
    "v_add_f32 v43, v43, v92\n\t"
    "v_mfma_f32_16x16x32_bf16 v[168:171], a[168:171], v[72:75], 0\n\t"
    "ds_read_b128 a[104:107], v6 offset:32768\n\t"
    "v_exp_f32 v94, v94\n\t"
    "v_max3_f32 v65, v80, v81, v82\n\t"
    "v_max3_f32 v65, v65, v83, v84\n\t"
    "v_max3_f32 v65, v65, v85, v86\n\t"
    "v_mfma_f32_16x16x32_bf16 v[172:175], a[172:175], v[72:75], 0\n\t"
    "ds_read_b128 a[108:111], v7 offset:32768\n\t"
    "v_add_f32 v43, v43, v93\n\t"
    "v_max_f32 v65, v65, v87\n\t"
    "v_mov_b32 v66, v65\n\t"
    "v_mfma_f32_16x16x32_bf16 v[176:179], a[176:179], v[72:75], 0\n\t"
    "ds_read_b128 a[112:115], v4 offset:36864\n\t"
    "v_cvt_pk_bf16_f32 v78, v92, v93\n\t"
    "v_exp_f32 v95, v95\n\t"
    "v_permlane16_swap_b32 v65, v66\n\t"
    "v_max_f32 v65, v65, v66\n\t"
    "v_mfma_f32_16x16x32_bf16 v[180:183], a[180:183], v[72:75], 0\n\t"
    "ds_read_b128 a[116:119], v5 offset:36864\n\t"
    "v_add_f32 v43, v43, v94\n\t"
    "v_mov_b32 v66, v65\n\t"
    "s_nop 1\n\t"
    "v_permlane32_swap_b32 v65, v66\n\t"
    "v_max_f32 v65, v65, v66\n\t"
    "v_mfma_f32_16x16x32_bf16 v[184:187], a[184:187], v[72:75], 0\n\t"
    "ds_read_b128 a[120:123], v6 offset:36864\n\t"
    "v_add_f32 v43, v43, v95\n\t"
    "v_sub_f32 v64, 0, v65\n\t"
    "v_mov_b32 v65, v64\n\t"
    "v_mfma_f32_16x16x32_bf16 v[188:191], a[188:191], v[72:75], 0\n\t"
    "ds_read_b128 a[124:127], v7 offset:36864\n\t"
    "v_cvt_pk_bf16_f32 v79, v94, v95\n\t"
    "v_mov_b32 v66, v64\n\t"
    "v_mov_b32 v67, v64\n\t"
    "s_nop 1\n\t"
    "v_mfma_f32_16x16x32_bf16 v[88:91], a[128:131], a[64:67], v[64:67]\n\t"
    "v_add_f32 v80, v64, v80\n\t"
    "v_add_f32 v81, v64, v81\n\t"
    "v_mfma_f32_16x16x32_bf16 v[88:91], a[132:135], a[68:71], v[88:91]\n\t"
    "v_add_f32 v82, v64, v82\n\t"
    "v_add_f32 v83, v64, v83\n\t"
    "v_mfma_f32_16x16x32_bf16 v[88:91], a[136:139], a[72:75], v[88:91]\n\t"
    "v_add_f32 v84, v64, v84\n\t"
    "v_add_f32 v85, v64, v85\n\t"
    "v_mfma_f32_16x16x32_bf16 v[88:91], a[140:143], a[76:79], v[88:91]\n\t"
    "v_add_f32 v86, v64, v86\n\t"
    "v_mfma_f32_16x16x32_bf16 v[92:95], a[144:147], a[64:67], v[64:67]\n\t"
    "v_add_f32 v87, v64, v87\n\t"
    "v_exp_f32 v80, v80\n\t"
    "v_mfma_f32_16x16x32_bf16 v[92:95], a[148:151], a[68:71], v[92:95]\n\t"
    "v_exp_f32 v81, v81\n\t"
    "v_mov_b32 v44, v80\n\t"
    "v_mfma_f32_16x16x32_bf16 v[92:95], a[152:155], a[72:75], v[92:95]\n\t"
    "v_exp_f32 v82, v82\n\t"
    "v_add_f32 v44, v44, v81\n\t"
    "v_mfma_f32_16x16x32_bf16 v[92:95], a[156:159], a[76:79], v[92:95]\n\t"
    "v_cvt_pk_bf16_f32 v72, v80, v81\n\t"
    "v_mfma_f32_16x16x32_bf16 v[160:163], a[192:195], v[76:79], v[160:163]\n\t"
    "v_exp_f32 v83, v83\n\t"
    "v_add_f32 v44, v44, v82\n\t"
    "v_mfma_f32_16x16x32_bf16 v[164:167], a[196:199], v[76:79], v[164:167]\n\t"
    "v_exp_f32 v84, v84\n\t"
    "v_add_f32 v44, v44, v83\n\t"
    "v_mfma_f32_16x16x32_bf16 v[168:171], a[200:203], v[76:79], v[168:171]\n\t"
    "v_cvt_pk_bf16_f32 v73, v82, v83\n\t"
    "v_exp_f32 v85, v85\n\t"
    "v_mfma_f32_16x16x32_bf16 v[172:175], a[204:207], v[76:79], v[172:175]\n\t"
    "v_add_f32 v44, v44, v84\n\t"
    "v_mfma_f32_16x16x32_bf16 v[176:179], a[208:211], v[76:79], v[176:179]\n\t"
    "v_exp_f32 v86, v86\n\t"
    "v_add_f32 v44, v44, v85\n\t"
    "v_mfma_f32_16x16x32_bf16 v[180:183], a[212:215], v[76:79], v[180:183]\n\t"
    "v_cvt_pk_bf16_f32 v74, v84, v85\n\t"
    "v_exp_f32 v87, v87\n\t"
    "v_mfma_f32_16x16x32_bf16 v[184:187], a[216:219], v[76:79], v[184:187]\n\t"
    "v_add_f32 v44, v44, v86\n\t"
    "v_add_f32 v44, v44, v87\n\t"
    "v_mfma_f32_16x16x32_bf16 v[188:191], a[220:223], v[76:79], v[188:191]\n\t"
    "s_waitcnt lgkmcnt(0)\n\t"
    "v_cvt_pk_bf16_f32 v75, v86, v87\n\t"
    "v_mfma_f32_16x16x32_bf16 v[80:83], a[96:99], a[0:3], v[48:51]\n\t"
    "s_add_u32 m0, s61, 65536\n\t"
    "v_exp_f32 v88, v88\n\t"
    "v_exp_f32 v89, v89\n\t"
    "v_mfma_f32_16x16x32_bf16 v[80:83], a[100:103], a[4:7], v[80:83]\n\t"
    "s_add_u32 s58, s62, 32768\n\t"
    "v_add_f32 v44, v44, v88\n\t"
    "v_mfma_f32_16x16x32_bf16 v[80:83], a[104:107], a[8:11], v[80:83]\n\t"
    "buffer_load_dwordx4 v16, s[44:47], s58 offen nt lds\n\t"
    "v_exp_f32 v90, v90\n\t"
    "v_mfma_f32_16x16x32_bf16 v[80:83], a[108:111], a[12:15], v[80:83]\n\t"
    "buffer_load_dwordx4 v17, s[44:47], s58 offen offset:1024 nt lds\n\t"
    "v_add_f32 v44, v44, v89\n\t"
    "v_mfma_f32_16x16x32_bf16 v[84:87], a[112:115], a[0:3], v[48:51]\n\t"
    "s_add_u32 m0, s63, 65536\n\t"
    "v_cvt_pk_bf16_f32 v76, v88, v89\n\t"
    "v_exp_f32 v91, v91\n\t"
    "v_mfma_f32_16x16x32_bf16 v[84:87], a[116:119], a[4:7], v[84:87]\n\t"
    "s_add_u32 s59, s64, 32768\n\t"
    "v_add_f32 v44, v44, v90\n\t"
    "v_mfma_f32_16x16x32_bf16 v[84:87], a[120:123], a[8:11], v[84:87]\n\t"
    "buffer_load_dwordx4 v18, s[48:51], s59 offen nt lds\n\t"
    "v_exp_f32 v92, v92\n\t"
    "v_mfma_f32_16x16x32_bf16 v[84:87], a[124:127], a[12:15], v[84:87]\n\t"
    "buffer_load_dwordx4 v18, s[48:51], s59 offen offset:2048 nt lds\n\t"
    "v_add_f32 v44, v44, v91\n\t"
    "v_mfma_f32_16x16x32_bf16 v[192:195], a[160:163], v[72:75], 0\n\t"
    "v_cvt_pk_bf16_f32 v77, v90, v91\n\t"
    "v_exp_f32 v93, v93\n\t"
    "v_mfma_f32_16x16x32_bf16 v[196:199], a[164:167], v[72:75], 0\n\t"
    "v_add_f32 v44, v44, v92\n\t"
    "v_mfma_f32_16x16x32_bf16 v[200:203], a[168:171], v[72:75], 0\n\t"
    "v_exp_f32 v94, v94\n\t"
    "v_mfma_f32_16x16x32_bf16 v[204:207], a[172:175], v[72:75], 0\n\t"
    "v_add_f32 v44, v44, v93\n\t"
    "v_mfma_f32_16x16x32_bf16 v[208:211], a[176:179], v[72:75], 0\n\t"
    "v_cvt_pk_bf16_f32 v78, v92, v93\n\t"
    "v_exp_f32 v95, v95\n\t"
    "v_mfma_f32_16x16x32_bf16 v[212:215], a[180:183], v[72:75], 0\n\t"
    "v_add_f32 v44, v44, v94\n\t"
    "v_mfma_f32_16x16x32_bf16 v[216:219], a[184:187], v[72:75], 0\n\t"
    "v_add_f32 v44, v44, v95\n\t"
    "v_mfma_f32_16x16x32_bf16 v[220:223], a[188:191], v[72:75], 0\n\t"
    "v_cvt_pk_bf16_f32 v79, v94, v95\n\t"
    "v_mfma_f32_16x16x32_bf16 v[88:91], a[96:99], a[16:19], v[52:55]\n\t"
    "ds_read_b64_tr_b16 a[160:161], v8 offset:40960\n\t"
    "v_exp_f32 v80, v80\n\t"
    "v_mfma_f32_16x16x32_bf16 v[88:91], a[100:103], a[20:23], v[88:91]\n\t"
    "ds_read_b64_tr_b16 a[162:163], v8 offset:45056\n\t"
    "v_add_f32 v40, v40, v80\n\t"
    "v_mfma_f32_16x16x32_bf16 v[88:91], a[104:107], a[24:27], v[88:91]\n\t"
    "ds_read_b64_tr_b16 a[164:165], v9 offset:40960\n\t"
    "v_exp_f32 v81, v81\n\t"
    "v_mfma_f32_16x16x32_bf16 v[88:91], a[108:111], a[28:31], v[88:91]\n\t"
    "ds_read_b64_tr_b16 a[166:167], v9 offset:45056\n\t"
    "v_add_f32 v40, v40, v81\n\t"
    "v_cvt_pk_bf16_f32 v72, v80, v81\n\t"
    "v_mfma_f32_16x16x32_bf16 v[92:95], a[112:115], a[16:19], v[52:55]\n\t"
    "ds_read_b64_tr_b16 a[168:169], v10 offset:40960\n\t"
    "v_exp_f32 v82, v82\n\t"
    "v_mfma_f32_16x16x32_bf16 v[92:95], a[116:119], a[20:23], v[92:95]\n\t"
    "ds_read_b64_tr_b16 a[170:171], v10 offset:45056\n\t"
    "v_add_f32 v40, v40, v82\n\t"
    "v_mfma_f32_16x16x32_bf16 v[92:95], a[120:123], a[24:27], v[92:95]\n\t"
    "ds_read_b64_tr_b16 a[172:173], v11 offset:40960\n\t"
    "v_exp_f32 v83, v83\n\t"
    "v_mfma_f32_16x16x32_bf16 v[92:95], a[124:127], a[28:31], v[92:95]\n\t"
    "ds_read_b64_tr_b16 a[174:175], v11 offset:45056\n\t"
    "v_add_f32 v40, v40, v83\n\t"
    "v_cvt_pk_bf16_f32 v73, v82, v83\n\t"
    "v_mfma_f32_16x16x32_bf16 v[192:195], a[192:195], v[76:79], v[192:195]\n\t"
    "ds_read_b64_tr_b16 a[176:177], v8 offset:41984\n\t"
    "v_exp_f32 v84, v84\n\t"
    "v_mfma_f32_16x16x32_bf16 v[196:199], a[196:199], v[76:79], v[196:199]\n\t"
    "ds_read_b64_tr_b16 a[178:179], v8 offset:46080\n\t"
    "v_add_f32 v40, v40, v84\n\t"
    "v_mfma_f32_16x16x32_bf16 v[200:203], a[200:203], v[76:79], v[200:203]\n\t"
    "ds_read_b64_tr_b16 a[180:181], v9 offset:41984\n\t"
    "v_exp_f32 v85, v85\n\t"
    "v_mfma_f32_16x16x32_bf16 v[204:207], a[204:207], v[76:79], v[204:207]\n\t"
    "ds_read_b64_tr_b16 a[182:183], v9 offset:46080\n\t"
    "v_add_f32 v40, v40, v85\n\t"
    "v_cvt_pk_bf16_f32 v74, v84, v85\n\t"
    "v_mfma_f32_16x16x32_bf16 v[208:211], a[208:211], v[76:79], v[208:211]\n\t"
    "ds_read_b64_tr_b16 a[184:185], v10 offset:41984\n\t"
    "v_exp_f32 v86, v86\n\t"
    "v_mfma_f32_16x16x32_bf16 v[212:215], a[212:215], v[76:79], v[212:215]\n\t"
    "ds_read_b64_tr_b16 a[186:187], v10 offset:46080\n\t"
    "v_add_f32 v40, v40, v86\n\t"
    "v_mfma_f32_16x16x32_bf16 v[216:219], a[216:219], v[76:79], v[216:219]\n\t"
    "ds_read_b64_tr_b16 a[188:189], v11 offset:41984\n\t"
    "v_exp_f32 v87, v87\n\t"
    "v_mfma_f32_16x16x32_bf16 v[220:223], a[220:223], v[76:79], v[220:223]\n\t"
    "ds_read_b64_tr_b16 a[190:191], v11 offset:46080\n\t"
    "s_waitcnt vmcnt(4)\n\t"
    "s_barrier\n\t"
    "v_add_f32 v40, v40, v87\n\t"
    "v_cvt_pk_bf16_f32 v75, v86, v87\n\t"
    "v_mfma_f32_16x16x32_bf16 v[80:83], a[96:99], a[32:35], v[56:59]\n\t"
    "s_add_u32 m0, s72, 81920\n\t"
    "v_exp_f32 v88, v88\n\t"
    "v_mfma_f32_16x16x32_bf16 v[80:83], a[100:103], a[36:39], v[80:83]\n\t"
    "ds_read_b128 a[128:131], v4 offset:49152\n\t"
    "v_add_f32 v41, v41, v88\n\t"
    "v_mfma_f32_16x16x32_bf16 v[80:83], a[104:107], a[40:43], v[80:83]\n\t"
    "s_mov_b32 s58, 40960\n\t"
    "v_exp_f32 v89, v89\n\t"
    "v_mfma_f32_16x16x32_bf16 v[80:83], a[108:111], a[44:47], v[80:83]\n\t"
    "ds_read_b128 a[132:135], v5 offset:49152\n\t"
    "v_add_f32 v41, v41, v89\n\t"
    "v_cvt_pk_bf16_f32 v76, v88, v89\n\t"
    "v_mfma_f32_16x16x32_bf16 v[84:87], a[112:115], a[32:35], v[56:59]\n\t"
    "buffer_load_dwordx4 v12, s[44:47], s58 offen nt lds\n\t"
    "v_exp_f32 v90, v90\n\t"
    "v_mfma_f32_16x16x32_bf16 v[84:87], a[116:119], a[36:39], v[84:87]\n\t"
    "ds_read_b128 a[136:139], v6 offset:49152\n\t"
    "v_add_f32 v41, v41, v90\n\t"
    "v_mfma_f32_16x16x32_bf16 v[84:87], a[120:123], a[40:43], v[84:87]\n\t"
    "buffer_load_dwordx4 v13, s[44:47], s58 offen offset:1024 nt lds\n\t"
    "v_exp_f32 v91, v91\n\t"
    "v_mfma_f32_16x16x32_bf16 v[84:87], a[124:127], a[44:47], v[84:87]\n\t"
    "s_waitcnt lgkmcnt(3)\n\t"
    "v_add_f32 v41, v41, v91\n\t"
    "v_cvt_pk_bf16_f32 v77, v90, v91\n\t"
    "v_mfma_f32_16x16x32_bf16 a[224:227], a[160:163], v[72:75], a[224:227]\n\t"
    "buffer_load_dwordx4 v14, s[44:47], s58 offen offset:2048 nt lds\n\t"
    "v_exp_f32 v92, v92\n\t"
    "v_mfma_f32_16x16x32_bf16 a[228:231], a[164:167], v[72:75], a[228:231]\n\t"
    "ds_read_b128 a[140:143], v7 offset:49152\n\t"
    "v_add_f32 v41, v41, v92\n\t"
    "v_mfma_f32_16x16x32_bf16 a[232:235], a[168:171], v[72:75], a[232:235]\n\t"
    "buffer_load_dwordx4 v15, s[44:47], s58 offen offset:3072 nt lds\n\t"
    "v_exp_f32 v93, v93\n\t"
    "v_mfma_f32_16x16x32_bf16 a[236:239], a[172:175], v[72:75], a[236:239]\n\t"
    "v_add_f32 v41, v41, v93\n\t"
    "v_cvt_pk_bf16_f32 v78, v92, v93\n\t"
    "v_mfma_f32_16x16x32_bf16 a[240:243], a[176:179], v[72:75], a[240:243]\n\t"
    "s_add_u32 m0, s72, 86016\n\t"
    "v_exp_f32 v94, v94\n\t"
    "v_mfma_f32_16x16x32_bf16 a[244:247], a[180:183], v[72:75], a[244:247]\n\t"
    "ds_read_b128 a[144:147], v4 offset:53248\n\t"
    "v_add_f32 v41, v41, v94\n\t"
    "v_mfma_f32_16x16x32_bf16 a[248:251], a[184:187], v[72:75], a[248:251]\n\t"
    "v_exp_f32 v95, v95\n\t"
    "v_mfma_f32_16x16x32_bf16 a[252:255], a[188:191], v[72:75], a[252:255]\n\t"
    "v_add_f32 v41, v41, v95\n\t"
    "v_cvt_pk_bf16_f32 v79, v94, v95\n\t"
    "v_mfma_f32_16x16x32_bf16 v[88:91], a[96:99], a[48:51], v[60:63]\n\t"
    "s_mov_b32 s58, 45056\n\t"
    "v_exp_f32 v80, v80\n\t"
    "v_mfma_f32_16x16x32_bf16 v[88:91], a[100:103], a[52:55], v[88:91]\n\t"
    "ds_read_b128 a[148:151], v5 offset:53248\n\t"
    "v_add_f32 v42, v42, v80\n\t"
    "v_mfma_f32_16x16x32_bf16 v[88:91], a[104:107], a[56:59], v[88:91]\n\t"
    "buffer_load_dwordx4 v12, s[44:47], s58 offen nt lds\n\t"
    "v_exp_f32 v81, v81\n\t"
    "v_mfma_f32_16x16x32_bf16 v[88:91], a[108:111], a[60:63], v[88:91]\n\t"
    "ds_read_b128 a[152:155], v6 offset:53248\n\t"
    "v_add_f32 v42, v42, v81\n\t"
    "v_cvt_pk_bf16_f32 v72, v80, v81\n\t"
    "v_mfma_f32_16x16x32_bf16 v[92:95], a[112:115], a[48:51], v[60:63]\n\t"
    "buffer_load_dwordx4 v13, s[44:47], s58 offen offset:1024 nt lds\n\t"
    "v_exp_f32 v82, v82\n\t"
    "v_mfma_f32_16x16x32_bf16 v[92:95], a[116:119], a[52:55], v[92:95]\n\t"
    "ds_read_b128 a[156:159], v7 offset:53248\n\t"
    "v_add_f32 v42, v42, v82\n\t"
    "v_mfma_f32_16x16x32_bf16 v[92:95], a[120:123], a[56:59], v[92:95]\n\t"
    "buffer_load_dwordx4 v14, s[44:47], s58 offen offset:2048 nt lds\n\t"
    "v_exp_f32 v83, v83\n\t"
    "v_mfma_f32_16x16x32_bf16 v[92:95], a[124:127], a[60:63], v[92:95]\n\t"
    "v_add_f32 v42, v42, v83\n\t"
    "v_cvt_pk_bf16_f32 v73, v82, v83\n\t"
    "v_mfma_f32_16x16x32_bf16 v[96:99], a[160:163], v[76:79], v[96:99]\n\t"
    "buffer_load_dwordx4 v15, s[44:47], s58 offen offset:3072 nt lds\n\t"
    "v_exp_f32 v84, v84\n\t"
    "v_mfma_f32_16x16x32_bf16 v[100:103], a[164:167], v[76:79], v[100:103]\n\t"
    "ds_read_b64_tr_b16 a[192:193], v8 offset:57344\n\t"
    "v_add_f32 v42, v42, v84\n\t"
    "v_mfma_f32_16x16x32_bf16 v[104:107], a[168:171], v[76:79], v[104:107]\n\t"
    "s_add_u32 m0, s72, 90112\n\t"
    "v_exp_f32 v85, v85\n\t"
    "v_mfma_f32_16x16x32_bf16 v[108:111], a[172:175], v[76:79], v[108:111]\n\t"
    "v_add_f32 v42, v42, v85\n\t"
    "v_cvt_pk_bf16_f32 v74, v84, v85\n\t"
    "v_mfma_f32_16x16x32_bf16 v[112:115], a[176:179], v[76:79], v[112:115]\n\t"
    "s_mov_b32 s59, 40960\n\t"
    "v_exp_f32 v86, v86\n\t"
    "v_mfma_f32_16x16x32_bf16 v[116:119], a[180:183], v[76:79], v[116:119]\n\t"
    "ds_read_b64_tr_b16 a[194:195], v8 offset:61440\n\t"
    "v_add_f32 v42, v42, v86\n\t"
    "v_mfma_f32_16x16x32_bf16 v[120:123], a[184:187], v[76:79], v[120:123]\n\t"
    "v_exp_f32 v87, v87\n\t"
    "v_mfma_f32_16x16x32_bf16 v[124:127], a[188:191], v[76:79], v[124:127]\n\t"
    "v_add_f32 v42, v42, v87\n\t"
    "v_cvt_pk_bf16_f32 v75, v86, v87\n\t"
    "v_mfma_f32_16x16x32_bf16 v[80:83], a[96:99], a[64:67], v[64:67]\n\t"
    "buffer_load_dwordx4 v18, s[48:51], s59 offen nt lds\n\t"
    "v_exp_f32 v88, v88\n\t"
    "v_mfma_f32_16x16x32_bf16 v[80:83], a[100:103], a[68:71], v[80:83]\n\t"
    "ds_read_b64_tr_b16 a[196:197], v9 offset:57344\n\t"
    "v_add_f32 v43, v43, v88\n\t"
    "v_mfma_f32_16x16x32_bf16 v[80:83], a[104:107], a[72:75], v[80:83]\n\t"
    "buffer_load_dwordx4 v18, s[48:51], s59 offen offset:2048 nt lds\n\t"
    "v_exp_f32 v89, v89\n\t"
    "v_mfma_f32_16x16x32_bf16 v[80:83], a[108:111], a[76:79], v[80:83]\n\t"
    "ds_read_b64_tr_b16 a[198:199], v9 offset:61440\n\t"
    "v_add_f32 v43, v43, v89\n\t"
    "v_cvt_pk_bf16_f32 v76, v88, v89\n\t"
    "v_mfma_f32_16x16x32_bf16 v[84:87], a[112:115], a[64:67], v[64:67]\n\t"
    "s_add_u32 m0, s72, 91136\n\t"
    "v_exp_f32 v90, v90\n\t"
    "v_mfma_f32_16x16x32_bf16 v[84:87], a[116:119], a[68:71], v[84:87]\n\t"
    "ds_read_b64_tr_b16 a[200:201], v10 offset:57344\n\t"
    "v_add_f32 v43, v43, v90\n\t"
    "v_mfma_f32_16x16x32_bf16 v[84:87], a[120:123], a[72:75], v[84:87]\n\t"
    "s_mov_b32 s59, 41088\n\t"
    "v_exp_f32 v91, v91\n\t"
    "v_mfma_f32_16x16x32_bf16 v[84:87], a[124:127], a[76:79], v[84:87]\n\t"
    "v_add_f32 v43, v43, v91\n\t"
    "v_cvt_pk_bf16_f32 v77, v90, v91\n\t"
    "v_mfma_f32_16x16x32_bf16 v[128:131], a[160:163], v[72:75], v[128:131]\n\t"
    "buffer_load_dwordx4 v18, s[48:51], s59 offen nt lds\n\t"
    "v_exp_f32 v92, v92\n\t"
    "v_mfma_f32_16x16x32_bf16 v[132:135], a[164:167], v[72:75], v[132:135]\n\t"
    "ds_read_b64_tr_b16 a[202:203], v10 offset:61440\n\t"
    "v_add_f32 v43, v43, v92\n\t"
    "v_mfma_f32_16x16x32_bf16 v[136:139], a[168:171], v[72:75], v[136:139]\n\t"
    "buffer_load_dwordx4 v18, s[48:51], s59 offen offset:2048 nt lds\n\t"
    "v_exp_f32 v93, v93\n\t"
    "v_mfma_f32_16x16x32_bf16 v[140:143], a[172:175], v[72:75], v[140:143]\n\t"
    "v_add_f32 v43, v43, v93\n\t"
    "v_cvt_pk_bf16_f32 v78, v92, v93\n\t"
    "v_mfma_f32_16x16x32_bf16 v[144:147], a[176:179], v[72:75], v[144:147]\n\t"
    "s_add_u32 m0, s72, 94208\n\t"
    "v_exp_f32 v94, v94\n\t"
    "v_mfma_f32_16x16x32_bf16 v[148:151], a[180:183], v[72:75], v[148:151]\n\t"
    "ds_read_b64_tr_b16 a[204:205], v11 offset:57344\n\t"
    "v_add_f32 v43, v43, v94\n\t"
    "v_mfma_f32_16x16x32_bf16 v[152:155], a[184:187], v[72:75], v[152:155]\n\t"
    "v_exp_f32 v95, v95\n\t"
    "v_mfma_f32_16x16x32_bf16 v[156:159], a[188:191], v[72:75], v[156:159]\n\t"
    "s_waitcnt lgkmcnt(7)\n\t"
    "v_add_f32 v43, v43, v95\n\t"
    "v_cvt_pk_bf16_f32 v79, v94, v95\n\t"
    "v_mfma_f32_16x16x32_bf16 v[88:91], a[128:131], a[0:3], v[48:51]\n\t"
    "s_mov_b32 s59, 45056\n\t"
    "v_exp_f32 v80, v80\n\t"
    "v_mfma_f32_16x16x32_bf16 v[88:91], a[132:135], a[4:7], v[88:91]\n\t"
    "ds_read_b64_tr_b16 a[206:207], v11 offset:61440\n\t"
    "v_add_f32 v44, v44, v80\n\t"
    "v_mfma_f32_16x16x32_bf16 v[88:91], a[136:139], a[8:11], v[88:91]\n\t"
    "buffer_load_dwordx4 v18, s[48:51], s59 offen nt lds\n\t"
    "v_exp_f32 v81, v81\n\t"
    "v_mfma_f32_16x16x32_bf16 v[88:91], a[140:143], a[12:15], v[88:91]\n\t"
    "ds_read_b64_tr_b16 a[208:209], v8 offset:58368\n\t"
    "v_add_f32 v44, v44, v81\n\t"
    "v_cvt_pk_bf16_f32 v72, v80, v81\n\t"
    "v_mfma_f32_16x16x32_bf16 v[92:95], a[144:147], a[0:3], v[48:51]\n\t"
    "buffer_load_dwordx4 v18, s[48:51], s59 offen offset:2048 nt lds\n\t"
    "v_exp_f32 v82, v82\n\t"
    "v_mfma_f32_16x16x32_bf16 v[92:95], a[148:151], a[4:7], v[92:95]\n\t"
    "ds_read_b64_tr_b16 a[210:211], v8 offset:62464\n\t"
    "v_add_f32 v44, v44, v82\n\t"
    "v_mfma_f32_16x16x32_bf16 v[92:95], a[152:155], a[8:11], v[92:95]\n\t"
    "s_add_u32 m0, s72, 95232\n\t"
    "v_exp_f32 v83, v83\n\t"
    "v_mfma_f32_16x16x32_bf16 v[92:95], a[156:159], a[12:15], v[92:95]\n\t"
    "v_add_f32 v44, v44, v83\n\t"
    "v_cvt_pk_bf16_f32 v73, v82, v83\n\t"
    "v_mfma_f32_16x16x32_bf16 v[160:163], a[160:163], v[76:79], v[160:163]\n\t"
    "s_mov_b32 s59, 45184\n\t"
    "v_exp_f32 v84, v84\n\t"
    "v_mfma_f32_16x16x32_bf16 v[164:167], a[164:167], v[76:79], v[164:167]\n\t"
    "ds_read_b64_tr_b16 a[212:213], v9 offset:58368\n\t"
    "v_add_f32 v44, v44, v84\n\t"
    "v_mfma_f32_16x16x32_bf16 v[168:171], a[168:171], v[76:79], v[168:171]\n\t"
    "buffer_load_dwordx4 v18, s[48:51], s59 offen nt lds\n\t"
    "v_exp_f32 v85, v85\n\t"
    "v_mfma_f32_16x16x32_bf16 v[172:175], a[172:175], v[76:79], v[172:175]\n\t"
    "v_add_f32 v44, v44, v85\n\t"
    "v_cvt_pk_bf16_f32 v74, v84, v85\n\t"
    "v_mfma_f32_16x16x32_bf16 v[176:179], a[176:179], v[76:79], v[176:179]\n\t"
    "buffer_load_dwordx4 v18, s[48:51], s59 offen offset:2048 nt lds\n\t"
    "v_exp_f32 v86, v86\n\t"
    "v_mfma_f32_16x16x32_bf16 v[180:183], a[180:183], v[76:79], v[180:183]\n\t"
    "ds_read_b64_tr_b16 a[214:215], v9 offset:62464\n\t"
    "v_add_f32 v44, v44, v86\n\t"
    "v_mfma_f32_16x16x32_bf16 v[184:187], a[184:187], v[76:79], v[184:187]\n\t"
    "v_exp_f32 v87, v87\n\t"
    "v_mfma_f32_16x16x32_bf16 v[188:191], a[188:191], v[76:79], v[188:191]\n\t"
    "v_add_f32 v44, v44, v87\n\t"
    "v_cvt_pk_bf16_f32 v75, v86, v87\n\t"
    "v_mfma_f32_16x16x32_bf16 v[80:83], a[128:131], a[16:19], v[52:55]\n\t"
    "v_exp_f32 v88, v88\n\t"
    "v_mfma_f32_16x16x32_bf16 v[80:83], a[132:135], a[20:23], v[80:83]\n\t"
    "ds_read_b64_tr_b16 a[216:217], v10 offset:58368\n\t"
    "v_add_f32 v40, v40, v88\n\t"
    "v_xor_b32 v4, 0x10000, v4\n\t"
    "v_mfma_f32_16x16x32_bf16 v[80:83], a[136:139], a[24:27], v[80:83]\n\t"
    "v_exp_f32 v89, v89\n\t"
    "v_mfma_f32_16x16x32_bf16 v[80:83], a[140:143], a[28:31], v[80:83]\n\t"
    "v_add_f32 v40, v40, v89\n\t"
    "v_cvt_pk_bf16_f32 v76, v88, v89\n\t"
    "v_mfma_f32_16x16x32_bf16 v[84:87], a[144:147], a[16:19], v[52:55]\n\t"
    "v_exp_f32 v90, v90\n\t"
    "v_mfma_f32_16x16x32_bf16 v[84:87], a[148:151], a[20:23], v[84:87]\n\t"
    "ds_read_b64_tr_b16 a[218:219], v10 offset:62464\n\t"
    "v_add_f32 v40, v40, v90\n\t"
    "v_xor_b32 v5, 0x10000, v5\n\t"
    "v_mfma_f32_16x16x32_bf16 v[84:87], a[152:155], a[24:27], v[84:87]\n\t"
    "v_exp_f32 v91, v91\n\t"
    "v_mfma_f32_16x16x32_bf16 v[84:87], a[156:159], a[28:31], v[84:87]\n\t"
    "v_add_f32 v40, v40, v91\n\t"
    "v_cvt_pk_bf16_f32 v77, v90, v91\n\t"
    "v_mfma_f32_16x16x32_bf16 v[192:195], a[160:163], v[72:75], v[192:195]\n\t"
    "v_exp_f32 v92, v92\n\t"
    "v_mfma_f32_16x16x32_bf16 v[196:199], a[164:167], v[72:75], v[196:199]\n\t"
    "ds_read_b64_tr_b16 a[220:221], v11 offset:58368\n\t"
    "v_add_f32 v40, v40, v92\n\t"
    "v_xor_b32 v6, 0x10000, v6\n\t"
    "v_mfma_f32_16x16x32_bf16 v[200:203], a[168:171], v[72:75], v[200:203]\n\t"
    "v_exp_f32 v93, v93\n\t"
    "v_mfma_f32_16x16x32_bf16 v[204:207], a[172:175], v[72:75], v[204:207]\n\t"
    "v_add_f32 v40, v40, v93\n\t"
    "v_cvt_pk_bf16_f32 v78, v92, v93\n\t"
    "v_mfma_f32_16x16x32_bf16 v[208:211], a[176:179], v[72:75], v[208:211]\n\t"
    "v_exp_f32 v94, v94\n\t"
    "v_mfma_f32_16x16x32_bf16 v[212:215], a[180:183], v[72:75], v[212:215]\n\t"
    "ds_read_b64_tr_b16 a[222:223], v11 offset:62464\n\t"
    "v_add_f32 v40, v40, v94\n\t"
    "v_xor_b32 v7, 0x10000, v7\n\t"
    "v_mfma_f32_16x16x32_bf16 v[216:219], a[184:187], v[72:75], v[216:219]\n\t"
    "v_exp_f32 v95, v95\n\t"
    "v_mfma_f32_16x16x32_bf16 v[220:223], a[188:191], v[72:75], v[220:223]\n\t"
    "s_waitcnt vmcnt(16)\n\t"
    "s_barrier\n\t"
    "v_add_f32 v40, v40, v95\n\t"
    "v_cvt_pk_bf16_f32 v79, v94, v95\n\t"
    "v_mfma_f32_16x16x32_bf16 v[88:91], a[128:131], a[32:35], v[56:59]\n\t"
    "s_add_u32 m0, s72, 98304\n\t"
    "v_exp_f32 v80, v80\n\t"
    "v_mfma_f32_16x16x32_bf16 v[88:91], a[132:135], a[36:39], v[88:91]\n\t"
    "ds_read_b128 a[96:99], v4 offset:0\n\t"
    "v_add_f32 v41, v41, v80\n\t"
    "v_xor_b32 v8, 0x10000, v8\n\t"
    "v_mfma_f32_16x16x32_bf16 v[88:91], a[136:139], a[40:43], v[88:91]\n\t"
    "s_mov_b32 s58, 49152\n\t"
    "v_exp_f32 v81, v81\n\t"
    "v_mfma_f32_16x16x32_bf16 v[88:91], a[140:143], a[44:47], v[88:91]\n\t"
    "ds_read_b128 a[100:103], v5 offset:0\n\t"
    "v_add_f32 v41, v41, v81\n\t"
    "v_cvt_pk_bf16_f32 v72, v80, v81\n\t"
    "v_mfma_f32_16x16x32_bf16 v[92:95], a[144:147], a[32:35], v[56:59]\n\t"
    "buffer_load_dwordx4 v12, s[44:47], s58 offen nt lds\n\t"
    "v_exp_f32 v82, v82\n\t"
    "v_mfma_f32_16x16x32_bf16 v[92:95], a[148:151], a[36:39], v[92:95]\n\t"
    "ds_read_b128 a[104:107], v6 offset:0\n\t"
    "v_add_f32 v41, v41, v82\n\t"
    "v_xor_b32 v9, 0x10000, v9\n\t"
    "v_mfma_f32_16x16x32_bf16 v[92:95], a[152:155], a[40:43], v[92:95]\n\t"
    "buffer_load_dwordx4 v13, s[44:47], s58 offen offset:1024 nt lds\n\t"
    "v_exp_f32 v83, v83\n\t"
    "v_mfma_f32_16x16x32_bf16 v[92:95], a[156:159], a[44:47], v[92:95]\n\t"
    "s_waitcnt lgkmcnt(3)\n\t"
    "v_add_f32 v41, v41, v83\n\t"
    "v_cvt_pk_bf16_f32 v73, v82, v83\n\t"
    "v_mfma_f32_16x16x32_bf16 a[224:227], a[192:195], v[76:79], a[224:227]\n\t"
    "buffer_load_dwordx4 v14, s[44:47], s58 offen offset:2048 nt lds\n\t"
    "v_exp_f32 v84, v84\n\t"
    "v_mfma_f32_16x16x32_bf16 a[228:231], a[196:199], v[76:79], a[228:231]\n\t"
    "ds_read_b128 a[108:111], v7 offset:0\n\t"
    "v_add_f32 v41, v41, v84\n\t"
    "v_xor_b32 v10, 0x10000, v10\n\t"
    "v_mfma_f32_16x16x32_bf16 a[232:235], a[200:203], v[76:79], a[232:235]\n\t"
    "buffer_load_dwordx4 v15, s[44:47], s58 offen offset:3072 nt lds\n\t"
    "v_exp_f32 v85, v85\n\t"
    "v_mfma_f32_16x16x32_bf16 a[236:239], a[204:207], v[76:79], a[236:239]\n\t"
    "v_add_f32 v41, v41, v85\n\t"
    "v_cvt_pk_bf16_f32 v74, v84, v85\n\t"
    "v_mfma_f32_16x16x32_bf16 a[240:243], a[208:211], v[76:79], a[240:243]\n\t"
    "s_add_u32 m0, s72, 102400\n\t"
    "v_exp_f32 v86, v86\n\t"
    "v_mfma_f32_16x16x32_bf16 a[244:247], a[212:215], v[76:79], a[244:247]\n\t"
    "ds_read_b128 a[112:115], v4 offset:4096\n\t"
    "v_add_f32 v41, v41, v86\n\t"
    "v_xor_b32 v11, 0x10000, v11\n\t"
    "v_mfma_f32_16x16x32_bf16 a[248:251], a[216:219], v[76:79], a[248:251]\n\t"
    "v_exp_f32 v87, v87\n\t"
    "v_mfma_f32_16x16x32_bf16 a[252:255], a[220:223], v[76:79], a[252:255]\n\t"
    "v_add_f32 v41, v41, v87\n\t"
    "v_cvt_pk_bf16_f32 v75, v86, v87\n\t"
    "v_mfma_f32_16x16x32_bf16 v[80:83], a[128:131], a[48:51], v[60:63]\n\t"
    "s_mov_b32 s58, 53248\n\t"
    "v_exp_f32 v88, v88\n\t"
    "v_mfma_f32_16x16x32_bf16 v[80:83], a[132:135], a[52:55], v[80:83]\n\t"
    "ds_read_b128 a[116:119], v5 offset:4096\n\t"
    "v_add_f32 v42, v42, v88\n\t"
    "v_mfma_f32_16x16x32_bf16 v[80:83], a[136:139], a[56:59], v[80:83]\n\t"
    "buffer_load_dwordx4 v12, s[44:47], s58 offen nt lds\n\t"
    "v_exp_f32 v89, v89\n\t"
    "v_mfma_f32_16x16x32_bf16 v[80:83], a[140:143], a[60:63], v[80:83]\n\t"
    "ds_read_b128 a[120:123], v6 offset:4096\n\t"
    "v_add_f32 v42, v42, v89\n\t"
    "v_cvt_pk_bf16_f32 v76, v88, v89\n\t"
    "v_mfma_f32_16x16x32_bf16 v[84:87], a[144:147], a[48:51], v[60:63]\n\t"
    "buffer_load_dwordx4 v13, s[44:47], s58 offen offset:1024 nt lds\n\t"
    "v_exp_f32 v90, v90\n\t"
    "v_mfma_f32_16x16x32_bf16 v[84:87], a[148:151], a[52:55], v[84:87]\n\t"
    "ds_read_b128 a[124:127], v7 offset:4096\n\t"
    "v_add_f32 v42, v42, v90\n\t"
    "v_mfma_f32_16x16x32_bf16 v[84:87], a[152:155], a[56:59], v[84:87]\n\t"
    "buffer_load_dwordx4 v14, s[44:47], s58 offen offset:2048 nt lds\n\t"
    "v_exp_f32 v91, v91\n\t"
    "v_mfma_f32_16x16x32_bf16 v[84:87], a[156:159], a[60:63], v[84:87]\n\t"
    "v_add_f32 v42, v42, v91\n\t"
    "v_cvt_pk_bf16_f32 v77, v90, v91\n\t"
    "v_mfma_f32_16x16x32_bf16 v[96:99], a[192:195], v[72:75], v[96:99]\n\t"
    "buffer_load_dwordx4 v15, s[44:47], s58 offen offset:3072 nt lds\n\t"
    "v_exp_f32 v92, v92\n\t"
    "v_mfma_f32_16x16x32_bf16 v[100:103], a[196:199], v[72:75], v[100:103]\n\t"
    "ds_read_b64_tr_b16 a[160:161], v8 offset:8192\n\t"
    "v_add_f32 v42, v42, v92\n\t"
    "v_mfma_f32_16x16x32_bf16 v[104:107], a[200:203], v[72:75], v[104:107]\n\t"
    "s_add_u32 m0, s72, 106496\n\t"
    "v_exp_f32 v93, v93\n\t"
    "v_mfma_f32_16x16x32_bf16 v[108:111], a[204:207], v[72:75], v[108:111]\n\t"
    "v_add_f32 v42, v42, v93\n\t"
    "v_cvt_pk_bf16_f32 v78, v92, v93\n\t"
    "v_mfma_f32_16x16x32_bf16 v[112:115], a[208:211], v[72:75], v[112:115]\n\t"
    "s_mov_b32 s59, 49152\n\t"
    "v_exp_f32 v94, v94\n\t"
    "v_mfma_f32_16x16x32_bf16 v[116:119], a[212:215], v[72:75], v[116:119]\n\t"
    "ds_read_b64_tr_b16 a[162:163], v8 offset:12288\n\t"
    "v_add_f32 v42, v42, v94\n\t"
    "v_mfma_f32_16x16x32_bf16 v[120:123], a[216:219], v[72:75], v[120:123]\n\t"
    "v_exp_f32 v95, v95\n\t"
    "v_mfma_f32_16x16x32_bf16 v[124:127], a[220:223], v[72:75], v[124:127]\n\t"
    "v_add_f32 v42, v42, v95\n\t"
    "v_cvt_pk_bf16_f32 v79, v94, v95\n\t"
    "v_mfma_f32_16x16x32_bf16 v[88:91], a[128:131], a[64:67], v[64:67]\n\t"
    "buffer_load_dwordx4 v18, s[48:51], s59 offen nt lds\n\t"
    "v_exp_f32 v80, v80\n\t"
    "v_mfma_f32_16x16x32_bf16 v[88:91], a[132:135], a[68:71], v[88:91]\n\t"
    "ds_read_b64_tr_b16 a[164:165], v9 offset:8192\n\t"
    "v_add_f32 v43, v43, v80\n\t"
    "v_mfma_f32_16x16x32_bf16 v[88:91], a[136:139], a[72:75], v[88:91]\n\t"
    "buffer_load_dwordx4 v18, s[48:51], s59 offen offset:2048 nt lds\n\t"
    "v_exp_f32 v81, v81\n\t"
    "v_mfma_f32_16x16x32_bf16 v[88:91], a[140:143], a[76:79], v[88:91]\n\t"
    "ds_read_b64_tr_b16 a[166:167], v9 offset:12288\n\t"
    "v_add_f32 v43, v43, v81\n\t"
    "v_cvt_pk_bf16_f32 v72, v80, v81\n\t"
    "v_mfma_f32_16x16x32_bf16 v[92:95], a[144:147], a[64:67], v[64:67]\n\t"
    "s_add_u32 m0, s72, 107520\n\t"
    "v_exp_f32 v82, v82\n\t"
    "v_mfma_f32_16x16x32_bf16 v[92:95], a[148:151], a[68:71], v[92:95]\n\t"
    "ds_read_b64_tr_b16 a[168:169], v10 offset:8192\n\t"
    "v_add_f32 v43, v43, v82\n\t"
    "v_mfma_f32_16x16x32_bf16 v[92:95], a[152:155], a[72:75], v[92:95]\n\t"
    "s_mov_b32 s59, 49280\n\t"
    "v_exp_f32 v83, v83\n\t"
    "v_mfma_f32_16x16x32_bf16 v[92:95], a[156:159], a[76:79], v[92:95]\n\t"
    "v_add_f32 v43, v43, v83\n\t"
    "v_cvt_pk_bf16_f32 v73, v82, v83\n\t"
    "v_mfma_f32_16x16x32_bf16 v[128:131], a[192:195], v[76:79], v[128:131]\n\t"
    "buffer_load_dwordx4 v18, s[48:51], s59 offen nt lds\n\t"
    "v_exp_f32 v84, v84\n\t"
    "v_mfma_f32_16x16x32_bf16 v[132:135], a[196:199], v[76:79], v[132:135]\n\t"
    "ds_read_b64_tr_b16 a[170:171], v10 offset:12288\n\t"
    "v_add_f32 v43, v43, v84\n\t"
    "v_mfma_f32_16x16x32_bf16 v[136:139], a[200:203], v[76:79], v[136:139]\n\t"
    "buffer_load_dwordx4 v18, s[48:51], s59 offen offset:2048 nt lds\n\t"
    "v_exp_f32 v85, v85\n\t"
    "v_mfma_f32_16x16x32_bf16 v[140:143], a[204:207], v[76:79], v[140:143]\n\t"
    "v_add_f32 v43, v43, v85\n\t"
    "v_cvt_pk_bf16_f32 v74, v84, v85\n\t"
    "v_mfma_f32_16x16x32_bf16 v[144:147], a[208:211], v[76:79], v[144:147]\n\t"
    "s_add_u32 m0, s72, 110592\n\t"
    "v_exp_f32 v86, v86\n\t"
    "v_mfma_f32_16x16x32_bf16 v[148:151], a[212:215], v[76:79], v[148:151]\n\t"
    "ds_read_b64_tr_b16 a[172:173], v11 offset:8192\n\t"
    "v_add_f32 v43, v43, v86\n\t"
    "v_mfma_f32_16x16x32_bf16 v[152:155], a[216:219], v[76:79], v[152:155]\n\t"
    "v_exp_f32 v87, v87\n\t"
    "v_mfma_f32_16x16x32_bf16 v[156:159], a[220:223], v[76:79], v[156:159]\n\t"
    "s_waitcnt lgkmcnt(7)\n\t"
    "v_add_f32 v43, v43, v87\n\t"
    "v_cvt_pk_bf16_f32 v75, v86, v87\n\t"
    "v_mfma_f32_16x16x32_bf16 v[80:83], a[96:99], a[0:3], v[48:51]\n\t"
    "s_mov_b32 s59, 53248\n\t"
    "v_exp_f32 v88, v88\n\t"
    "v_mfma_f32_16x16x32_bf16 v[80:83], a[100:103], a[4:7], v[80:83]\n\t"
    "ds_read_b64_tr_b16 a[174:175], v11 offset:12288\n\t"
    "v_add_f32 v44, v44, v88\n\t"
    "v_mfma_f32_16x16x32_bf16 v[80:83], a[104:107], a[8:11], v[80:83]\n\t"
    "buffer_load_dwordx4 v18, s[48:51], s59 offen nt lds\n\t"
    "v_exp_f32 v89, v89\n\t"
    "v_mfma_f32_16x16x32_bf16 v[80:83], a[108:111], a[12:15], v[80:83]\n\t"
    "ds_read_b64_tr_b16 a[176:177], v8 offset:9216\n\t"
    "v_add_f32 v44, v44, v89\n\t"
    "v_cvt_pk_bf16_f32 v76, v88, v89\n\t"
    "v_mfma_f32_16x16x32_bf16 v[84:87], a[112:115], a[0:3], v[48:51]\n\t"
    "buffer_load_dwordx4 v18, s[48:51], s59 offen offset:2048 nt lds\n\t"
    "v_exp_f32 v90, v90\n\t"
    "v_mfma_f32_16x16x32_bf16 v[84:87], a[116:119], a[4:7], v[84:87]\n\t"
    "ds_read_b64_tr_b16 a[178:179], v8 offset:13312\n\t"
    "v_add_f32 v44, v44, v90\n\t"
    "v_mfma_f32_16x16x32_bf16 v[84:87], a[120:123], a[8:11], v[84:87]\n\t"
    "s_add_u32 m0, s72, 111616\n\t"
    "v_exp_f32 v91, v91\n\t"
    "v_mfma_f32_16x16x32_bf16 v[84:87], a[124:127], a[12:15], v[84:87]\n\t"
    "v_add_f32 v44, v44, v91\n\t"
    "v_cvt_pk_bf16_f32 v77, v90, v91\n\t"
    "v_mfma_f32_16x16x32_bf16 v[160:163], a[192:195], v[72:75], v[160:163]\n\t"
    "s_mov_b32 s59, 53376\n\t"
    "v_exp_f32 v92, v92\n\t"
    "v_mfma_f32_16x16x32_bf16 v[164:167], a[196:199], v[72:75], v[164:167]\n\t"
    "ds_read_b64_tr_b16 a[180:181], v9 offset:9216\n\t"
    "v_add_f32 v44, v44, v92\n\t"
    "v_mfma_f32_16x16x32_bf16 v[168:171], a[200:203], v[72:75], v[168:171]\n\t"
    "buffer_load_dwordx4 v18, s[48:51], s59 offen nt lds\n\t"
    "v_exp_f32 v93, v93\n\t"
    "v_mfma_f32_16x16x32_bf16 v[172:175], a[204:207], v[72:75], v[172:175]\n\t"
    "v_add_f32 v44, v44, v93\n\t"
    "v_cvt_pk_bf16_f32 v78, v92, v93\n\t"
    "v_mfma_f32_16x16x32_bf16 v[176:179], a[208:211], v[72:75], v[176:179]\n\t"
    "buffer_load_dwordx4 v18, s[48:51], s59 offen offset:2048 nt lds\n\t"
    "v_exp_f32 v94, v94\n\t"
    "v_mfma_f32_16x16x32_bf16 v[180:183], a[212:215], v[72:75], v[180:183]\n\t"
    "ds_read_b64_tr_b16 a[182:183], v9 offset:13312\n\t"
    "v_add_f32 v44, v44, v94\n\t"
    "v_mfma_f32_16x16x32_bf16 v[184:187], a[216:219], v[72:75], v[184:187]\n\t"
    "v_exp_f32 v95, v95\n\t"
    "v_mfma_f32_16x16x32_bf16 v[188:191], a[220:223], v[72:75], v[188:191]\n\t"
    "v_add_f32 v44, v44, v95\n\t"
    "v_cvt_pk_bf16_f32 v79, v94, v95\n\t"
    "v_mfma_f32_16x16x32_bf16 v[88:91], a[96:99], a[16:19], v[52:55]\n\t"
    "v_exp_f32 v80, v80\n\t"
    "v_mfma_f32_16x16x32_bf16 v[88:91], a[100:103], a[20:23], v[88:91]\n\t"
    "ds_read_b64_tr_b16 a[184:185], v10 offset:9216\n\t"
    "v_add_f32 v40, v40, v80\n\t"
    "v_mfma_f32_16x16x32_bf16 v[88:91], a[104:107], a[24:27], v[88:91]\n\t"
    "v_exp_f32 v81, v81\n\t"
    "v_mfma_f32_16x16x32_bf16 v[88:91], a[108:111], a[28:31], v[88:91]\n\t"
    "v_add_f32 v40, v40, v81\n\t"
    "v_cvt_pk_bf16_f32 v72, v80, v81\n\t"
    "v_mfma_f32_16x16x32_bf16 v[92:95], a[112:115], a[16:19], v[52:55]\n\t"
    "v_exp_f32 v82, v82\n\t"
    "v_mfma_f32_16x16x32_bf16 v[92:95], a[116:119], a[20:23], v[92:95]\n\t"
    "ds_read_b64_tr_b16 a[186:187], v10 offset:13312\n\t"
    "v_add_f32 v40, v40, v82\n\t"
    "v_mfma_f32_16x16x32_bf16 v[92:95], a[120:123], a[24:27], v[92:95]\n\t"
    "v_exp_f32 v83, v83\n\t"
    "v_mfma_f32_16x16x32_bf16 v[92:95], a[124:127], a[28:31], v[92:95]\n\t"
    "v_add_f32 v40, v40, v83\n\t"
    "v_cvt_pk_bf16_f32 v73, v82, v83\n\t"
    "v_mfma_f32_16x16x32_bf16 v[192:195], a[192:195], v[76:79], v[192:195]\n\t"
    "v_exp_f32 v84, v84\n\t"
    "v_mfma_f32_16x16x32_bf16 v[196:199], a[196:199], v[76:79], v[196:199]\n\t"
    "ds_read_b64_tr_b16 a[188:189], v11 offset:9216\n\t"
    "v_add_f32 v40, v40, v84\n\t"
    "v_mfma_f32_16x16x32_bf16 v[200:203], a[200:203], v[76:79], v[200:203]\n\t"
    "v_exp_f32 v85, v85\n\t"
    "v_mfma_f32_16x16x32_bf16 v[204:207], a[204:207], v[76:79], v[204:207]\n\t"
    "v_add_f32 v40, v40, v85\n\t"
    "v_cvt_pk_bf16_f32 v74, v84, v85\n\t"
    "v_mfma_f32_16x16x32_bf16 v[208:211], a[208:211], v[76:79], v[208:211]\n\t"
    "v_exp_f32 v86, v86\n\t"
    "v_mfma_f32_16x16x32_bf16 v[212:215], a[212:215], v[76:79], v[212:215]\n\t"
    "ds_read_b64_tr_b16 a[190:191], v11 offset:13312\n\t"
    "v_add_f32 v40, v40, v86\n\t"
    "v_mfma_f32_16x16x32_bf16 v[216:219], a[216:219], v[76:79], v[216:219]\n\t"
    "v_exp_f32 v87, v87\n\t"
    "v_mfma_f32_16x16x32_bf16 v[220:223], a[220:223], v[76:79], v[220:223]\n\t"
    "s_waitcnt vmcnt(16)\n\t"
    "s_barrier\n\t"
    "v_add_f32 v40, v40, v87\n\t"
    "v_cvt_pk_bf16_f32 v75, v86, v87\n\t"
    "v_mfma_f32_16x16x32_bf16 v[80:83], a[96:99], a[32:35], v[56:59]\n\t"
    "s_add_u32 m0, s72, 114688\n\t"
    "v_exp_f32 v88, v88\n\t"
    "v_mfma_f32_16x16x32_bf16 v[80:83], a[100:103], a[36:39], v[80:83]\n\t"
    "ds_read_b128 a[128:131], v4 offset:16384\n\t"
    "v_add_f32 v41, v41, v88\n\t"
    "v_mfma_f32_16x16x32_bf16 v[80:83], a[104:107], a[40:43], v[80:83]\n\t"
    "s_mov_b32 s58, 57344\n\t"
    "v_exp_f32 v89, v89\n\t"
    "v_mfma_f32_16x16x32_bf16 v[80:83], a[108:111], a[44:47], v[80:83]\n\t"
    "ds_read_b128 a[132:135], v5 offset:16384\n\t"
    "v_add_f32 v41, v41, v89\n\t"
    "v_cvt_pk_bf16_f32 v76, v88, v89\n\t"
    "v_mfma_f32_16x16x32_bf16 v[84:87], a[112:115], a[32:35], v[56:59]\n\t"
    "buffer_load_dwordx4 v12, s[44:47], s58 offen nt lds\n\t"
    "v_exp_f32 v90, v90\n\t"
    "v_mfma_f32_16x16x32_bf16 v[84:87], a[116:119], a[36:39], v[84:87]\n\t"
    "ds_read_b128 a[136:139], v6 offset:16384\n\t"
    "v_add_f32 v41, v41, v90\n\t"
    "v_mfma_f32_16x16x32_bf16 v[84:87], a[120:123], a[40:43], v[84:87]\n\t"
    "buffer_load_dwordx4 v13, s[44:47], s58 offen offset:1024 nt lds\n\t"
    "v_exp_f32 v91, v91\n\t"
    "v_mfma_f32_16x16x32_bf16 v[84:87], a[124:127], a[44:47], v[84:87]\n\t"
    "s_waitcnt lgkmcnt(3)\n\t"
    "v_add_f32 v41, v41, v91\n\t"
    "v_cvt_pk_bf16_f32 v77, v90, v91\n\t"
    "v_mfma_f32_16x16x32_bf16 a[224:227], a[160:163], v[72:75], a[224:227]\n\t"
    "buffer_load_dwordx4 v14, s[44:47], s58 offen offset:2048 nt lds\n\t"
    "v_exp_f32 v92, v92\n\t"
    "v_mfma_f32_16x16x32_bf16 a[228:231], a[164:167], v[72:75], a[228:231]\n\t"
    "ds_read_b128 a[140:143], v7 offset:16384\n\t"
    "v_add_f32 v41, v41, v92\n\t"
    "v_mfma_f32_16x16x32_bf16 a[232:235], a[168:171], v[72:75], a[232:235]\n\t"
    "buffer_load_dwordx4 v15, s[44:47], s58 offen offset:3072 nt lds\n\t"
    "v_exp_f32 v93, v93\n\t"
    "v_mfma_f32_16x16x32_bf16 a[236:239], a[172:175], v[72:75], a[236:239]\n\t"
    "v_add_f32 v41, v41, v93\n\t"
    "v_cvt_pk_bf16_f32 v78, v92, v93\n\t"
    "v_mfma_f32_16x16x32_bf16 a[240:243], a[176:179], v[72:75], a[240:243]\n\t"
    "s_add_u32 m0, s72, 118784\n\t"
    "v_exp_f32 v94, v94\n\t"
    "v_mfma_f32_16x16x32_bf16 a[244:247], a[180:183], v[72:75], a[244:247]\n\t"
    "ds_read_b128 a[144:147], v4 offset:20480\n\t"
    "v_add_f32 v41, v41, v94\n\t"
    "v_mfma_f32_16x16x32_bf16 a[248:251], a[184:187], v[72:75], a[248:251]\n\t"
    "v_exp_f32 v95, v95\n\t"
    "v_mfma_f32_16x16x32_bf16 a[252:255], a[188:191], v[72:75], a[252:255]\n\t"
    "v_add_f32 v41, v41, v95\n\t"
    "v_cvt_pk_bf16_f32 v79, v94, v95\n\t"
    "v_mfma_f32_16x16x32_bf16 v[88:91], a[96:99], a[48:51], v[60:63]\n\t"
    "s_mov_b32 s58, 61440\n\t"
    "v_exp_f32 v80, v80\n\t"
    "v_mfma_f32_16x16x32_bf16 v[88:91], a[100:103], a[52:55], v[88:91]\n\t"
    "ds_read_b128 a[148:151], v5 offset:20480\n\t"
    "v_add_f32 v42, v42, v80\n\t"
    "v_mfma_f32_16x16x32_bf16 v[88:91], a[104:107], a[56:59], v[88:91]\n\t"
    "buffer_load_dwordx4 v12, s[44:47], s58 offen nt lds\n\t"
    "v_exp_f32 v81, v81\n\t"
    "v_mfma_f32_16x16x32_bf16 v[88:91], a[108:111], a[60:63], v[88:91]\n\t"
    "ds_read_b128 a[152:155], v6 offset:20480\n\t"
    "v_add_f32 v42, v42, v81\n\t"
    "v_cvt_pk_bf16_f32 v72, v80, v81\n\t"
    "v_mfma_f32_16x16x32_bf16 v[92:95], a[112:115], a[48:51], v[60:63]\n\t"
    "buffer_load_dwordx4 v13, s[44:47], s58 offen offset:1024 nt lds\n\t"
    "v_exp_f32 v82, v82\n\t"
    "v_mfma_f32_16x16x32_bf16 v[92:95], a[116:119], a[52:55], v[92:95]\n\t"
    "ds_read_b128 a[156:159], v7 offset:20480\n\t"
    "v_add_f32 v42, v42, v82\n\t"
    "v_mfma_f32_16x16x32_bf16 v[92:95], a[120:123], a[56:59], v[92:95]\n\t"
    "buffer_load_dwordx4 v14, s[44:47], s58 offen offset:2048 nt lds\n\t"
    "v_exp_f32 v83, v83\n\t"
    "v_mfma_f32_16x16x32_bf16 v[92:95], a[124:127], a[60:63], v[92:95]\n\t"
    "v_add_f32 v42, v42, v83\n\t"
    "v_cvt_pk_bf16_f32 v73, v82, v83\n\t"
    "v_mfma_f32_16x16x32_bf16 v[96:99], a[160:163], v[76:79], v[96:99]\n\t"
    "buffer_load_dwordx4 v15, s[44:47], s58 offen offset:3072 nt lds\n\t"
    "v_exp_f32 v84, v84\n\t"
    "v_mfma_f32_16x16x32_bf16 v[100:103], a[164:167], v[76:79], v[100:103]\n\t"
    "ds_read_b64_tr_b16 a[192:193], v8 offset:24576\n\t"
    "v_add_f32 v42, v42, v84\n\t"
    "v_mfma_f32_16x16x32_bf16 v[104:107], a[168:171], v[76:79], v[104:107]\n\t"
    "s_add_u32 m0, s72, 122880\n\t"
    "v_exp_f32 v85, v85\n\t"
    "v_mfma_f32_16x16x32_bf16 v[108:111], a[172:175], v[76:79], v[108:111]\n\t"
    "v_add_f32 v42, v42, v85\n\t"
    "v_cvt_pk_bf16_f32 v74, v84, v85\n\t"
    "v_mfma_f32_16x16x32_bf16 v[112:115], a[176:179], v[76:79], v[112:115]\n\t"
    "s_mov_b32 s59, 57344\n\t"
    "v_exp_f32 v86, v86\n\t"
    "v_mfma_f32_16x16x32_bf16 v[116:119], a[180:183], v[76:79], v[116:119]\n\t"
    "ds_read_b64_tr_b16 a[194:195], v8 offset:28672\n\t"
    "v_add_f32 v42, v42, v86\n\t"
    "v_mfma_f32_16x16x32_bf16 v[120:123], a[184:187], v[76:79], v[120:123]\n\t"
    "v_exp_f32 v87, v87\n\t"
    "v_mfma_f32_16x16x32_bf16 v[124:127], a[188:191], v[76:79], v[124:127]\n\t"
    "v_add_f32 v42, v42, v87\n\t"
    "v_cvt_pk_bf16_f32 v75, v86, v87\n\t"
    "v_mfma_f32_16x16x32_bf16 v[80:83], a[96:99], a[64:67], v[64:67]\n\t"
    "buffer_load_dwordx4 v18, s[48:51], s59 offen nt lds\n\t"
    "v_exp_f32 v88, v88\n\t"
    "v_mfma_f32_16x16x32_bf16 v[80:83], a[100:103], a[68:71], v[80:83]\n\t"
    "ds_read_b64_tr_b16 a[196:197], v9 offset:24576\n\t"
    "v_add_f32 v43, v43, v88\n\t"
    "v_mfma_f32_16x16x32_bf16 v[80:83], a[104:107], a[72:75], v[80:83]\n\t"
    "buffer_load_dwordx4 v18, s[48:51], s59 offen offset:2048 nt lds\n\t"
    "v_exp_f32 v89, v89\n\t"
    "v_mfma_f32_16x16x32_bf16 v[80:83], a[108:111], a[76:79], v[80:83]\n\t"
    "ds_read_b64_tr_b16 a[198:199], v9 offset:28672\n\t"
    "v_add_f32 v43, v43, v89\n\t"
    "v_cvt_pk_bf16_f32 v76, v88, v89\n\t"
    "v_mfma_f32_16x16x32_bf16 v[84:87], a[112:115], a[64:67], v[64:67]\n\t"
    "s_add_u32 m0, s72, 123904\n\t"
    "v_exp_f32 v90, v90\n\t"
    "v_mfma_f32_16x16x32_bf16 v[84:87], a[116:119], a[68:71], v[84:87]\n\t"
    "ds_read_b64_tr_b16 a[200:201], v10 offset:24576\n\t"
    "v_add_f32 v43, v43, v90\n\t"
    "v_mfma_f32_16x16x32_bf16 v[84:87], a[120:123], a[72:75], v[84:87]\n\t"
    "s_mov_b32 s59, 57472\n\t"
    "v_exp_f32 v91, v91\n\t"
    "v_mfma_f32_16x16x32_bf16 v[84:87], a[124:127], a[76:79], v[84:87]\n\t"
    "v_add_f32 v43, v43, v91\n\t"
    "v_cvt_pk_bf16_f32 v77, v90, v91\n\t"
    "v_mfma_f32_16x16x32_bf16 v[128:131], a[160:163], v[72:75], v[128:131]\n\t"
    "buffer_load_dwordx4 v18, s[48:51], s59 offen nt lds\n\t"
    "v_exp_f32 v92, v92\n\t"
    "v_mfma_f32_16x16x32_bf16 v[132:135], a[164:167], v[72:75], v[132:135]\n\t"
    "ds_read_b64_tr_b16 a[202:203], v10 offset:28672\n\t"
    "v_add_f32 v43, v43, v92\n\t"
    "v_mfma_f32_16x16x32_bf16 v[136:139], a[168:171], v[72:75], v[136:139]\n\t"
    "buffer_load_dwordx4 v18, s[48:51], s59 offen offset:2048 nt lds\n\t"
    "v_exp_f32 v93, v93\n\t"
    "v_mfma_f32_16x16x32_bf16 v[140:143], a[172:175], v[72:75], v[140:143]\n\t"
    "v_add_f32 v43, v43, v93\n\t"
    "v_cvt_pk_bf16_f32 v78, v92, v93\n\t"
    "v_mfma_f32_16x16x32_bf16 v[144:147], a[176:179], v[72:75], v[144:147]\n\t"
    "s_add_u32 m0, s72, 126976\n\t"
    "v_exp_f32 v94, v94\n\t"
    "v_mfma_f32_16x16x32_bf16 v[148:151], a[180:183], v[72:75], v[148:151]\n\t"
    "ds_read_b64_tr_b16 a[204:205], v11 offset:24576\n\t"
    "v_add_f32 v43, v43, v94\n\t"
    "v_mfma_f32_16x16x32_bf16 v[152:155], a[184:187], v[72:75], v[152:155]\n\t"
    "v_exp_f32 v95, v95\n\t"
    "v_mfma_f32_16x16x32_bf16 v[156:159], a[188:191], v[72:75], v[156:159]\n\t"
    "s_waitcnt lgkmcnt(7)\n\t"
    "v_add_f32 v43, v43, v95\n\t"
    "v_cvt_pk_bf16_f32 v79, v94, v95\n\t"
    "v_mfma_f32_16x16x32_bf16 v[88:91], a[128:131], a[0:3], v[48:51]\n\t"
    "s_mov_b32 s59, 61440\n\t"
    "v_exp_f32 v80, v80\n\t"
    "v_mfma_f32_16x16x32_bf16 v[88:91], a[132:135], a[4:7], v[88:91]\n\t"
    "ds_read_b64_tr_b16 a[206:207], v11 offset:28672\n\t"
    "v_add_f32 v44, v44, v80\n\t"
    "v_mfma_f32_16x16x32_bf16 v[88:91], a[136:139], a[8:11], v[88:91]\n\t"
    "buffer_load_dwordx4 v18, s[48:51], s59 offen nt lds\n\t"
    "v_exp_f32 v81, v81\n\t"
    "v_mfma_f32_16x16x32_bf16 v[88:91], a[140:143], a[12:15], v[88:91]\n\t"
    "ds_read_b64_tr_b16 a[208:209], v8 offset:25600\n\t"
    "v_add_f32 v44, v44, v81\n\t"
    "v_cvt_pk_bf16_f32 v72, v80, v81\n\t"
    "v_mfma_f32_16x16x32_bf16 v[92:95], a[144:147], a[0:3], v[48:51]\n\t"
    "buffer_load_dwordx4 v18, s[48:51], s59 offen offset:2048 nt lds\n\t"
    "v_exp_f32 v82, v82\n\t"
    "v_mfma_f32_16x16x32_bf16 v[92:95], a[148:151], a[4:7], v[92:95]\n\t"
    "ds_read_b64_tr_b16 a[210:211], v8 offset:29696\n\t"
    "v_add_f32 v44, v44, v82\n\t"
    "v_mfma_f32_16x16x32_bf16 v[92:95], a[152:155], a[8:11], v[92:95]\n\t"
    "s_add_u32 m0, s72, 128000\n\t"
    "v_exp_f32 v83, v83\n\t"
    "v_mfma_f32_16x16x32_bf16 v[92:95], a[156:159], a[12:15], v[92:95]\n\t"
    "v_add_f32 v44, v44, v83\n\t"
    "v_cvt_pk_bf16_f32 v73, v82, v83\n\t"
    "v_mfma_f32_16x16x32_bf16 v[160:163], a[160:163], v[76:79], v[160:163]\n\t"
    "s_mov_b32 s59, 61568\n\t"
    "v_exp_f32 v84, v84\n\t"
    "v_mfma_f32_16x16x32_bf16 v[164:167], a[164:167], v[76:79], v[164:167]\n\t"
    "ds_read_b64_tr_b16 a[212:213], v9 offset:25600\n\t"
    "v_add_f32 v44, v44, v84\n\t"
    "v_mfma_f32_16x16x32_bf16 v[168:171], a[168:171], v[76:79], v[168:171]\n\t"
    "buffer_load_dwordx4 v18, s[48:51], s59 offen nt lds\n\t"
    "v_exp_f32 v85, v85\n\t"
    "v_mfma_f32_16x16x32_bf16 v[172:175], a[172:175], v[76:79], v[172:175]\n\t"
    "v_add_f32 v44, v44, v85\n\t"
    "v_cvt_pk_bf16_f32 v74, v84, v85\n\t"
    "v_mfma_f32_16x16x32_bf16 v[176:179], a[176:179], v[76:79], v[176:179]\n\t"
    "buffer_load_dwordx4 v18, s[48:51], s59 offen offset:2048 nt lds\n\t"
    "v_exp_f32 v86, v86\n\t"
    "v_mfma_f32_16x16x32_bf16 v[180:183], a[180:183], v[76:79], v[180:183]\n\t"
    "ds_read_b64_tr_b16 a[214:215], v9 offset:29696\n\t"
    "v_add_f32 v44, v44, v86\n\t"
    "v_mfma_f32_16x16x32_bf16 v[184:187], a[184:187], v[76:79], v[184:187]\n\t"
    "v_exp_f32 v87, v87\n\t"
    "v_mfma_f32_16x16x32_bf16 v[188:191], a[188:191], v[76:79], v[188:191]\n\t"
    "v_add_f32 v44, v44, v87\n\t"
    "v_cvt_pk_bf16_f32 v75, v86, v87\n\t"
    "v_mfma_f32_16x16x32_bf16 v[80:83], a[128:131], a[16:19], v[52:55]\n\t"
    "v_exp_f32 v88, v88\n\t"
    "v_mfma_f32_16x16x32_bf16 v[80:83], a[132:135], a[20:23], v[80:83]\n\t"
    "ds_read_b64_tr_b16 a[216:217], v10 offset:25600\n\t"
    "v_add_f32 v40, v40, v88\n\t"
    "v_mfma_f32_16x16x32_bf16 v[80:83], a[136:139], a[24:27], v[80:83]\n\t"
    "v_exp_f32 v89, v89\n\t"
    "v_mfma_f32_16x16x32_bf16 v[80:83], a[140:143], a[28:31], v[80:83]\n\t"
    "v_add_f32 v40, v40, v89\n\t"
    "v_cvt_pk_bf16_f32 v76, v88, v89\n\t"
    "v_mfma_f32_16x16x32_bf16 v[84:87], a[144:147], a[16:19], v[52:55]\n\t"
    "v_exp_f32 v90, v90\n\t"
    "v_mfma_f32_16x16x32_bf16 v[84:87], a[148:151], a[20:23], v[84:87]\n\t"
    "ds_read_b64_tr_b16 a[218:219], v10 offset:29696\n\t"
    "v_add_f32 v40, v40, v90\n\t"
    "v_mfma_f32_16x16x32_bf16 v[84:87], a[152:155], a[24:27], v[84:87]\n\t"
    "v_exp_f32 v91, v91\n\t"
    "v_mfma_f32_16x16x32_bf16 v[84:87], a[156:159], a[28:31], v[84:87]\n\t"
    "v_add_f32 v40, v40, v91\n\t"
    "v_cvt_pk_bf16_f32 v77, v90, v91\n\t"
    "v_mfma_f32_16x16x32_bf16 v[192:195], a[160:163], v[72:75], v[192:195]\n\t"
    "v_exp_f32 v92, v92\n\t"
    "v_mfma_f32_16x16x32_bf16 v[196:199], a[164:167], v[72:75], v[196:199]\n\t"
    "ds_read_b64_tr_b16 a[220:221], v11 offset:25600\n\t"
    "v_add_f32 v40, v40, v92\n\t"
    "v_mfma_f32_16x16x32_bf16 v[200:203], a[168:171], v[72:75], v[200:203]\n\t"
    "v_exp_f32 v93, v93\n\t"
    "v_mfma_f32_16x16x32_bf16 v[204:207], a[172:175], v[72:75], v[204:207]\n\t"
    "v_add_f32 v40, v40, v93\n\t"
    "v_cvt_pk_bf16_f32 v78, v92, v93\n\t"
    "v_mfma_f32_16x16x32_bf16 v[208:211], a[176:179], v[72:75], v[208:211]\n\t"
    "v_exp_f32 v94, v94\n\t"
    "v_mfma_f32_16x16x32_bf16 v[212:215], a[180:183], v[72:75], v[212:215]\n\t"
    "ds_read_b64_tr_b16 a[222:223], v11 offset:29696\n\t"
    "v_add_f32 v40, v40, v94\n\t"
    "v_mfma_f32_16x16x32_bf16 v[216:219], a[184:187], v[72:75], v[216:219]\n\t"
    "v_exp_f32 v95, v95\n\t"
    "v_mfma_f32_16x16x32_bf16 v[220:223], a[188:191], v[72:75], v[220:223]\n\t"
    "s_waitcnt vmcnt(16)\n\t"
    "s_barrier\n\t"
    "v_add_f32 v40, v40, v95\n\t"
    "v_cvt_pk_bf16_f32 v79, v94, v95\n\t"
    "v_mfma_f32_16x16x32_bf16 v[88:91], a[128:131], a[32:35], v[56:59]\n\t"
    "s_add_u32 m0, s72, 0\n\t"
    "v_exp_f32 v80, v80\n\t"
    "v_mfma_f32_16x16x32_bf16 v[88:91], a[132:135], a[36:39], v[88:91]\n\t"
    "ds_read_b128 a[96:99], v4 offset:32768\n\t"
    "v_add_f32 v41, v41, v80\n\t"
    "v_mfma_f32_16x16x32_bf16 v[88:91], a[136:139], a[40:43], v[88:91]\n\t"
    "s_mov_b32 s58, 65536\n\t"
    "v_exp_f32 v81, v81\n\t"
    "v_mfma_f32_16x16x32_bf16 v[88:91], a[140:143], a[44:47], v[88:91]\n\t"
    "ds_read_b128 a[100:103], v5 offset:32768\n\t"
    "v_add_f32 v41, v41, v81\n\t"
    "v_cvt_pk_bf16_f32 v72, v80, v81\n\t"
    "v_mfma_f32_16x16x32_bf16 v[92:95], a[144:147], a[32:35], v[56:59]\n\t"
    "buffer_load_dwordx4 v12, s[44:47], s58 offen nt lds\n\t"
    "v_exp_f32 v82, v82\n\t"
    "v_mfma_f32_16x16x32_bf16 v[92:95], a[148:151], a[36:39], v[92:95]\n\t"
    "ds_read_b128 a[104:107], v6 offset:32768\n\t"
    "v_add_f32 v41, v41, v82\n\t"
    "v_mfma_f32_16x16x32_bf16 v[92:95], a[152:155], a[40:43], v[92:95]\n\t"
    "buffer_load_dwordx4 v13, s[44:47], s58 offen offset:1024 nt lds\n\t"
    "v_exp_f32 v83, v83\n\t"
    "v_mfma_f32_16x16x32_bf16 v[92:95], a[156:159], a[44:47], v[92:95]\n\t"
    "s_waitcnt lgkmcnt(3)\n\t"
    "v_add_f32 v41, v41, v83\n\t"
    "v_cvt_pk_bf16_f32 v73, v82, v83\n\t"
    "v_mfma_f32_16x16x32_bf16 a[224:227], a[192:195], v[76:79], a[224:227]\n\t"
    "buffer_load_dwordx4 v14, s[44:47], s58 offen offset:2048 nt lds\n\t"
    "v_exp_f32 v84, v84\n\t"
    "v_mfma_f32_16x16x32_bf16 a[228:231], a[196:199], v[76:79], a[228:231]\n\t"
    "ds_read_b128 a[108:111], v7 offset:32768\n\t"
    "v_add_f32 v41, v41, v84\n\t"
    "v_mfma_f32_16x16x32_bf16 a[232:235], a[200:203], v[76:79], a[232:235]\n\t"
    "buffer_load_dwordx4 v15, s[44:47], s58 offen offset:3072 nt lds\n\t"
    "v_exp_f32 v85, v85\n\t"
    "v_mfma_f32_16x16x32_bf16 a[236:239], a[204:207], v[76:79], a[236:239]\n\t"
    "v_add_f32 v41, v41, v85\n\t"
    "v_cvt_pk_bf16_f32 v74, v84, v85\n\t"
    "v_mfma_f32_16x16x32_bf16 a[240:243], a[208:211], v[76:79], a[240:243]\n\t"
    "s_add_u32 m0, s72, 4096\n\t"
    "v_exp_f32 v86, v86\n\t"
    "v_mfma_f32_16x16x32_bf16 a[244:247], a[212:215], v[76:79], a[244:247]\n\t"
    "ds_read_b128 a[112:115], v4 offset:36864\n\t"
    "v_add_f32 v41, v41, v86\n\t"
    "v_mfma_f32_16x16x32_bf16 a[248:251], a[216:219], v[76:79], a[248:251]\n\t"
    "v_exp_f32 v87, v87\n\t"
    "v_mfma_f32_16x16x32_bf16 a[252:255], a[220:223], v[76:79], a[252:255]\n\t"
    "v_add_f32 v41, v41, v87\n\t"
    "v_cvt_pk_bf16_f32 v75, v86, v87\n\t"
    "v_mfma_f32_16x16x32_bf16 v[80:83], a[128:131], a[48:51], v[60:63]\n\t"
    "s_mov_b32 s58, 69632\n\t"
    "v_exp_f32 v88, v88\n\t"
    "v_mfma_f32_16x16x32_bf16 v[80:83], a[132:135], a[52:55], v[80:83]\n\t"
    "ds_read_b128 a[116:119], v5 offset:36864\n\t"
    "v_add_f32 v42, v42, v88\n\t"
    "v_mfma_f32_16x16x32_bf16 v[80:83], a[136:139], a[56:59], v[80:83]\n\t"
    "buffer_load_dwordx4 v12, s[44:47], s58 offen nt lds\n\t"
    "v_exp_f32 v89, v89\n\t"
    "v_mfma_f32_16x16x32_bf16 v[80:83], a[140:143], a[60:63], v[80:83]\n\t"
    "ds_read_b128 a[120:123], v6 offset:36864\n\t"
    "v_add_f32 v42, v42, v89\n\t"
    "v_cvt_pk_bf16_f32 v76, v88, v89\n\t"
    "v_mfma_f32_16x16x32_bf16 v[84:87], a[144:147], a[48:51], v[60:63]\n\t"
    "buffer_load_dwordx4 v13, s[44:47], s58 offen offset:1024 nt lds\n\t"
    "v_exp_f32 v90, v90\n\t"
    "v_mfma_f32_16x16x32_bf16 v[84:87], a[148:151], a[52:55], v[84:87]\n\t"
    "ds_read_b128 a[124:127], v7 offset:36864\n\t"
    "v_add_f32 v42, v42, v90\n\t"
    "v_mfma_f32_16x16x32_bf16 v[84:87], a[152:155], a[56:59], v[84:87]\n\t"
    "buffer_load_dwordx4 v14, s[44:47], s58 offen offset:2048 nt lds\n\t"
    "v_exp_f32 v91, v91\n\t"
    "v_mfma_f32_16x16x32_bf16 v[84:87], a[156:159], a[60:63], v[84:87]\n\t"
    "v_add_f32 v42, v42, v91\n\t"
    "v_cvt_pk_bf16_f32 v77, v90, v91\n\t"
    "v_mfma_f32_16x16x32_bf16 v[96:99], a[192:195], v[72:75], v[96:99]\n\t"
    "buffer_load_dwordx4 v15, s[44:47], s58 offen offset:3072 nt lds\n\t"
    "v_exp_f32 v92, v92\n\t"
    "v_mfma_f32_16x16x32_bf16 v[100:103], a[196:199], v[72:75], v[100:103]\n\t"
    "ds_read_b64_tr_b16 a[160:161], v8 offset:40960\n\t"
    "v_add_f32 v42, v42, v92\n\t"
    "v_mfma_f32_16x16x32_bf16 v[104:107], a[200:203], v[72:75], v[104:107]\n\t"
    "s_add_u32 m0, s72, 8192\n\t"
    "v_exp_f32 v93, v93\n\t"
    "v_mfma_f32_16x16x32_bf16 v[108:111], a[204:207], v[72:75], v[108:111]\n\t"
    "v_add_f32 v42, v42, v93\n\t"
    "v_cvt_pk_bf16_f32 v78, v92, v93\n\t"
    "v_mfma_f32_16x16x32_bf16 v[112:115], a[208:211], v[72:75], v[112:115]\n\t"
    "s_mov_b32 s59, 65536\n\t"
    "v_exp_f32 v94, v94\n\t"
    "v_mfma_f32_16x16x32_bf16 v[116:119], a[212:215], v[72:75], v[116:119]\n\t"
    "ds_read_b64_tr_b16 a[162:163], v8 offset:45056\n\t"
    "v_add_f32 v42, v42, v94\n\t"
    "v_mfma_f32_16x16x32_bf16 v[120:123], a[216:219], v[72:75], v[120:123]\n\t"
    "v_exp_f32 v95, v95\n\t"
    "v_mfma_f32_16x16x32_bf16 v[124:127], a[220:223], v[72:75], v[124:127]\n\t"
    "v_add_f32 v42, v42, v95\n\t"
    "v_cvt_pk_bf16_f32 v79, v94, v95\n\t"
    "v_mfma_f32_16x16x32_bf16 v[88:91], a[128:131], a[64:67], v[64:67]\n\t"
    "buffer_load_dwordx4 v18, s[48:51], s59 offen nt lds\n\t"
    "v_exp_f32 v80, v80\n\t"
    "v_mfma_f32_16x16x32_bf16 v[88:91], a[132:135], a[68:71], v[88:91]\n\t"
    "ds_read_b64_tr_b16 a[164:165], v9 offset:40960\n\t"
    "v_add_f32 v43, v43, v80\n\t"
    "v_mfma_f32_16x16x32_bf16 v[88:91], a[136:139], a[72:75], v[88:91]\n\t"
    "buffer_load_dwordx4 v18, s[48:51], s59 offen offset:2048 nt lds\n\t"
    "v_exp_f32 v81, v81\n\t"
    "v_mfma_f32_16x16x32_bf16 v[88:91], a[140:143], a[76:79], v[88:91]\n\t"
    "ds_read_b64_tr_b16 a[166:167], v9 offset:45056\n\t"
    "v_add_f32 v43, v43, v81\n\t"
    "v_cvt_pk_bf16_f32 v72, v80, v81\n\t"
    "v_mfma_f32_16x16x32_bf16 v[92:95], a[144:147], a[64:67], v[64:67]\n\t"
    "s_add_u32 m0, s72, 9216\n\t"
    "v_exp_f32 v82, v82\n\t"
    "v_mfma_f32_16x16x32_bf16 v[92:95], a[148:151], a[68:71], v[92:95]\n\t"
    "ds_read_b64_tr_b16 a[168:169], v10 offset:40960\n\t"
    "v_add_f32 v43, v43, v82\n\t"
    "v_mfma_f32_16x16x32_bf16 v[92:95], a[152:155], a[72:75], v[92:95]\n\t"
    "s_mov_b32 s59, 65664\n\t"
    "v_exp_f32 v83, v83\n\t"
    "v_mfma_f32_16x16x32_bf16 v[92:95], a[156:159], a[76:79], v[92:95]\n\t"
    "v_add_f32 v43, v43, v83\n\t"
    "v_cvt_pk_bf16_f32 v73, v82, v83\n\t"
    "v_mfma_f32_16x16x32_bf16 v[128:131], a[192:195], v[76:79], v[128:131]\n\t"
    "buffer_load_dwordx4 v18, s[48:51], s59 offen nt lds\n\t"
    "v_exp_f32 v84, v84\n\t"
    "v_mfma_f32_16x16x32_bf16 v[132:135], a[196:199], v[76:79], v[132:135]\n\t"
    "ds_read_b64_tr_b16 a[170:171], v10 offset:45056\n\t"
    "v_add_f32 v43, v43, v84\n\t"
    "v_mfma_f32_16x16x32_bf16 v[136:139], a[200:203], v[76:79], v[136:139]\n\t"
    "buffer_load_dwordx4 v18, s[48:51], s59 offen offset:2048 nt lds\n\t"
    "v_exp_f32 v85, v85\n\t"
    "v_mfma_f32_16x16x32_bf16 v[140:143], a[204:207], v[76:79], v[140:143]\n\t"
    "v_add_f32 v43, v43, v85\n\t"
    "v_cvt_pk_bf16_f32 v74, v84, v85\n\t"
    "v_mfma_f32_16x16x32_bf16 v[144:147], a[208:211], v[76:79], v[144:147]\n\t"
    "s_add_u32 m0, s72, 12288\n\t"
    "v_exp_f32 v86, v86\n\t"
    "v_mfma_f32_16x16x32_bf16 v[148:151], a[212:215], v[76:79], v[148:151]\n\t"
    "ds_read_b64_tr_b16 a[172:173], v11 offset:40960\n\t"
    "v_add_f32 v43, v43, v86\n\t"
    "v_mfma_f32_16x16x32_bf16 v[152:155], a[216:219], v[76:79], v[152:155]\n\t"
    "v_exp_f32 v87, v87\n\t"
    "v_mfma_f32_16x16x32_bf16 v[156:159], a[220:223], v[76:79], v[156:159]\n\t"
    "s_waitcnt lgkmcnt(7)\n\t"
    "v_add_f32 v43, v43, v87\n\t"
    "v_cvt_pk_bf16_f32 v75, v86, v87\n\t"
    "v_mfma_f32_16x16x32_bf16 v[80:83], a[96:99], a[0:3], v[48:51]\n\t"
    "s_mov_b32 s59, 69632\n\t"
    "v_exp_f32 v88, v88\n\t"
    "v_mfma_f32_16x16x32_bf16 v[80:83], a[100:103], a[4:7], v[80:83]\n\t"
    "ds_read_b64_tr_b16 a[174:175], v11 offset:45056\n\t"
    "v_add_f32 v44, v44, v88\n\t"
    "v_mfma_f32_16x16x32_bf16 v[80:83], a[104:107], a[8:11], v[80:83]\n\t"
    "buffer_load_dwordx4 v18, s[48:51], s59 offen nt lds\n\t"
    "v_exp_f32 v89, v89\n\t"
    "v_mfma_f32_16x16x32_bf16 v[80:83], a[108:111], a[12:15], v[80:83]\n\t"
    "ds_read_b64_tr_b16 a[176:177], v8 offset:41984\n\t"
    "v_add_f32 v44, v44, v89\n\t"
    "v_cvt_pk_bf16_f32 v76, v88, v89\n\t"
    "v_mfma_f32_16x16x32_bf16 v[84:87], a[112:115], a[0:3], v[48:51]\n\t"
    "buffer_load_dwordx4 v18, s[48:51], s59 offen offset:2048 nt lds\n\t"
    "v_exp_f32 v90, v90\n\t"
    "v_mfma_f32_16x16x32_bf16 v[84:87], a[116:119], a[4:7], v[84:87]\n\t"
    "ds_read_b64_tr_b16 a[178:179], v8 offset:46080\n\t"
    "v_add_f32 v44, v44, v90\n\t"
    "v_mfma_f32_16x16x32_bf16 v[84:87], a[120:123], a[8:11], v[84:87]\n\t"
    "s_add_u32 m0, s72, 13312\n\t"
    "v_exp_f32 v91, v91\n\t"
    "v_mfma_f32_16x16x32_bf16 v[84:87], a[124:127], a[12:15], v[84:87]\n\t"
    "v_add_f32 v44, v44, v91\n\t"
    "v_cvt_pk_bf16_f32 v77, v90, v91\n\t"
    "v_mfma_f32_16x16x32_bf16 v[160:163], a[192:195], v[72:75], v[160:163]\n\t"
    "s_mov_b32 s59, 69760\n\t"
    "v_exp_f32 v92, v92\n\t"
    "v_mfma_f32_16x16x32_bf16 v[164:167], a[196:199], v[72:75], v[164:167]\n\t"
    "ds_read_b64_tr_b16 a[180:181], v9 offset:41984\n\t"
    "v_add_f32 v44, v44, v92\n\t"
    "v_mfma_f32_16x16x32_bf16 v[168:171], a[200:203], v[72:75], v[168:171]\n\t"
    "buffer_load_dwordx4 v18, s[48:51], s59 offen nt lds\n\t"
    "v_exp_f32 v93, v93\n\t"
    "v_mfma_f32_16x16x32_bf16 v[172:175], a[204:207], v[72:75], v[172:175]\n\t"
    "v_add_f32 v44, v44, v93\n\t"
    "v_cvt_pk_bf16_f32 v78, v92, v93\n\t"
    "v_mfma_f32_16x16x32_bf16 v[176:179], a[208:211], v[72:75], v[176:179]\n\t"
    "buffer_load_dwordx4 v18, s[48:51], s59 offen offset:2048 nt lds\n\t"
    "v_exp_f32 v94, v94\n\t"
    "v_mfma_f32_16x16x32_bf16 v[180:183], a[212:215], v[72:75], v[180:183]\n\t"
    "ds_read_b64_tr_b16 a[182:183], v9 offset:46080\n\t"
    "v_add_f32 v44, v44, v94\n\t"
    "v_mfma_f32_16x16x32_bf16 v[184:187], a[216:219], v[72:75], v[184:187]\n\t"
    "v_exp_f32 v95, v95\n\t"
    "v_mfma_f32_16x16x32_bf16 v[188:191], a[220:223], v[72:75], v[188:191]\n\t"
    "v_add_f32 v44, v44, v95\n\t"
    "v_cvt_pk_bf16_f32 v79, v94, v95\n\t"
    "v_mfma_f32_16x16x32_bf16 v[88:91], a[96:99], a[16:19], v[52:55]\n\t"
    "v_exp_f32 v80, v80\n\t"
    "v_mfma_f32_16x16x32_bf16 v[88:91], a[100:103], a[20:23], v[88:91]\n\t"
    "ds_read_b64_tr_b16 a[184:185], v10 offset:41984\n\t"
    "v_add_f32 v40, v40, v80\n\t"
    "v_mfma_f32_16x16x32_bf16 v[88:91], a[104:107], a[24:27], v[88:91]\n\t"
    "v_exp_f32 v81, v81\n\t"
    "v_mfma_f32_16x16x32_bf16 v[88:91], a[108:111], a[28:31], v[88:91]\n\t"
    "v_add_f32 v40, v40, v81\n\t"
    "v_cvt_pk_bf16_f32 v72, v80, v81\n\t"
    "v_mfma_f32_16x16x32_bf16 v[92:95], a[112:115], a[16:19], v[52:55]\n\t"
    "v_exp_f32 v82, v82\n\t"
    "v_mfma_f32_16x16x32_bf16 v[92:95], a[116:119], a[20:23], v[92:95]\n\t"
    "ds_read_b64_tr_b16 a[186:187], v10 offset:46080\n\t"
    "v_add_f32 v40, v40, v82\n\t"
    "v_mfma_f32_16x16x32_bf16 v[92:95], a[120:123], a[24:27], v[92:95]\n\t"
    "v_exp_f32 v83, v83\n\t"
    "v_mfma_f32_16x16x32_bf16 v[92:95], a[124:127], a[28:31], v[92:95]\n\t"
    "v_add_f32 v40, v40, v83\n\t"
    "v_cvt_pk_bf16_f32 v73, v82, v83\n\t"
    "v_mfma_f32_16x16x32_bf16 v[192:195], a[192:195], v[76:79], v[192:195]\n\t"
    "v_exp_f32 v84, v84\n\t"
    "v_mfma_f32_16x16x32_bf16 v[196:199], a[196:199], v[76:79], v[196:199]\n\t"
    "ds_read_b64_tr_b16 a[188:189], v11 offset:41984\n\t"
    "v_add_f32 v40, v40, v84\n\t"
    "v_mfma_f32_16x16x32_bf16 v[200:203], a[200:203], v[76:79], v[200:203]\n\t"
    "v_exp_f32 v85, v85\n\t"
    "v_mfma_f32_16x16x32_bf16 v[204:207], a[204:207], v[76:79], v[204:207]\n\t"
    "v_add_f32 v40, v40, v85\n\t"
    "v_cvt_pk_bf16_f32 v74, v84, v85\n\t"
    "v_mfma_f32_16x16x32_bf16 v[208:211], a[208:211], v[76:79], v[208:211]\n\t"
    "v_exp_f32 v86, v86\n\t"
    "v_mfma_f32_16x16x32_bf16 v[212:215], a[212:215], v[76:79], v[212:215]\n\t"
    "ds_read_b64_tr_b16 a[190:191], v11 offset:46080\n\t"
    "v_add_f32 v40, v40, v86\n\t"
    "v_mfma_f32_16x16x32_bf16 v[216:219], a[216:219], v[76:79], v[216:219]\n\t"
    "v_exp_f32 v87, v87\n\t"
    "v_mfma_f32_16x16x32_bf16 v[220:223], a[220:223], v[76:79], v[220:223]\n\t"
    "s_waitcnt vmcnt(16)\n\t"
    "s_barrier\n\t"
    "v_add_f32 v40, v40, v87\n\t"
    "v_cvt_pk_bf16_f32 v75, v86, v87\n\t"
    "v_mfma_f32_16x16x32_bf16 v[80:83], a[96:99], a[32:35], v[56:59]\n\t"
    "s_add_u32 m0, s72, 16384\n\t"
    "v_exp_f32 v88, v88\n\t"
    "v_mfma_f32_16x16x32_bf16 v[80:83], a[100:103], a[36:39], v[80:83]\n\t"
    "ds_read_b128 a[128:131], v4 offset:49152\n\t"
    "v_add_f32 v41, v41, v88\n\t"
    "v_mfma_f32_16x16x32_bf16 v[80:83], a[104:107], a[40:43], v[80:83]\n\t"
    "s_mov_b32 s58, 73728\n\t"
    "v_exp_f32 v89, v89\n\t"
    "v_mfma_f32_16x16x32_bf16 v[80:83], a[108:111], a[44:47], v[80:83]\n\t"
    "ds_read_b128 a[132:135], v5 offset:49152\n\t"
    "v_add_f32 v41, v41, v89\n\t"
    "v_cvt_pk_bf16_f32 v76, v88, v89\n\t"
    "v_mfma_f32_16x16x32_bf16 v[84:87], a[112:115], a[32:35], v[56:59]\n\t"
    "buffer_load_dwordx4 v12, s[44:47], s58 offen nt lds\n\t"
    "v_exp_f32 v90, v90\n\t"
    "v_mfma_f32_16x16x32_bf16 v[84:87], a[116:119], a[36:39], v[84:87]\n\t"
    "ds_read_b128 a[136:139], v6 offset:49152\n\t"
    "v_add_f32 v41, v41, v90\n\t"
    "v_mfma_f32_16x16x32_bf16 v[84:87], a[120:123], a[40:43], v[84:87]\n\t"
    "buffer_load_dwordx4 v13, s[44:47], s58 offen offset:1024 nt lds\n\t"
    "v_exp_f32 v91, v91\n\t"
    "v_mfma_f32_16x16x32_bf16 v[84:87], a[124:127], a[44:47], v[84:87]\n\t"
    "s_waitcnt lgkmcnt(3)\n\t"
    "v_add_f32 v41, v41, v91\n\t"
    "v_cvt_pk_bf16_f32 v77, v90, v91\n\t"
    "v_mfma_f32_16x16x32_bf16 a[224:227], a[160:163], v[72:75], a[224:227]\n\t"
    "buffer_load_dwordx4 v14, s[44:47], s58 offen offset:2048 nt lds\n\t"
    "v_exp_f32 v92, v92\n\t"
    "v_mfma_f32_16x16x32_bf16 a[228:231], a[164:167], v[72:75], a[228:231]\n\t"
    "ds_read_b128 a[140:143], v7 offset:49152\n\t"
    "v_add_f32 v41, v41, v92\n\t"
    "v_mfma_f32_16x16x32_bf16 a[232:235], a[168:171], v[72:75], a[232:235]\n\t"
    "buffer_load_dwordx4 v15, s[44:47], s58 offen offset:3072 nt lds\n\t"
    "v_exp_f32 v93, v93\n\t"
    "v_mfma_f32_16x16x32_bf16 a[236:239], a[172:175], v[72:75], a[236:239]\n\t"
    "v_add_f32 v41, v41, v93\n\t"
    "v_cvt_pk_bf16_f32 v78, v92, v93\n\t"
    "v_mfma_f32_16x16x32_bf16 a[240:243], a[176:179], v[72:75], a[240:243]\n\t"
    "s_add_u32 m0, s72, 20480\n\t"
    "v_exp_f32 v94, v94\n\t"
    "v_mfma_f32_16x16x32_bf16 a[244:247], a[180:183], v[72:75], a[244:247]\n\t"
    "ds_read_b128 a[144:147], v4 offset:53248\n\t"
    "v_add_f32 v41, v41, v94\n\t"
    "v_mfma_f32_16x16x32_bf16 a[248:251], a[184:187], v[72:75], a[248:251]\n\t"
    "v_exp_f32 v95, v95\n\t"
    "v_mfma_f32_16x16x32_bf16 a[252:255], a[188:191], v[72:75], a[252:255]\n\t"
    "v_add_f32 v41, v41, v95\n\t"
    "v_cvt_pk_bf16_f32 v79, v94, v95\n\t"
    "v_mfma_f32_16x16x32_bf16 v[88:91], a[96:99], a[48:51], v[60:63]\n\t"
    "s_mov_b32 s58, 77824\n\t"
    "v_exp_f32 v80, v80\n\t"
    "v_mfma_f32_16x16x32_bf16 v[88:91], a[100:103], a[52:55], v[88:91]\n\t"
    "ds_read_b128 a[148:151], v5 offset:53248\n\t"
    "v_add_f32 v42, v42, v80\n\t"
    "v_mfma_f32_16x16x32_bf16 v[88:91], a[104:107], a[56:59], v[88:91]\n\t"
    "buffer_load_dwordx4 v12, s[44:47], s58 offen nt lds\n\t"
    "v_exp_f32 v81, v81\n\t"
    "v_mfma_f32_16x16x32_bf16 v[88:91], a[108:111], a[60:63], v[88:91]\n\t"
    "ds_read_b128 a[152:155], v6 offset:53248\n\t"
    "v_add_f32 v42, v42, v81\n\t"
    "v_cvt_pk_bf16_f32 v72, v80, v81\n\t"
    "v_mfma_f32_16x16x32_bf16 v[92:95], a[112:115], a[48:51], v[60:63]\n\t"
    "buffer_load_dwordx4 v13, s[44:47], s58 offen offset:1024 nt lds\n\t"
    "v_exp_f32 v82, v82\n\t"
    "v_mfma_f32_16x16x32_bf16 v[92:95], a[116:119], a[52:55], v[92:95]\n\t"
    "ds_read_b128 a[156:159], v7 offset:53248\n\t"
    "v_add_f32 v42, v42, v82\n\t"
    "v_mfma_f32_16x16x32_bf16 v[92:95], a[120:123], a[56:59], v[92:95]\n\t"
    "buffer_load_dwordx4 v14, s[44:47], s58 offen offset:2048 nt lds\n\t"
    "v_exp_f32 v83, v83\n\t"
    "v_mfma_f32_16x16x32_bf16 v[92:95], a[124:127], a[60:63], v[92:95]\n\t"
    "v_add_f32 v42, v42, v83\n\t"
    "v_cvt_pk_bf16_f32 v73, v82, v83\n\t"
    "v_mfma_f32_16x16x32_bf16 v[96:99], a[160:163], v[76:79], v[96:99]\n\t"
    "buffer_load_dwordx4 v15, s[44:47], s58 offen offset:3072 nt lds\n\t"
    "v_exp_f32 v84, v84\n\t"
    "v_mfma_f32_16x16x32_bf16 v[100:103], a[164:167], v[76:79], v[100:103]\n\t"
    "ds_read_b64_tr_b16 a[192:193], v8 offset:57344\n\t"
    "v_add_f32 v42, v42, v84\n\t"
    "v_mfma_f32_16x16x32_bf16 v[104:107], a[168:171], v[76:79], v[104:107]\n\t"
    "s_add_u32 m0, s72, 24576\n\t"
    "v_exp_f32 v85, v85\n\t"
    "v_mfma_f32_16x16x32_bf16 v[108:111], a[172:175], v[76:79], v[108:111]\n\t"
    "v_add_f32 v42, v42, v85\n\t"
    "v_cvt_pk_bf16_f32 v74, v84, v85\n\t"
    "v_mfma_f32_16x16x32_bf16 v[112:115], a[176:179], v[76:79], v[112:115]\n\t"
    "s_mov_b32 s59, 73728\n\t"
    "v_exp_f32 v86, v86\n\t"
    "v_mfma_f32_16x16x32_bf16 v[116:119], a[180:183], v[76:79], v[116:119]\n\t"
    "ds_read_b64_tr_b16 a[194:195], v8 offset:61440\n\t"
    "v_add_f32 v42, v42, v86\n\t"
    "v_mfma_f32_16x16x32_bf16 v[120:123], a[184:187], v[76:79], v[120:123]\n\t"
    "v_exp_f32 v87, v87\n\t"
    "v_mfma_f32_16x16x32_bf16 v[124:127], a[188:191], v[76:79], v[124:127]\n\t"
    "v_add_f32 v42, v42, v87\n\t"
    "v_cvt_pk_bf16_f32 v75, v86, v87\n\t"
    "v_mfma_f32_16x16x32_bf16 v[80:83], a[96:99], a[64:67], v[64:67]\n\t"
    "buffer_load_dwordx4 v18, s[48:51], s59 offen nt lds\n\t"
    "v_exp_f32 v88, v88\n\t"
    "v_mfma_f32_16x16x32_bf16 v[80:83], a[100:103], a[68:71], v[80:83]\n\t"
    "ds_read_b64_tr_b16 a[196:197], v9 offset:57344\n\t"
    "v_add_f32 v43, v43, v88\n\t"
    "v_mfma_f32_16x16x32_bf16 v[80:83], a[104:107], a[72:75], v[80:83]\n\t"
    "buffer_load_dwordx4 v18, s[48:51], s59 offen offset:2048 nt lds\n\t"
    "v_exp_f32 v89, v89\n\t"
    "v_mfma_f32_16x16x32_bf16 v[80:83], a[108:111], a[76:79], v[80:83]\n\t"
    "ds_read_b64_tr_b16 a[198:199], v9 offset:61440\n\t"
    "v_add_f32 v43, v43, v89\n\t"
    "v_cvt_pk_bf16_f32 v76, v88, v89\n\t"
    "v_mfma_f32_16x16x32_bf16 v[84:87], a[112:115], a[64:67], v[64:67]\n\t"
    "s_add_u32 m0, s72, 25600\n\t"
    "v_exp_f32 v90, v90\n\t"
    "v_mfma_f32_16x16x32_bf16 v[84:87], a[116:119], a[68:71], v[84:87]\n\t"
    "ds_read_b64_tr_b16 a[200:201], v10 offset:57344\n\t"
    "v_add_f32 v43, v43, v90\n\t"
    "v_mfma_f32_16x16x32_bf16 v[84:87], a[120:123], a[72:75], v[84:87]\n\t"
    "s_mov_b32 s59, 73856\n\t"
    "v_exp_f32 v91, v91\n\t"
    "v_mfma_f32_16x16x32_bf16 v[84:87], a[124:127], a[76:79], v[84:87]\n\t"
    "v_add_f32 v43, v43, v91\n\t"
    "v_cvt_pk_bf16_f32 v77, v90, v91\n\t"
    "v_mfma_f32_16x16x32_bf16 v[128:131], a[160:163], v[72:75], v[128:131]\n\t"
    "buffer_load_dwordx4 v18, s[48:51], s59 offen nt lds\n\t"
    "v_exp_f32 v92, v92\n\t"
    "v_mfma_f32_16x16x32_bf16 v[132:135], a[164:167], v[72:75], v[132:135]\n\t"
    "ds_read_b64_tr_b16 a[202:203], v10 offset:61440\n\t"
    "v_add_f32 v43, v43, v92\n\t"
    "v_mfma_f32_16x16x32_bf16 v[136:139], a[168:171], v[72:75], v[136:139]\n\t"
    "buffer_load_dwordx4 v18, s[48:51], s59 offen offset:2048 nt lds\n\t"
    "v_exp_f32 v93, v93\n\t"
    "v_mfma_f32_16x16x32_bf16 v[140:143], a[172:175], v[72:75], v[140:143]\n\t"
    "v_add_f32 v43, v43, v93\n\t"
    "v_cvt_pk_bf16_f32 v78, v92, v93\n\t"
    "v_mfma_f32_16x16x32_bf16 v[144:147], a[176:179], v[72:75], v[144:147]\n\t"
    "s_add_u32 m0, s72, 28672\n\t"
    "v_exp_f32 v94, v94\n\t"
    "v_mfma_f32_16x16x32_bf16 v[148:151], a[180:183], v[72:75], v[148:151]\n\t"
    "ds_read_b64_tr_b16 a[204:205], v11 offset:57344\n\t"
    "v_add_f32 v43, v43, v94\n\t"
    "v_mfma_f32_16x16x32_bf16 v[152:155], a[184:187], v[72:75], v[152:155]\n\t"
    "v_exp_f32 v95, v95\n\t"
    "v_mfma_f32_16x16x32_bf16 v[156:159], a[188:191], v[72:75], v[156:159]\n\t"
    "s_waitcnt lgkmcnt(7)\n\t"
    "v_add_f32 v43, v43, v95\n\t"
    "v_cvt_pk_bf16_f32 v79, v94, v95\n\t"
    "v_mfma_f32_16x16x32_bf16 v[88:91], a[128:131], a[0:3], v[48:51]\n\t"
    "s_mov_b32 s59, 77824\n\t"
    "v_exp_f32 v80, v80\n\t"
    "v_mfma_f32_16x16x32_bf16 v[88:91], a[132:135], a[4:7], v[88:91]\n\t"
    "ds_read_b64_tr_b16 a[206:207], v11 offset:61440\n\t"
    "v_add_f32 v44, v44, v80\n\t"
    "v_mfma_f32_16x16x32_bf16 v[88:91], a[136:139], a[8:11], v[88:91]\n\t"
    "buffer_load_dwordx4 v18, s[48:51], s59 offen nt lds\n\t"
    "v_exp_f32 v81, v81\n\t"
    "v_mfma_f32_16x16x32_bf16 v[88:91], a[140:143], a[12:15], v[88:91]\n\t"
    "ds_read_b64_tr_b16 a[208:209], v8 offset:58368\n\t"
    "v_add_f32 v44, v44, v81\n\t"
    "v_cvt_pk_bf16_f32 v72, v80, v81\n\t"
    "v_mfma_f32_16x16x32_bf16 v[92:95], a[144:147], a[0:3], v[48:51]\n\t"
    "buffer_load_dwordx4 v18, s[48:51], s59 offen offset:2048 nt lds\n\t"
    "v_exp_f32 v82, v82\n\t"
    "v_mfma_f32_16x16x32_bf16 v[92:95], a[148:151], a[4:7], v[92:95]\n\t"
    "ds_read_b64_tr_b16 a[210:211], v8 offset:62464\n\t"
    "v_add_f32 v44, v44, v82\n\t"
    "v_mfma_f32_16x16x32_bf16 v[92:95], a[152:155], a[8:11], v[92:95]\n\t"
    "s_add_u32 m0, s72, 29696\n\t"
    "v_exp_f32 v83, v83\n\t"
    "v_mfma_f32_16x16x32_bf16 v[92:95], a[156:159], a[12:15], v[92:95]\n\t"
    "v_add_f32 v44, v44, v83\n\t"
    "v_cvt_pk_bf16_f32 v73, v82, v83\n\t"
    "v_mfma_f32_16x16x32_bf16 v[160:163], a[160:163], v[76:79], v[160:163]\n\t"
    "s_mov_b32 s59, 77952\n\t"
    "v_exp_f32 v84, v84\n\t"
    "v_mfma_f32_16x16x32_bf16 v[164:167], a[164:167], v[76:79], v[164:167]\n\t"
    "ds_read_b64_tr_b16 a[212:213], v9 offset:58368\n\t"
    "v_add_f32 v44, v44, v84\n\t"
    "v_mfma_f32_16x16x32_bf16 v[168:171], a[168:171], v[76:79], v[168:171]\n\t"
    "buffer_load_dwordx4 v18, s[48:51], s59 offen nt lds\n\t"
    "v_exp_f32 v85, v85\n\t"
    "v_mfma_f32_16x16x32_bf16 v[172:175], a[172:175], v[76:79], v[172:175]\n\t"
    "v_add_f32 v44, v44, v85\n\t"
    "v_cvt_pk_bf16_f32 v74, v84, v85\n\t"
    "v_mfma_f32_16x16x32_bf16 v[176:179], a[176:179], v[76:79], v[176:179]\n\t"
    "buffer_load_dwordx4 v18, s[48:51], s59 offen offset:2048 nt lds\n\t"
    "v_exp_f32 v86, v86\n\t"
    "v_mfma_f32_16x16x32_bf16 v[180:183], a[180:183], v[76:79], v[180:183]\n\t"
    "ds_read_b64_tr_b16 a[214:215], v9 offset:62464\n\t"
    "v_add_f32 v44, v44, v86\n\t"
    "v_mfma_f32_16x16x32_bf16 v[184:187], a[184:187], v[76:79], v[184:187]\n\t"
    "v_exp_f32 v87, v87\n\t"
    "v_mfma_f32_16x16x32_bf16 v[188:191], a[188:191], v[76:79], v[188:191]\n\t"
    "v_add_f32 v44, v44, v87\n\t"
    "v_cvt_pk_bf16_f32 v75, v86, v87\n\t"
    "v_mfma_f32_16x16x32_bf16 v[80:83], a[128:131], a[16:19], v[52:55]\n\t"
    "v_exp_f32 v88, v88\n\t"
    "v_mfma_f32_16x16x32_bf16 v[80:83], a[132:135], a[20:23], v[80:83]\n\t"
    "ds_read_b64_tr_b16 a[216:217], v10 offset:58368\n\t"
    "v_add_f32 v40, v40, v88\n\t"
    "v_xor_b32 v4, 0x10000, v4\n\t"
    "v_mfma_f32_16x16x32_bf16 v[80:83], a[136:139], a[24:27], v[80:83]\n\t"
    "v_exp_f32 v89, v89\n\t"
    "v_mfma_f32_16x16x32_bf16 v[80:83], a[140:143], a[28:31], v[80:83]\n\t"
    "v_add_f32 v40, v40, v89\n\t"
    "v_cvt_pk_bf16_f32 v76, v88, v89\n\t"
    "v_mfma_f32_16x16x32_bf16 v[84:87], a[144:147], a[16:19], v[52:55]\n\t"
    "v_exp_f32 v90, v90\n\t"
    "v_mfma_f32_16x16x32_bf16 v[84:87], a[148:151], a[20:23], v[84:87]\n\t"
    "ds_read_b64_tr_b16 a[218:219], v10 offset:62464\n\t"
    "v_add_f32 v40, v40, v90\n\t"
    "v_xor_b32 v5, 0x10000, v5\n\t"
    "v_mfma_f32_16x16x32_bf16 v[84:87], a[152:155], a[24:27], v[84:87]\n\t"
    "v_exp_f32 v91, v91\n\t"
    "v_mfma_f32_16x16x32_bf16 v[84:87], a[156:159], a[28:31], v[84:87]\n\t"
    "v_add_f32 v40, v40, v91\n\t"
    "v_cvt_pk_bf16_f32 v77, v90, v91\n\t"
    "v_mfma_f32_16x16x32_bf16 v[192:195], a[160:163], v[72:75], v[192:195]\n\t"
    "v_exp_f32 v92, v92\n\t"
    "v_mfma_f32_16x16x32_bf16 v[196:199], a[164:167], v[72:75], v[196:199]\n\t"
    "ds_read_b64_tr_b16 a[220:221], v11 offset:58368\n\t"
    "v_add_f32 v40, v40, v92\n\t"
    "v_xor_b32 v6, 0x10000, v6\n\t"
    "v_mfma_f32_16x16x32_bf16 v[200:203], a[168:171], v[72:75], v[200:203]\n\t"
    "v_exp_f32 v93, v93\n\t"
    "v_mfma_f32_16x16x32_bf16 v[204:207], a[172:175], v[72:75], v[204:207]\n\t"
    "v_add_f32 v40, v40, v93\n\t"
    "v_cvt_pk_bf16_f32 v78, v92, v93\n\t"
    "v_mfma_f32_16x16x32_bf16 v[208:211], a[176:179], v[72:75], v[208:211]\n\t"
    "v_exp_f32 v94, v94\n\t"
    "v_mfma_f32_16x16x32_bf16 v[212:215], a[180:183], v[72:75], v[212:215]\n\t"
    "ds_read_b64_tr_b16 a[222:223], v11 offset:62464\n\t"
    "v_add_f32 v40, v40, v94\n\t"
    "v_xor_b32 v7, 0x10000, v7\n\t"
    "v_mfma_f32_16x16x32_bf16 v[216:219], a[184:187], v[72:75], v[216:219]\n\t"
    "v_exp_f32 v95, v95\n\t"
    "v_mfma_f32_16x16x32_bf16 v[220:223], a[188:191], v[72:75], v[220:223]\n\t"
    "s_waitcnt vmcnt(16)\n\t"
    "s_barrier\n\t"
    "v_add_f32 v40, v40, v95\n\t"
    "v_cvt_pk_bf16_f32 v79, v94, v95\n\t"
    "v_mfma_f32_16x16x32_bf16 v[88:91], a[128:131], a[32:35], v[56:59]\n\t"
    "s_add_u32 m0, s72, 32768\n\t"
    "v_exp_f32 v80, v80\n\t"
    "v_mfma_f32_16x16x32_bf16 v[88:91], a[132:135], a[36:39], v[88:91]\n\t"
    "ds_read_b128 a[96:99], v4 offset:0\n\t"
    "v_add_f32 v41, v41, v80\n\t"
    "v_xor_b32 v8, 0x10000, v8\n\t"
    "v_mfma_f32_16x16x32_bf16 v[88:91], a[136:139], a[40:43], v[88:91]\n\t"
    "s_mov_b32 s58, 81920\n\t"
    "v_exp_f32 v81, v81\n\t"
    "v_mfma_f32_16x16x32_bf16 v[88:91], a[140:143], a[44:47], v[88:91]\n\t"
    "ds_read_b128 a[100:103], v5 offset:0\n\t"
    "v_add_f32 v41, v41, v81\n\t"
    "v_cvt_pk_bf16_f32 v72, v80, v81\n\t"
    "v_mfma_f32_16x16x32_bf16 v[92:95], a[144:147], a[32:35], v[56:59]\n\t"
    "buffer_load_dwordx4 v12, s[44:47], s58 offen nt lds\n\t"
    "v_exp_f32 v82, v82\n\t"
    "v_mfma_f32_16x16x32_bf16 v[92:95], a[148:151], a[36:39], v[92:95]\n\t"
    "ds_read_b128 a[104:107], v6 offset:0\n\t"
    "v_add_f32 v41, v41, v82\n\t"
    "v_xor_b32 v9, 0x10000, v9\n\t"
    "v_mfma_f32_16x16x32_bf16 v[92:95], a[152:155], a[40:43], v[92:95]\n\t"
    "buffer_load_dwordx4 v13, s[44:47], s58 offen offset:1024 nt lds\n\t"
    "v_exp_f32 v83, v83\n\t"
    "v_mfma_f32_16x16x32_bf16 v[92:95], a[156:159], a[44:47], v[92:95]\n\t"
    "s_waitcnt lgkmcnt(3)\n\t"
    "v_add_f32 v41, v41, v83\n\t"
    "v_cvt_pk_bf16_f32 v73, v82, v83\n\t"
    "v_mfma_f32_16x16x32_bf16 a[224:227], a[192:195], v[76:79], a[224:227]\n\t"
    "buffer_load_dwordx4 v14, s[44:47], s58 offen offset:2048 nt lds\n\t"
    "v_exp_f32 v84, v84\n\t"
    "v_mfma_f32_16x16x32_bf16 a[228:231], a[196:199], v[76:79], a[228:231]\n\t"
    "ds_read_b128 a[108:111], v7 offset:0\n\t"
    "v_add_f32 v41, v41, v84\n\t"
    "v_xor_b32 v10, 0x10000, v10\n\t"
    "v_mfma_f32_16x16x32_bf16 a[232:235], a[200:203], v[76:79], a[232:235]\n\t"
    "buffer_load_dwordx4 v15, s[44:47], s58 offen offset:3072 nt lds\n\t"
    "v_exp_f32 v85, v85\n\t"
    "v_mfma_f32_16x16x32_bf16 a[236:239], a[204:207], v[76:79], a[236:239]\n\t"
    "v_add_f32 v41, v41, v85\n\t"
    "v_cvt_pk_bf16_f32 v74, v84, v85\n\t"
    "v_mfma_f32_16x16x32_bf16 a[240:243], a[208:211], v[76:79], a[240:243]\n\t"
    "s_add_u32 m0, s72, 36864\n\t"
    "v_exp_f32 v86, v86\n\t"
    "v_mfma_f32_16x16x32_bf16 a[244:247], a[212:215], v[76:79], a[244:247]\n\t"
    "ds_read_b128 a[112:115], v4 offset:4096\n\t"
    "v_add_f32 v41, v41, v86\n\t"
    "v_xor_b32 v11, 0x10000, v11\n\t"
    "v_mfma_f32_16x16x32_bf16 a[248:251], a[216:219], v[76:79], a[248:251]\n\t"
    "v_exp_f32 v87, v87\n\t"
    "v_mfma_f32_16x16x32_bf16 a[252:255], a[220:223], v[76:79], a[252:255]\n\t"
    "v_add_f32 v41, v41, v87\n\t"
    "v_cvt_pk_bf16_f32 v75, v86, v87\n\t"
    "v_mfma_f32_16x16x32_bf16 v[80:83], a[128:131], a[48:51], v[60:63]\n\t"
    "s_mov_b32 s58, 86016\n\t"
    "v_exp_f32 v88, v88\n\t"
    "v_mfma_f32_16x16x32_bf16 v[80:83], a[132:135], a[52:55], v[80:83]\n\t"
    "ds_read_b128 a[116:119], v5 offset:4096\n\t"
    "v_add_f32 v42, v42, v88\n\t"
    "v_mfma_f32_16x16x32_bf16 v[80:83], a[136:139], a[56:59], v[80:83]\n\t"
    "buffer_load_dwordx4 v12, s[44:47], s58 offen nt lds\n\t"
    "v_exp_f32 v89, v89\n\t"
    "v_mfma_f32_16x16x32_bf16 v[80:83], a[140:143], a[60:63], v[80:83]\n\t"
    "ds_read_b128 a[120:123], v6 offset:4096\n\t"
    "v_add_f32 v42, v42, v89\n\t"
    "v_cvt_pk_bf16_f32 v76, v88, v89\n\t"
    "v_mfma_f32_16x16x32_bf16 v[84:87], a[144:147], a[48:51], v[60:63]\n\t"
    "buffer_load_dwordx4 v13, s[44:47], s58 offen offset:1024 nt lds\n\t"
    "v_exp_f32 v90, v90\n\t"
    "v_mfma_f32_16x16x32_bf16 v[84:87], a[148:151], a[52:55], v[84:87]\n\t"
    "ds_read_b128 a[124:127], v7 offset:4096\n\t"
    "v_add_f32 v42, v42, v90\n\t"
    "v_mfma_f32_16x16x32_bf16 v[84:87], a[152:155], a[56:59], v[84:87]\n\t"
    "buffer_load_dwordx4 v14, s[44:47], s58 offen offset:2048 nt lds\n\t"
    "v_exp_f32 v91, v91\n\t"
    "v_mfma_f32_16x16x32_bf16 v[84:87], a[156:159], a[60:63], v[84:87]\n\t"
    "v_add_f32 v42, v42, v91\n\t"
    "v_cvt_pk_bf16_f32 v77, v90, v91\n\t"
    "v_mfma_f32_16x16x32_bf16 v[96:99], a[192:195], v[72:75], v[96:99]\n\t"
    "buffer_load_dwordx4 v15, s[44:47], s58 offen offset:3072 nt lds\n\t"
    "v_exp_f32 v92, v92\n\t"
    "v_mfma_f32_16x16x32_bf16 v[100:103], a[196:199], v[72:75], v[100:103]\n\t"
    "ds_read_b64_tr_b16 a[160:161], v8 offset:8192\n\t"
    "v_add_f32 v42, v42, v92\n\t"
    "v_mfma_f32_16x16x32_bf16 v[104:107], a[200:203], v[72:75], v[104:107]\n\t"
    "s_add_u32 m0, s72, 40960\n\t"
    "v_exp_f32 v93, v93\n\t"
    "v_mfma_f32_16x16x32_bf16 v[108:111], a[204:207], v[72:75], v[108:111]\n\t"
    "v_add_f32 v42, v42, v93\n\t"
    "v_cvt_pk_bf16_f32 v78, v92, v93\n\t"
    "v_mfma_f32_16x16x32_bf16 v[112:115], a[208:211], v[72:75], v[112:115]\n\t"
    "s_mov_b32 s59, 81920\n\t"
    "v_exp_f32 v94, v94\n\t"
    "v_mfma_f32_16x16x32_bf16 v[116:119], a[212:215], v[72:75], v[116:119]\n\t"
    "ds_read_b64_tr_b16 a[162:163], v8 offset:12288\n\t"
    "v_add_f32 v42, v42, v94\n\t"
    "v_mfma_f32_16x16x32_bf16 v[120:123], a[216:219], v[72:75], v[120:123]\n\t"
    "v_exp_f32 v95, v95\n\t"
    "v_mfma_f32_16x16x32_bf16 v[124:127], a[220:223], v[72:75], v[124:127]\n\t"
    "v_add_f32 v42, v42, v95\n\t"
    "v_cvt_pk_bf16_f32 v79, v94, v95\n\t"
    "v_mfma_f32_16x16x32_bf16 v[88:91], a[128:131], a[64:67], v[64:67]\n\t"
    "buffer_load_dwordx4 v18, s[48:51], s59 offen nt lds\n\t"
    "v_exp_f32 v80, v80\n\t"
    "v_mfma_f32_16x16x32_bf16 v[88:91], a[132:135], a[68:71], v[88:91]\n\t"
    "ds_read_b64_tr_b16 a[164:165], v9 offset:8192\n\t"
    "v_add_f32 v43, v43, v80\n\t"
    "v_mfma_f32_16x16x32_bf16 v[88:91], a[136:139], a[72:75], v[88:91]\n\t"
    "buffer_load_dwordx4 v18, s[48:51], s59 offen offset:2048 nt lds\n\t"
    "v_exp_f32 v81, v81\n\t"
    "v_mfma_f32_16x16x32_bf16 v[88:91], a[140:143], a[76:79], v[88:91]\n\t"
    "ds_read_b64_tr_b16 a[166:167], v9 offset:12288\n\t"
    "v_add_f32 v43, v43, v81\n\t"
    "v_cvt_pk_bf16_f32 v72, v80, v81\n\t"
    "v_mfma_f32_16x16x32_bf16 v[92:95], a[144:147], a[64:67], v[64:67]\n\t"
    "s_add_u32 m0, s72, 41984\n\t"
    "v_exp_f32 v82, v82\n\t"
    "v_mfma_f32_16x16x32_bf16 v[92:95], a[148:151], a[68:71], v[92:95]\n\t"
    "ds_read_b64_tr_b16 a[168:169], v10 offset:8192\n\t"
    "v_add_f32 v43, v43, v82\n\t"
    "v_mfma_f32_16x16x32_bf16 v[92:95], a[152:155], a[72:75], v[92:95]\n\t"
    "s_mov_b32 s59, 82048\n\t"
    "v_exp_f32 v83, v83\n\t"
    "v_mfma_f32_16x16x32_bf16 v[92:95], a[156:159], a[76:79], v[92:95]\n\t"
    "v_add_f32 v43, v43, v83\n\t"
    "v_cvt_pk_bf16_f32 v73, v82, v83\n\t"
    "v_mfma_f32_16x16x32_bf16 v[128:131], a[192:195], v[76:79], v[128:131]\n\t"
    "buffer_load_dwordx4 v18, s[48:51], s59 offen nt lds\n\t"
    "v_exp_f32 v84, v84\n\t"
    "v_mfma_f32_16x16x32_bf16 v[132:135], a[196:199], v[76:79], v[132:135]\n\t"
    "ds_read_b64_tr_b16 a[170:171], v10 offset:12288\n\t"
    "v_add_f32 v43, v43, v84\n\t"
    "v_mfma_f32_16x16x32_bf16 v[136:139], a[200:203], v[76:79], v[136:139]\n\t"
    "buffer_load_dwordx4 v18, s[48:51], s59 offen offset:2048 nt lds\n\t"
    "v_exp_f32 v85, v85\n\t"
    "v_mfma_f32_16x16x32_bf16 v[140:143], a[204:207], v[76:79], v[140:143]\n\t"
    "v_add_f32 v43, v43, v85\n\t"
    "v_cvt_pk_bf16_f32 v74, v84, v85\n\t"
    "v_mfma_f32_16x16x32_bf16 v[144:147], a[208:211], v[76:79], v[144:147]\n\t"
    "s_add_u32 m0, s72, 45056\n\t"
    "v_exp_f32 v86, v86\n\t"
    "v_mfma_f32_16x16x32_bf16 v[148:151], a[212:215], v[76:79], v[148:151]\n\t"
    "ds_read_b64_tr_b16 a[172:173], v11 offset:8192\n\t"
    "v_add_f32 v43, v43, v86\n\t"
    "v_mfma_f32_16x16x32_bf16 v[152:155], a[216:219], v[76:79], v[152:155]\n\t"
    "v_exp_f32 v87, v87\n\t"
    "v_mfma_f32_16x16x32_bf16 v[156:159], a[220:223], v[76:79], v[156:159]\n\t"
    "s_waitcnt lgkmcnt(7)\n\t"
    "v_add_f32 v43, v43, v87\n\t"
    "v_cvt_pk_bf16_f32 v75, v86, v87\n\t"
    "v_mfma_f32_16x16x32_bf16 v[80:83], a[96:99], a[0:3], v[48:51]\n\t"
    "s_mov_b32 s59, 86016\n\t"
    "v_exp_f32 v88, v88\n\t"
    "v_mfma_f32_16x16x32_bf16 v[80:83], a[100:103], a[4:7], v[80:83]\n\t"
    "ds_read_b64_tr_b16 a[174:175], v11 offset:12288\n\t"
    "v_add_f32 v44, v44, v88\n\t"
    "v_mfma_f32_16x16x32_bf16 v[80:83], a[104:107], a[8:11], v[80:83]\n\t"
    "buffer_load_dwordx4 v18, s[48:51], s59 offen nt lds\n\t"
    "v_exp_f32 v89, v89\n\t"
    "v_mfma_f32_16x16x32_bf16 v[80:83], a[108:111], a[12:15], v[80:83]\n\t"
    "ds_read_b64_tr_b16 a[176:177], v8 offset:9216\n\t"
    "v_add_f32 v44, v44, v89\n\t"
    "v_cvt_pk_bf16_f32 v76, v88, v89\n\t"
    "v_mfma_f32_16x16x32_bf16 v[84:87], a[112:115], a[0:3], v[48:51]\n\t"
    "buffer_load_dwordx4 v18, s[48:51], s59 offen offset:2048 nt lds\n\t"
    "v_exp_f32 v90, v90\n\t"
    "v_mfma_f32_16x16x32_bf16 v[84:87], a[116:119], a[4:7], v[84:87]\n\t"
    "ds_read_b64_tr_b16 a[178:179], v8 offset:13312\n\t"
    "v_add_f32 v44, v44, v90\n\t"
    "v_mfma_f32_16x16x32_bf16 v[84:87], a[120:123], a[8:11], v[84:87]\n\t"
    "s_add_u32 m0, s72, 46080\n\t"
    "v_exp_f32 v91, v91\n\t"
    "v_mfma_f32_16x16x32_bf16 v[84:87], a[124:127], a[12:15], v[84:87]\n\t"
    "v_add_f32 v44, v44, v91\n\t"
    "v_cvt_pk_bf16_f32 v77, v90, v91\n\t"
    "v_mfma_f32_16x16x32_bf16 v[160:163], a[192:195], v[72:75], v[160:163]\n\t"
    "s_mov_b32 s59, 86144\n\t"
    "v_exp_f32 v92, v92\n\t"
    "v_mfma_f32_16x16x32_bf16 v[164:167], a[196:199], v[72:75], v[164:167]\n\t"
    "ds_read_b64_tr_b16 a[180:181], v9 offset:9216\n\t"
    "v_add_f32 v44, v44, v92\n\t"
    "v_mfma_f32_16x16x32_bf16 v[168:171], a[200:203], v[72:75], v[168:171]\n\t"
    "buffer_load_dwordx4 v18, s[48:51], s59 offen nt lds\n\t"
    "v_exp_f32 v93, v93\n\t"
    "v_mfma_f32_16x16x32_bf16 v[172:175], a[204:207], v[72:75], v[172:175]\n\t"
    "v_add_f32 v44, v44, v93\n\t"
    "v_cvt_pk_bf16_f32 v78, v92, v93\n\t"
    "v_mfma_f32_16x16x32_bf16 v[176:179], a[208:211], v[72:75], v[176:179]\n\t"
    "buffer_load_dwordx4 v18, s[48:51], s59 offen offset:2048 nt lds\n\t"
    "v_exp_f32 v94, v94\n\t"
    "v_mfma_f32_16x16x32_bf16 v[180:183], a[212:215], v[72:75], v[180:183]\n\t"
    "ds_read_b64_tr_b16 a[182:183], v9 offset:13312\n\t"
    "v_add_f32 v44, v44, v94\n\t"
    "v_mfma_f32_16x16x32_bf16 v[184:187], a[216:219], v[72:75], v[184:187]\n\t"
    "v_exp_f32 v95, v95\n\t"
    "v_mfma_f32_16x16x32_bf16 v[188:191], a[220:223], v[72:75], v[188:191]\n\t"
    "v_add_f32 v44, v44, v95\n\t"
    "v_cvt_pk_bf16_f32 v79, v94, v95\n\t"
    "v_mfma_f32_16x16x32_bf16 v[88:91], a[96:99], a[16:19], v[52:55]\n\t"
    "v_exp_f32 v80, v80\n\t"
    "v_mfma_f32_16x16x32_bf16 v[88:91], a[100:103], a[20:23], v[88:91]\n\t"
    "ds_read_b64_tr_b16 a[184:185], v10 offset:9216\n\t"
    "v_add_f32 v40, v40, v80\n\t"
    "v_mfma_f32_16x16x32_bf16 v[88:91], a[104:107], a[24:27], v[88:91]\n\t"
    "v_exp_f32 v81, v81\n\t"
    "v_mfma_f32_16x16x32_bf16 v[88:91], a[108:111], a[28:31], v[88:91]\n\t"
    "v_add_f32 v40, v40, v81\n\t"
    "v_cvt_pk_bf16_f32 v72, v80, v81\n\t"
    "v_mfma_f32_16x16x32_bf16 v[92:95], a[112:115], a[16:19], v[52:55]\n\t"
    "v_exp_f32 v82, v82\n\t"
    "v_mfma_f32_16x16x32_bf16 v[92:95], a[116:119], a[20:23], v[92:95]\n\t"
    "ds_read_b64_tr_b16 a[186:187], v10 offset:13312\n\t"
    "v_add_f32 v40, v40, v82\n\t"
    "v_mfma_f32_16x16x32_bf16 v[92:95], a[120:123], a[24:27], v[92:95]\n\t"
    "v_exp_f32 v83, v83\n\t"
    "v_mfma_f32_16x16x32_bf16 v[92:95], a[124:127], a[28:31], v[92:95]\n\t"
    "v_add_f32 v40, v40, v83\n\t"
    "v_cvt_pk_bf16_f32 v73, v82, v83\n\t"
    "v_mfma_f32_16x16x32_bf16 v[192:195], a[192:195], v[76:79], v[192:195]\n\t"
    "v_exp_f32 v84, v84\n\t"
    "v_mfma_f32_16x16x32_bf16 v[196:199], a[196:199], v[76:79], v[196:199]\n\t"
    "ds_read_b64_tr_b16 a[188:189], v11 offset:9216\n\t"
    "v_add_f32 v40, v40, v84\n\t"
    "v_mfma_f32_16x16x32_bf16 v[200:203], a[200:203], v[76:79], v[200:203]\n\t"
    "v_exp_f32 v85, v85\n\t"
    "v_mfma_f32_16x16x32_bf16 v[204:207], a[204:207], v[76:79], v[204:207]\n\t"
    "v_add_f32 v40, v40, v85\n\t"
    "v_cvt_pk_bf16_f32 v74, v84, v85\n\t"
    "v_mfma_f32_16x16x32_bf16 v[208:211], a[208:211], v[76:79], v[208:211]\n\t"
    "v_exp_f32 v86, v86\n\t"
    "v_mfma_f32_16x16x32_bf16 v[212:215], a[212:215], v[76:79], v[212:215]\n\t"
    "ds_read_b64_tr_b16 a[190:191], v11 offset:13312\n\t"
    "v_add_f32 v40, v40, v86\n\t"
    "v_mfma_f32_16x16x32_bf16 v[216:219], a[216:219], v[76:79], v[216:219]\n\t"
    "v_exp_f32 v87, v87\n\t"
    "v_mfma_f32_16x16x32_bf16 v[220:223], a[220:223], v[76:79], v[220:223]\n\t"
    "s_waitcnt vmcnt(16)\n\t"
    "s_barrier\n\t"
    "v_add_f32 v40, v40, v87\n\t"
    "v_cvt_pk_bf16_f32 v75, v86, v87\n\t"
    "v_mfma_f32_16x16x32_bf16 v[80:83], a[96:99], a[32:35], v[56:59]\n\t"
    "s_add_u32 m0, s72, 49152\n\t"
    "v_exp_f32 v88, v88\n\t"
    "v_mfma_f32_16x16x32_bf16 v[80:83], a[100:103], a[36:39], v[80:83]\n\t"
    "ds_read_b128 a[128:131], v4 offset:16384\n\t"
    "v_add_f32 v41, v41, v88\n\t"
    "v_mfma_f32_16x16x32_bf16 v[80:83], a[104:107], a[40:43], v[80:83]\n\t"
    "s_mov_b32 s58, 90112\n\t"
    "v_exp_f32 v89, v89\n\t"
    "v_mfma_f32_16x16x32_bf16 v[80:83], a[108:111], a[44:47], v[80:83]\n\t"
    "ds_read_b128 a[132:135], v5 offset:16384\n\t"
    "v_add_f32 v41, v41, v89\n\t"
    "v_cvt_pk_bf16_f32 v76, v88, v89\n\t"
    "v_mfma_f32_16x16x32_bf16 v[84:87], a[112:115], a[32:35], v[56:59]\n\t"
    "buffer_load_dwordx4 v12, s[44:47], s58 offen nt lds\n\t"
    "v_exp_f32 v90, v90\n\t"
    "v_mfma_f32_16x16x32_bf16 v[84:87], a[116:119], a[36:39], v[84:87]\n\t"
    "ds_read_b128 a[136:139], v6 offset:16384\n\t"
    "v_add_f32 v41, v41, v90\n\t"
    "v_mfma_f32_16x16x32_bf16 v[84:87], a[120:123], a[40:43], v[84:87]\n\t"
    "buffer_load_dwordx4 v13, s[44:47], s58 offen offset:1024 nt lds\n\t"
    "v_exp_f32 v91, v91\n\t"
    "v_mfma_f32_16x16x32_bf16 v[84:87], a[124:127], a[44:47], v[84:87]\n\t"
    "s_waitcnt lgkmcnt(3)\n\t"
    "v_add_f32 v41, v41, v91\n\t"
    "v_cvt_pk_bf16_f32 v77, v90, v91\n\t"
    "v_mfma_f32_16x16x32_bf16 a[224:227], a[160:163], v[72:75], a[224:227]\n\t"
    "buffer_load_dwordx4 v14, s[44:47], s58 offen offset:2048 nt lds\n\t"
    "v_exp_f32 v92, v92\n\t"
    "v_mfma_f32_16x16x32_bf16 a[228:231], a[164:167], v[72:75], a[228:231]\n\t"
    "ds_read_b128 a[140:143], v7 offset:16384\n\t"
    "v_add_f32 v41, v41, v92\n\t"
    "v_mfma_f32_16x16x32_bf16 a[232:235], a[168:171], v[72:75], a[232:235]\n\t"
    "buffer_load_dwordx4 v15, s[44:47], s58 offen offset:3072 nt lds\n\t"
    "v_exp_f32 v93, v93\n\t"
    "v_mfma_f32_16x16x32_bf16 a[236:239], a[172:175], v[72:75], a[236:239]\n\t"
    "v_add_f32 v41, v41, v93\n\t"
    "v_cvt_pk_bf16_f32 v78, v92, v93\n\t"
    "v_mfma_f32_16x16x32_bf16 a[240:243], a[176:179], v[72:75], a[240:243]\n\t"
    "s_add_u32 m0, s72, 53248\n\t"
    "v_exp_f32 v94, v94\n\t"
    "v_mfma_f32_16x16x32_bf16 a[244:247], a[180:183], v[72:75], a[244:247]\n\t"
    "ds_read_b128 a[144:147], v4 offset:20480\n\t"
    "v_add_f32 v41, v41, v94\n\t"
    "v_mfma_f32_16x16x32_bf16 a[248:251], a[184:187], v[72:75], a[248:251]\n\t"
    "v_exp_f32 v95, v95\n\t"
    "v_mfma_f32_16x16x32_bf16 a[252:255], a[188:191], v[72:75], a[252:255]\n\t"
    "v_add_f32 v41, v41, v95\n\t"
    "v_cvt_pk_bf16_f32 v79, v94, v95\n\t"
    "v_mfma_f32_16x16x32_bf16 v[88:91], a[96:99], a[48:51], v[60:63]\n\t"
    "s_mov_b32 s58, 94208\n\t"
    "v_exp_f32 v80, v80\n\t"
    "v_mfma_f32_16x16x32_bf16 v[88:91], a[100:103], a[52:55], v[88:91]\n\t"
    "ds_read_b128 a[148:151], v5 offset:20480\n\t"
    "v_add_f32 v42, v42, v80\n\t"
    "v_mfma_f32_16x16x32_bf16 v[88:91], a[104:107], a[56:59], v[88:91]\n\t"
    "buffer_load_dwordx4 v12, s[44:47], s58 offen nt lds\n\t"
    "v_exp_f32 v81, v81\n\t"
    "v_mfma_f32_16x16x32_bf16 v[88:91], a[108:111], a[60:63], v[88:91]\n\t"
    "ds_read_b128 a[152:155], v6 offset:20480\n\t"
    "v_add_f32 v42, v42, v81\n\t"
    "v_cvt_pk_bf16_f32 v72, v80, v81\n\t"
    "v_mfma_f32_16x16x32_bf16 v[92:95], a[112:115], a[48:51], v[60:63]\n\t"
    "buffer_load_dwordx4 v13, s[44:47], s58 offen offset:1024 nt lds\n\t"
    "v_exp_f32 v82, v82\n\t"
    "v_mfma_f32_16x16x32_bf16 v[92:95], a[116:119], a[52:55], v[92:95]\n\t"
    "ds_read_b128 a[156:159], v7 offset:20480\n\t"
    "v_add_f32 v42, v42, v82\n\t"
    "v_mfma_f32_16x16x32_bf16 v[92:95], a[120:123], a[56:59], v[92:95]\n\t"
    "buffer_load_dwordx4 v14, s[44:47], s58 offen offset:2048 nt lds\n\t"
    "v_exp_f32 v83, v83\n\t"
    "v_mfma_f32_16x16x32_bf16 v[92:95], a[124:127], a[60:63], v[92:95]\n\t"
    "v_add_f32 v42, v42, v83\n\t"
    "v_cvt_pk_bf16_f32 v73, v82, v83\n\t"
    "v_mfma_f32_16x16x32_bf16 v[96:99], a[160:163], v[76:79], v[96:99]\n\t"
    "buffer_load_dwordx4 v15, s[44:47], s58 offen offset:3072 nt lds\n\t"
    "v_exp_f32 v84, v84\n\t"
    "v_mfma_f32_16x16x32_bf16 v[100:103], a[164:167], v[76:79], v[100:103]\n\t"
    "ds_read_b64_tr_b16 a[192:193], v8 offset:24576\n\t"
    "v_add_f32 v42, v42, v84\n\t"
    "v_mfma_f32_16x16x32_bf16 v[104:107], a[168:171], v[76:79], v[104:107]\n\t"
    "s_add_u32 m0, s72, 57344\n\t"
    "v_exp_f32 v85, v85\n\t"
    "v_mfma_f32_16x16x32_bf16 v[108:111], a[172:175], v[76:79], v[108:111]\n\t"
    "v_add_f32 v42, v42, v85\n\t"
    "v_cvt_pk_bf16_f32 v74, v84, v85\n\t"
    "v_mfma_f32_16x16x32_bf16 v[112:115], a[176:179], v[76:79], v[112:115]\n\t"
    "s_mov_b32 s59, 90112\n\t"
    "v_exp_f32 v86, v86\n\t"
    "v_mfma_f32_16x16x32_bf16 v[116:119], a[180:183], v[76:79], v[116:119]\n\t"
    "ds_read_b64_tr_b16 a[194:195], v8 offset:28672\n\t"
    "v_add_f32 v42, v42, v86\n\t"
    "v_mfma_f32_16x16x32_bf16 v[120:123], a[184:187], v[76:79], v[120:123]\n\t"
    "v_exp_f32 v87, v87\n\t"
    "v_mfma_f32_16x16x32_bf16 v[124:127], a[188:191], v[76:79], v[124:127]\n\t"
    "v_add_f32 v42, v42, v87\n\t"
    "v_cvt_pk_bf16_f32 v75, v86, v87\n\t"
    "v_mfma_f32_16x16x32_bf16 v[80:83], a[96:99], a[64:67], v[64:67]\n\t"
    "buffer_load_dwordx4 v18, s[48:51], s59 offen nt lds\n\t"
    "v_exp_f32 v88, v88\n\t"
    "v_mfma_f32_16x16x32_bf16 v[80:83], a[100:103], a[68:71], v[80:83]\n\t"
    "ds_read_b64_tr_b16 a[196:197], v9 offset:24576\n\t"
    "v_add_f32 v43, v43, v88\n\t"
    "v_mfma_f32_16x16x32_bf16 v[80:83], a[104:107], a[72:75], v[80:83]\n\t"
    "buffer_load_dwordx4 v18, s[48:51], s59 offen offset:2048 nt lds\n\t"
    "v_exp_f32 v89, v89\n\t"
    "v_mfma_f32_16x16x32_bf16 v[80:83], a[108:111], a[76:79], v[80:83]\n\t"
    "ds_read_b64_tr_b16 a[198:199], v9 offset:28672\n\t"
    "v_add_f32 v43, v43, v89\n\t"
    "v_cvt_pk_bf16_f32 v76, v88, v89\n\t"
    "v_mfma_f32_16x16x32_bf16 v[84:87], a[112:115], a[64:67], v[64:67]\n\t"
    "s_add_u32 m0, s72, 58368\n\t"
    "v_exp_f32 v90, v90\n\t"
    "v_mfma_f32_16x16x32_bf16 v[84:87], a[116:119], a[68:71], v[84:87]\n\t"
    "ds_read_b64_tr_b16 a[200:201], v10 offset:24576\n\t"
    "v_add_f32 v43, v43, v90\n\t"
    "v_mfma_f32_16x16x32_bf16 v[84:87], a[120:123], a[72:75], v[84:87]\n\t"
    "s_mov_b32 s59, 90240\n\t"
    "v_exp_f32 v91, v91\n\t"
    "v_mfma_f32_16x16x32_bf16 v[84:87], a[124:127], a[76:79], v[84:87]\n\t"
    "v_add_f32 v43, v43, v91\n\t"
    "v_cvt_pk_bf16_f32 v77, v90, v91\n\t"
    "v_mfma_f32_16x16x32_bf16 v[128:131], a[160:163], v[72:75], v[128:131]\n\t"
    "buffer_load_dwordx4 v18, s[48:51], s59 offen nt lds\n\t"
    "v_exp_f32 v92, v92\n\t"
    "v_mfma_f32_16x16x32_bf16 v[132:135], a[164:167], v[72:75], v[132:135]\n\t"
    "ds_read_b64_tr_b16 a[202:203], v10 offset:28672\n\t"
    "v_add_f32 v43, v43, v92\n\t"
    "v_mfma_f32_16x16x32_bf16 v[136:139], a[168:171], v[72:75], v[136:139]\n\t"
    "buffer_load_dwordx4 v18, s[48:51], s59 offen offset:2048 nt lds\n\t"
    "v_exp_f32 v93, v93\n\t"
    "v_mfma_f32_16x16x32_bf16 v[140:143], a[172:175], v[72:75], v[140:143]\n\t"
    "v_add_f32 v43, v43, v93\n\t"
    "v_cvt_pk_bf16_f32 v78, v92, v93\n\t"
    "v_mfma_f32_16x16x32_bf16 v[144:147], a[176:179], v[72:75], v[144:147]\n\t"
    "s_add_u32 m0, s72, 61440\n\t"
    "v_exp_f32 v94, v94\n\t"
    "v_mfma_f32_16x16x32_bf16 v[148:151], a[180:183], v[72:75], v[148:151]\n\t"
    "ds_read_b64_tr_b16 a[204:205], v11 offset:24576\n\t"
    "v_add_f32 v43, v43, v94\n\t"
    "v_mfma_f32_16x16x32_bf16 v[152:155], a[184:187], v[72:75], v[152:155]\n\t"
    "v_exp_f32 v95, v95\n\t"
    "v_mfma_f32_16x16x32_bf16 v[156:159], a[188:191], v[72:75], v[156:159]\n\t"
    "s_waitcnt lgkmcnt(7)\n\t"
    "v_add_f32 v43, v43, v95\n\t"
    "v_cvt_pk_bf16_f32 v79, v94, v95\n\t"
    "v_mfma_f32_16x16x32_bf16 v[88:91], a[128:131], a[0:3], v[48:51]\n\t"
    "s_mov_b32 s59, 94208\n\t"
    "v_exp_f32 v80, v80\n\t"
    "v_mfma_f32_16x16x32_bf16 v[88:91], a[132:135], a[4:7], v[88:91]\n\t"
    "ds_read_b64_tr_b16 a[206:207], v11 offset:28672\n\t"
    "v_add_f32 v44, v44, v80\n\t"
    "v_mfma_f32_16x16x32_bf16 v[88:91], a[136:139], a[8:11], v[88:91]\n\t"
    "buffer_load_dwordx4 v18, s[48:51], s59 offen nt lds\n\t"
    "v_exp_f32 v81, v81\n\t"
    "v_mfma_f32_16x16x32_bf16 v[88:91], a[140:143], a[12:15], v[88:91]\n\t"
    "ds_read_b64_tr_b16 a[208:209], v8 offset:25600\n\t"
    "v_add_f32 v44, v44, v81\n\t"
    "v_cvt_pk_bf16_f32 v72, v80, v81\n\t"
    "v_mfma_f32_16x16x32_bf16 v[92:95], a[144:147], a[0:3], v[48:51]\n\t"
    "buffer_load_dwordx4 v18, s[48:51], s59 offen offset:2048 nt lds\n\t"
    "v_exp_f32 v82, v82\n\t"
    "v_mfma_f32_16x16x32_bf16 v[92:95], a[148:151], a[4:7], v[92:95]\n\t"
    "ds_read_b64_tr_b16 a[210:211], v8 offset:29696\n\t"
    "v_add_f32 v44, v44, v82\n\t"
    "v_mfma_f32_16x16x32_bf16 v[92:95], a[152:155], a[8:11], v[92:95]\n\t"
    "s_add_u32 m0, s72, 62464\n\t"
    "v_exp_f32 v83, v83\n\t"
    "v_mfma_f32_16x16x32_bf16 v[92:95], a[156:159], a[12:15], v[92:95]\n\t"
    "v_add_f32 v44, v44, v83\n\t"
    "v_cvt_pk_bf16_f32 v73, v82, v83\n\t"
    "v_mfma_f32_16x16x32_bf16 v[160:163], a[160:163], v[76:79], v[160:163]\n\t"
    "s_mov_b32 s59, 94336\n\t"
    "v_exp_f32 v84, v84\n\t"
    "v_mfma_f32_16x16x32_bf16 v[164:167], a[164:167], v[76:79], v[164:167]\n\t"
    "ds_read_b64_tr_b16 a[212:213], v9 offset:25600\n\t"
    "v_add_f32 v44, v44, v84\n\t"
    "v_mfma_f32_16x16x32_bf16 v[168:171], a[168:171], v[76:79], v[168:171]\n\t"
    "buffer_load_dwordx4 v18, s[48:51], s59 offen nt lds\n\t"
    "v_exp_f32 v85, v85\n\t"
    "v_mfma_f32_16x16x32_bf16 v[172:175], a[172:175], v[76:79], v[172:175]\n\t"
    "v_add_f32 v44, v44, v85\n\t"
    "v_cvt_pk_bf16_f32 v74, v84, v85\n\t"
    "v_mfma_f32_16x16x32_bf16 v[176:179], a[176:179], v[76:79], v[176:179]\n\t"
    "buffer_load_dwordx4 v18, s[48:51], s59 offen offset:2048 nt lds\n\t"
    "v_exp_f32 v86, v86\n\t"
    "v_mfma_f32_16x16x32_bf16 v[180:183], a[180:183], v[76:79], v[180:183]\n\t"
    "ds_read_b64_tr_b16 a[214:215], v9 offset:29696\n\t"
    "v_add_f32 v44, v44, v86\n\t"
    "v_mfma_f32_16x16x32_bf16 v[184:187], a[184:187], v[76:79], v[184:187]\n\t"
    "v_exp_f32 v87, v87\n\t"
    "v_mfma_f32_16x16x32_bf16 v[188:191], a[188:191], v[76:79], v[188:191]\n\t"
    "v_add_f32 v44, v44, v87\n\t"
    "v_cvt_pk_bf16_f32 v75, v86, v87\n\t"
    "v_mfma_f32_16x16x32_bf16 v[80:83], a[128:131], a[16:19], v[52:55]\n\t"
    "v_exp_f32 v88, v88\n\t"
    "v_mfma_f32_16x16x32_bf16 v[80:83], a[132:135], a[20:23], v[80:83]\n\t"
    "ds_read_b64_tr_b16 a[216:217], v10 offset:25600\n\t"
    "v_add_f32 v40, v40, v88\n\t"
    "v_mfma_f32_16x16x32_bf16 v[80:83], a[136:139], a[24:27], v[80:83]\n\t"
    "v_exp_f32 v89, v89\n\t"
    "v_mfma_f32_16x16x32_bf16 v[80:83], a[140:143], a[28:31], v[80:83]\n\t"
    "v_add_f32 v40, v40, v89\n\t"
    "v_cvt_pk_bf16_f32 v76, v88, v89\n\t"
    "v_mfma_f32_16x16x32_bf16 v[84:87], a[144:147], a[16:19], v[52:55]\n\t"
    "v_exp_f32 v90, v90\n\t"
    "v_mfma_f32_16x16x32_bf16 v[84:87], a[148:151], a[20:23], v[84:87]\n\t"
    "ds_read_b64_tr_b16 a[218:219], v10 offset:29696\n\t"
    "v_add_f32 v40, v40, v90\n\t"
    "v_mfma_f32_16x16x32_bf16 v[84:87], a[152:155], a[24:27], v[84:87]\n\t"
    "v_exp_f32 v91, v91\n\t"
    "v_mfma_f32_16x16x32_bf16 v[84:87], a[156:159], a[28:31], v[84:87]\n\t"
    "v_add_f32 v40, v40, v91\n\t"
    "v_cvt_pk_bf16_f32 v77, v90, v91\n\t"
    "v_mfma_f32_16x16x32_bf16 v[192:195], a[160:163], v[72:75], v[192:195]\n\t"
    "v_exp_f32 v92, v92\n\t"
    "v_mfma_f32_16x16x32_bf16 v[196:199], a[164:167], v[72:75], v[196:199]\n\t"
    "ds_read_b64_tr_b16 a[220:221], v11 offset:25600\n\t"
    "v_add_f32 v40, v40, v92\n\t"
    "v_mfma_f32_16x16x32_bf16 v[200:203], a[168:171], v[72:75], v[200:203]\n\t"
    "v_exp_f32 v93, v93\n\t"
    "v_mfma_f32_16x16x32_bf16 v[204:207], a[172:175], v[72:75], v[204:207]\n\t"
    "v_add_f32 v40, v40, v93\n\t"
    "v_cvt_pk_bf16_f32 v78, v92, v93\n\t"
    "v_mfma_f32_16x16x32_bf16 v[208:211], a[176:179], v[72:75], v[208:211]\n\t"
    "v_exp_f32 v94, v94\n\t"
    "v_mfma_f32_16x16x32_bf16 v[212:215], a[180:183], v[72:75], v[212:215]\n\t"
    "ds_read_b64_tr_b16 a[222:223], v11 offset:29696\n\t"
    "v_add_f32 v40, v40, v94\n\t"
    "v_mfma_f32_16x16x32_bf16 v[216:219], a[184:187], v[72:75], v[216:219]\n\t"
    "v_exp_f32 v95, v95\n\t"
    "v_mfma_f32_16x16x32_bf16 v[220:223], a[188:191], v[72:75], v[220:223]\n\t"
    "s_waitcnt vmcnt(16)\n\t"
    "s_barrier\n\t"
    "v_add_f32 v40, v40, v95\n\t"
    "v_cvt_pk_bf16_f32 v79, v94, v95\n\t"
    "v_mfma_f32_16x16x32_bf16 v[88:91], a[128:131], a[32:35], v[56:59]\n\t"
    "s_add_u32 m0, s72, 65536\n\t"
    "v_exp_f32 v80, v80\n\t"
    "v_mfma_f32_16x16x32_bf16 v[88:91], a[132:135], a[36:39], v[88:91]\n\t"
    "ds_read_b128 a[96:99], v4 offset:32768\n\t"
    "v_add_f32 v41, v41, v80\n\t"
    "v_mfma_f32_16x16x32_bf16 v[88:91], a[136:139], a[40:43], v[88:91]\n\t"
    "s_mov_b32 s58, 98304\n\t"
    "v_exp_f32 v81, v81\n\t"
    "v_mfma_f32_16x16x32_bf16 v[88:91], a[140:143], a[44:47], v[88:91]\n\t"
    "ds_read_b128 a[100:103], v5 offset:32768\n\t"
    "v_add_f32 v41, v41, v81\n\t"
    "v_cvt_pk_bf16_f32 v72, v80, v81\n\t"
    "v_mfma_f32_16x16x32_bf16 v[92:95], a[144:147], a[32:35], v[56:59]\n\t"
    "buffer_load_dwordx4 v12, s[44:47], s58 offen nt lds\n\t"
    "v_exp_f32 v82, v82\n\t"
    "v_mfma_f32_16x16x32_bf16 v[92:95], a[148:151], a[36:39], v[92:95]\n\t"
    "ds_read_b128 a[104:107], v6 offset:32768\n\t"
    "v_add_f32 v41, v41, v82\n\t"
    "v_mfma_f32_16x16x32_bf16 v[92:95], a[152:155], a[40:43], v[92:95]\n\t"
    "buffer_load_dwordx4 v13, s[44:47], s58 offen offset:1024 nt lds\n\t"
    "v_exp_f32 v83, v83\n\t"
    "v_mfma_f32_16x16x32_bf16 v[92:95], a[156:159], a[44:47], v[92:95]\n\t"
    "s_waitcnt lgkmcnt(3)\n\t"
    "v_add_f32 v41, v41, v83\n\t"
    "v_cvt_pk_bf16_f32 v73, v82, v83\n\t"
    "v_mfma_f32_16x16x32_bf16 a[224:227], a[192:195], v[76:79], a[224:227]\n\t"
    "buffer_load_dwordx4 v14, s[44:47], s58 offen offset:2048 nt lds\n\t"
    "v_exp_f32 v84, v84\n\t"
    "v_mfma_f32_16x16x32_bf16 a[228:231], a[196:199], v[76:79], a[228:231]\n\t"
    "ds_read_b128 a[108:111], v7 offset:32768\n\t"
    "v_add_f32 v41, v41, v84\n\t"
    "v_mfma_f32_16x16x32_bf16 a[232:235], a[200:203], v[76:79], a[232:235]\n\t"
    "buffer_load_dwordx4 v15, s[44:47], s58 offen offset:3072 nt lds\n\t"
    "v_exp_f32 v85, v85\n\t"
    "v_mfma_f32_16x16x32_bf16 a[236:239], a[204:207], v[76:79], a[236:239]\n\t"
    "v_add_f32 v41, v41, v85\n\t"
    "v_cvt_pk_bf16_f32 v74, v84, v85\n\t"
    "v_mfma_f32_16x16x32_bf16 a[240:243], a[208:211], v[76:79], a[240:243]\n\t"
    "s_add_u32 m0, s72, 69632\n\t"
    "v_exp_f32 v86, v86\n\t"
    "v_mfma_f32_16x16x32_bf16 a[244:247], a[212:215], v[76:79], a[244:247]\n\t"
    "ds_read_b128 a[112:115], v4 offset:36864\n\t"
    "v_add_f32 v41, v41, v86\n\t"
    "v_mfma_f32_16x16x32_bf16 a[248:251], a[216:219], v[76:79], a[248:251]\n\t"
    "v_exp_f32 v87, v87\n\t"
    "v_mfma_f32_16x16x32_bf16 a[252:255], a[220:223], v[76:79], a[252:255]\n\t"
    "v_add_f32 v41, v41, v87\n\t"
    "v_cvt_pk_bf16_f32 v75, v86, v87\n\t"
    "v_mfma_f32_16x16x32_bf16 v[80:83], a[128:131], a[48:51], v[60:63]\n\t"
    "s_mov_b32 s58, 102400\n\t"
    "v_exp_f32 v88, v88\n\t"
    "v_mfma_f32_16x16x32_bf16 v[80:83], a[132:135], a[52:55], v[80:83]\n\t"
    "ds_read_b128 a[116:119], v5 offset:36864\n\t"
    "v_add_f32 v42, v42, v88\n\t"
    "v_mfma_f32_16x16x32_bf16 v[80:83], a[136:139], a[56:59], v[80:83]\n\t"
    "buffer_load_dwordx4 v12, s[44:47], s58 offen nt lds\n\t"
    "v_exp_f32 v89, v89\n\t"
    "v_mfma_f32_16x16x32_bf16 v[80:83], a[140:143], a[60:63], v[80:83]\n\t"
    "ds_read_b128 a[120:123], v6 offset:36864\n\t"
    "v_add_f32 v42, v42, v89\n\t"
    "v_cvt_pk_bf16_f32 v76, v88, v89\n\t"
    "v_mfma_f32_16x16x32_bf16 v[84:87], a[144:147], a[48:51], v[60:63]\n\t"
    "buffer_load_dwordx4 v13, s[44:47], s58 offen offset:1024 nt lds\n\t"
    "v_exp_f32 v90, v90\n\t"
    "v_mfma_f32_16x16x32_bf16 v[84:87], a[148:151], a[52:55], v[84:87]\n\t"
    "ds_read_b128 a[124:127], v7 offset:36864\n\t"
    "v_add_f32 v42, v42, v90\n\t"
    "v_mfma_f32_16x16x32_bf16 v[84:87], a[152:155], a[56:59], v[84:87]\n\t"
    "buffer_load_dwordx4 v14, s[44:47], s58 offen offset:2048 nt lds\n\t"
    "v_exp_f32 v91, v91\n\t"
    "v_mfma_f32_16x16x32_bf16 v[84:87], a[156:159], a[60:63], v[84:87]\n\t"
    "v_add_f32 v42, v42, v91\n\t"
    "v_cvt_pk_bf16_f32 v77, v90, v91\n\t"
    "v_mfma_f32_16x16x32_bf16 v[96:99], a[192:195], v[72:75], v[96:99]\n\t"
    "buffer_load_dwordx4 v15, s[44:47], s58 offen offset:3072 nt lds\n\t"
    "v_exp_f32 v92, v92\n\t"
    "v_mfma_f32_16x16x32_bf16 v[100:103], a[196:199], v[72:75], v[100:103]\n\t"
    "ds_read_b64_tr_b16 a[160:161], v8 offset:40960\n\t"
    "v_add_f32 v42, v42, v92\n\t"
    "v_mfma_f32_16x16x32_bf16 v[104:107], a[200:203], v[72:75], v[104:107]\n\t"
    "s_add_u32 m0, s72, 73728\n\t"
    "v_exp_f32 v93, v93\n\t"
    "v_mfma_f32_16x16x32_bf16 v[108:111], a[204:207], v[72:75], v[108:111]\n\t"
    "v_add_f32 v42, v42, v93\n\t"
    "v_cvt_pk_bf16_f32 v78, v92, v93\n\t"
    "v_mfma_f32_16x16x32_bf16 v[112:115], a[208:211], v[72:75], v[112:115]\n\t"
    "s_mov_b32 s59, 98304\n\t"
    "v_exp_f32 v94, v94\n\t"
    "v_mfma_f32_16x16x32_bf16 v[116:119], a[212:215], v[72:75], v[116:119]\n\t"
    "ds_read_b64_tr_b16 a[162:163], v8 offset:45056\n\t"
    "v_add_f32 v42, v42, v94\n\t"
    "v_mfma_f32_16x16x32_bf16 v[120:123], a[216:219], v[72:75], v[120:123]\n\t"
    "v_exp_f32 v95, v95\n\t"
    "v_mfma_f32_16x16x32_bf16 v[124:127], a[220:223], v[72:75], v[124:127]\n\t"
    "v_add_f32 v42, v42, v95\n\t"
    "v_cvt_pk_bf16_f32 v79, v94, v95\n\t"
    "v_mfma_f32_16x16x32_bf16 v[88:91], a[128:131], a[64:67], v[64:67]\n\t"
    "buffer_load_dwordx4 v18, s[48:51], s59 offen nt lds\n\t"
    "v_exp_f32 v80, v80\n\t"
    "v_mfma_f32_16x16x32_bf16 v[88:91], a[132:135], a[68:71], v[88:91]\n\t"
    "ds_read_b64_tr_b16 a[164:165], v9 offset:40960\n\t"
    "v_add_f32 v43, v43, v80\n\t"
    "v_mfma_f32_16x16x32_bf16 v[88:91], a[136:139], a[72:75], v[88:91]\n\t"
    "buffer_load_dwordx4 v18, s[48:51], s59 offen offset:2048 nt lds\n\t"
    "v_exp_f32 v81, v81\n\t"
    "v_mfma_f32_16x16x32_bf16 v[88:91], a[140:143], a[76:79], v[88:91]\n\t"
    "ds_read_b64_tr_b16 a[166:167], v9 offset:45056\n\t"
    "v_add_f32 v43, v43, v81\n\t"
    "v_cvt_pk_bf16_f32 v72, v80, v81\n\t"
    "v_mfma_f32_16x16x32_bf16 v[92:95], a[144:147], a[64:67], v[64:67]\n\t"
    "s_add_u32 m0, s72, 74752\n\t"
    "v_exp_f32 v82, v82\n\t"
    "v_mfma_f32_16x16x32_bf16 v[92:95], a[148:151], a[68:71], v[92:95]\n\t"
    "ds_read_b64_tr_b16 a[168:169], v10 offset:40960\n\t"
    "v_add_f32 v43, v43, v82\n\t"
    "v_mfma_f32_16x16x32_bf16 v[92:95], a[152:155], a[72:75], v[92:95]\n\t"
    "s_mov_b32 s59, 98432\n\t"
    "v_exp_f32 v83, v83\n\t"
    "v_mfma_f32_16x16x32_bf16 v[92:95], a[156:159], a[76:79], v[92:95]\n\t"
    "v_add_f32 v43, v43, v83\n\t"
    "v_cvt_pk_bf16_f32 v73, v82, v83\n\t"
    "v_mfma_f32_16x16x32_bf16 v[128:131], a[192:195], v[76:79], v[128:131]\n\t"
    "buffer_load_dwordx4 v18, s[48:51], s59 offen nt lds\n\t"
    "v_exp_f32 v84, v84\n\t"
    "v_mfma_f32_16x16x32_bf16 v[132:135], a[196:199], v[76:79], v[132:135]\n\t"
    "ds_read_b64_tr_b16 a[170:171], v10 offset:45056\n\t"
    "v_add_f32 v43, v43, v84\n\t"
    "v_mfma_f32_16x16x32_bf16 v[136:139], a[200:203], v[76:79], v[136:139]\n\t"
    "buffer_load_dwordx4 v18, s[48:51], s59 offen offset:2048 nt lds\n\t"
    "v_exp_f32 v85, v85\n\t"
    "v_mfma_f32_16x16x32_bf16 v[140:143], a[204:207], v[76:79], v[140:143]\n\t"
    "v_add_f32 v43, v43, v85\n\t"
    "v_cvt_pk_bf16_f32 v74, v84, v85\n\t"
    "v_mfma_f32_16x16x32_bf16 v[144:147], a[208:211], v[76:79], v[144:147]\n\t"
    "s_add_u32 m0, s72, 77824\n\t"
    "v_exp_f32 v86, v86\n\t"
    "v_mfma_f32_16x16x32_bf16 v[148:151], a[212:215], v[76:79], v[148:151]\n\t"
    "ds_read_b64_tr_b16 a[172:173], v11 offset:40960\n\t"
    "v_add_f32 v43, v43, v86\n\t"
    "v_mfma_f32_16x16x32_bf16 v[152:155], a[216:219], v[76:79], v[152:155]\n\t"
    "v_exp_f32 v87, v87\n\t"
    "v_mfma_f32_16x16x32_bf16 v[156:159], a[220:223], v[76:79], v[156:159]\n\t"
    "s_waitcnt lgkmcnt(7)\n\t"
    "v_add_f32 v43, v43, v87\n\t"
    "v_cvt_pk_bf16_f32 v75, v86, v87\n\t"
    "v_mfma_f32_16x16x32_bf16 v[80:83], a[96:99], a[0:3], v[48:51]\n\t"
    "s_mov_b32 s59, 102400\n\t"
    "v_exp_f32 v88, v88\n\t"
    "v_mfma_f32_16x16x32_bf16 v[80:83], a[100:103], a[4:7], v[80:83]\n\t"
    "ds_read_b64_tr_b16 a[174:175], v11 offset:45056\n\t"
    "v_add_f32 v44, v44, v88\n\t"
    "v_mfma_f32_16x16x32_bf16 v[80:83], a[104:107], a[8:11], v[80:83]\n\t"
    "buffer_load_dwordx4 v18, s[48:51], s59 offen nt lds\n\t"
    "v_exp_f32 v89, v89\n\t"
    "v_mfma_f32_16x16x32_bf16 v[80:83], a[108:111], a[12:15], v[80:83]\n\t"
    "ds_read_b64_tr_b16 a[176:177], v8 offset:41984\n\t"
    "v_add_f32 v44, v44, v89\n\t"
    "v_cvt_pk_bf16_f32 v76, v88, v89\n\t"
    "v_mfma_f32_16x16x32_bf16 v[84:87], a[112:115], a[0:3], v[48:51]\n\t"
    "buffer_load_dwordx4 v18, s[48:51], s59 offen offset:2048 nt lds\n\t"
    "v_exp_f32 v90, v90\n\t"
    "v_mfma_f32_16x16x32_bf16 v[84:87], a[116:119], a[4:7], v[84:87]\n\t"
    "ds_read_b64_tr_b16 a[178:179], v8 offset:46080\n\t"
    "v_add_f32 v44, v44, v90\n\t"
    "v_mfma_f32_16x16x32_bf16 v[84:87], a[120:123], a[8:11], v[84:87]\n\t"
    "s_add_u32 m0, s72, 78848\n\t"
    "v_exp_f32 v91, v91\n\t"
    "v_mfma_f32_16x16x32_bf16 v[84:87], a[124:127], a[12:15], v[84:87]\n\t"
    "v_add_f32 v44, v44, v91\n\t"
    "v_cvt_pk_bf16_f32 v77, v90, v91\n\t"
    "v_mfma_f32_16x16x32_bf16 v[160:163], a[192:195], v[72:75], v[160:163]\n\t"
    "s_mov_b32 s59, 102528\n\t"
    "v_exp_f32 v92, v92\n\t"
    "v_mfma_f32_16x16x32_bf16 v[164:167], a[196:199], v[72:75], v[164:167]\n\t"
    "ds_read_b64_tr_b16 a[180:181], v9 offset:41984\n\t"
    "v_add_f32 v44, v44, v92\n\t"
    "v_mfma_f32_16x16x32_bf16 v[168:171], a[200:203], v[72:75], v[168:171]\n\t"
    "buffer_load_dwordx4 v18, s[48:51], s59 offen nt lds\n\t"
    "v_exp_f32 v93, v93\n\t"
    "v_mfma_f32_16x16x32_bf16 v[172:175], a[204:207], v[72:75], v[172:175]\n\t"
    "v_add_f32 v44, v44, v93\n\t"
    "v_cvt_pk_bf16_f32 v78, v92, v93\n\t"
    "v_mfma_f32_16x16x32_bf16 v[176:179], a[208:211], v[72:75], v[176:179]\n\t"
    "buffer_load_dwordx4 v18, s[48:51], s59 offen offset:2048 nt lds\n\t"
    "v_exp_f32 v94, v94\n\t"
    "v_mfma_f32_16x16x32_bf16 v[180:183], a[212:215], v[72:75], v[180:183]\n\t"
    "ds_read_b64_tr_b16 a[182:183], v9 offset:46080\n\t"
    "v_add_f32 v44, v44, v94\n\t"
    "v_mfma_f32_16x16x32_bf16 v[184:187], a[216:219], v[72:75], v[184:187]\n\t"
    "v_exp_f32 v95, v95\n\t"
    "v_mfma_f32_16x16x32_bf16 v[188:191], a[220:223], v[72:75], v[188:191]\n\t"
    "v_add_f32 v44, v44, v95\n\t"
    "v_cvt_pk_bf16_f32 v79, v94, v95\n\t"
    "v_mfma_f32_16x16x32_bf16 v[88:91], a[96:99], a[16:19], v[52:55]\n\t"
    "v_exp_f32 v80, v80\n\t"
    "v_mfma_f32_16x16x32_bf16 v[88:91], a[100:103], a[20:23], v[88:91]\n\t"
    "ds_read_b64_tr_b16 a[184:185], v10 offset:41984\n\t"
    "v_add_f32 v40, v40, v80\n\t"
    "v_mfma_f32_16x16x32_bf16 v[88:91], a[104:107], a[24:27], v[88:91]\n\t"
    "v_exp_f32 v81, v81\n\t"
    "v_mfma_f32_16x16x32_bf16 v[88:91], a[108:111], a[28:31], v[88:91]\n\t"
    "v_add_f32 v40, v40, v81\n\t"
    "v_cvt_pk_bf16_f32 v72, v80, v81\n\t"
    "v_mfma_f32_16x16x32_bf16 v[92:95], a[112:115], a[16:19], v[52:55]\n\t"
    "v_exp_f32 v82, v82\n\t"
    "v_mfma_f32_16x16x32_bf16 v[92:95], a[116:119], a[20:23], v[92:95]\n\t"
    "ds_read_b64_tr_b16 a[186:187], v10 offset:46080\n\t"
    "v_add_f32 v40, v40, v82\n\t"
    "v_mfma_f32_16x16x32_bf16 v[92:95], a[120:123], a[24:27], v[92:95]\n\t"
    "v_exp_f32 v83, v83\n\t"
    "v_mfma_f32_16x16x32_bf16 v[92:95], a[124:127], a[28:31], v[92:95]\n\t"
    "v_add_f32 v40, v40, v83\n\t"
    "v_cvt_pk_bf16_f32 v73, v82, v83\n\t"
    "v_mfma_f32_16x16x32_bf16 v[192:195], a[192:195], v[76:79], v[192:195]\n\t"
    "v_exp_f32 v84, v84\n\t"
    "v_mfma_f32_16x16x32_bf16 v[196:199], a[196:199], v[76:79], v[196:199]\n\t"
    "ds_read_b64_tr_b16 a[188:189], v11 offset:41984\n\t"
    "v_add_f32 v40, v40, v84\n\t"
    "v_mfma_f32_16x16x32_bf16 v[200:203], a[200:203], v[76:79], v[200:203]\n\t"
    "v_exp_f32 v85, v85\n\t"
    "v_mfma_f32_16x16x32_bf16 v[204:207], a[204:207], v[76:79], v[204:207]\n\t"
    "v_add_f32 v40, v40, v85\n\t"
    "v_cvt_pk_bf16_f32 v74, v84, v85\n\t"
    "v_mfma_f32_16x16x32_bf16 v[208:211], a[208:211], v[76:79], v[208:211]\n\t"
    "v_exp_f32 v86, v86\n\t"
    "v_mfma_f32_16x16x32_bf16 v[212:215], a[212:215], v[76:79], v[212:215]\n\t"
    "ds_read_b64_tr_b16 a[190:191], v11 offset:46080\n\t"
    "v_add_f32 v40, v40, v86\n\t"
    "v_mfma_f32_16x16x32_bf16 v[216:219], a[216:219], v[76:79], v[216:219]\n\t"
    "v_exp_f32 v87, v87\n\t"
    "v_mfma_f32_16x16x32_bf16 v[220:223], a[220:223], v[76:79], v[220:223]\n\t"
    "s_waitcnt vmcnt(16)\n\t"
    "s_barrier\n\t"
    "v_add_f32 v40, v40, v87\n\t"
    "v_cvt_pk_bf16_f32 v75, v86, v87\n\t"
    "v_mfma_f32_16x16x32_bf16 v[80:83], a[96:99], a[32:35], v[56:59]\n\t"
    "s_add_u32 m0, s72, 81920\n\t"
    "v_exp_f32 v88, v88\n\t"
    "v_mfma_f32_16x16x32_bf16 v[80:83], a[100:103], a[36:39], v[80:83]\n\t"
    "ds_read_b128 a[128:131], v4 offset:49152\n\t"
    "v_add_f32 v41, v41, v88\n\t"
    "v_mfma_f32_16x16x32_bf16 v[80:83], a[104:107], a[40:43], v[80:83]\n\t"
    "s_mov_b32 s58, 106496\n\t"
    "v_exp_f32 v89, v89\n\t"
    "v_mfma_f32_16x16x32_bf16 v[80:83], a[108:111], a[44:47], v[80:83]\n\t"
    "ds_read_b128 a[132:135], v5 offset:49152\n\t"
    "v_add_f32 v41, v41, v89\n\t"
    "v_cvt_pk_bf16_f32 v76, v88, v89\n\t"
    "v_mfma_f32_16x16x32_bf16 v[84:87], a[112:115], a[32:35], v[56:59]\n\t"
    "buffer_load_dwordx4 v12, s[44:47], s58 offen nt lds\n\t"
    "v_exp_f32 v90, v90\n\t"
    "v_mfma_f32_16x16x32_bf16 v[84:87], a[116:119], a[36:39], v[84:87]\n\t"
    "ds_read_b128 a[136:139], v6 offset:49152\n\t"
    "v_add_f32 v41, v41, v90\n\t"
    "v_mfma_f32_16x16x32_bf16 v[84:87], a[120:123], a[40:43], v[84:87]\n\t"
    "buffer_load_dwordx4 v13, s[44:47], s58 offen offset:1024 nt lds\n\t"
    "v_exp_f32 v91, v91\n\t"
    "v_mfma_f32_16x16x32_bf16 v[84:87], a[124:127], a[44:47], v[84:87]\n\t"
    "s_waitcnt lgkmcnt(3)\n\t"
    "v_add_f32 v41, v41, v91\n\t"
    "v_cvt_pk_bf16_f32 v77, v90, v91\n\t"
    "v_mfma_f32_16x16x32_bf16 a[224:227], a[160:163], v[72:75], a[224:227]\n\t"
    "buffer_load_dwordx4 v14, s[44:47], s58 offen offset:2048 nt lds\n\t"
    "v_exp_f32 v92, v92\n\t"
    "v_mfma_f32_16x16x32_bf16 a[228:231], a[164:167], v[72:75], a[228:231]\n\t"
    "ds_read_b128 a[140:143], v7 offset:49152\n\t"
    "v_add_f32 v41, v41, v92\n\t"
    "v_mfma_f32_16x16x32_bf16 a[232:235], a[168:171], v[72:75], a[232:235]\n\t"
    "buffer_load_dwordx4 v15, s[44:47], s58 offen offset:3072 nt lds\n\t"
    "v_exp_f32 v93, v93\n\t"
    "v_mfma_f32_16x16x32_bf16 a[236:239], a[172:175], v[72:75], a[236:239]\n\t"
    "v_add_f32 v41, v41, v93\n\t"
    "v_cvt_pk_bf16_f32 v78, v92, v93\n\t"
    "v_mfma_f32_16x16x32_bf16 a[240:243], a[176:179], v[72:75], a[240:243]\n\t"
    "s_add_u32 m0, s72, 86016\n\t"
    "v_exp_f32 v94, v94\n\t"
    "v_mfma_f32_16x16x32_bf16 a[244:247], a[180:183], v[72:75], a[244:247]\n\t"
    "ds_read_b128 a[144:147], v4 offset:53248\n\t"
    "v_add_f32 v41, v41, v94\n\t"
    "v_mfma_f32_16x16x32_bf16 a[248:251], a[184:187], v[72:75], a[248:251]\n\t"
    "v_exp_f32 v95, v95\n\t"
    "v_mfma_f32_16x16x32_bf16 a[252:255], a[188:191], v[72:75], a[252:255]\n\t"
    "v_add_f32 v41, v41, v95\n\t"
    "v_cvt_pk_bf16_f32 v79, v94, v95\n\t"
    "v_mfma_f32_16x16x32_bf16 v[88:91], a[96:99], a[48:51], v[60:63]\n\t"
    "s_mov_b32 s58, 110592\n\t"
    "v_exp_f32 v80, v80\n\t"
    "v_mfma_f32_16x16x32_bf16 v[88:91], a[100:103], a[52:55], v[88:91]\n\t"
    "ds_read_b128 a[148:151], v5 offset:53248\n\t"
    "v_add_f32 v42, v42, v80\n\t"
    "v_mfma_f32_16x16x32_bf16 v[88:91], a[104:107], a[56:59], v[88:91]\n\t"
    "buffer_load_dwordx4 v12, s[44:47], s58 offen nt lds\n\t"
    "v_exp_f32 v81, v81\n\t"
    "v_mfma_f32_16x16x32_bf16 v[88:91], a[108:111], a[60:63], v[88:91]\n\t"
    "ds_read_b128 a[152:155], v6 offset:53248\n\t"
    "v_add_f32 v42, v42, v81\n\t"
    "v_cvt_pk_bf16_f32 v72, v80, v81\n\t"
    "v_mfma_f32_16x16x32_bf16 v[92:95], a[112:115], a[48:51], v[60:63]\n\t"
    "buffer_load_dwordx4 v13, s[44:47], s58 offen offset:1024 nt lds\n\t"
    "v_exp_f32 v82, v82\n\t"
    "v_mfma_f32_16x16x32_bf16 v[92:95], a[116:119], a[52:55], v[92:95]\n\t"
    "ds_read_b128 a[156:159], v7 offset:53248\n\t"
    "v_add_f32 v42, v42, v82\n\t"
    "v_mfma_f32_16x16x32_bf16 v[92:95], a[120:123], a[56:59], v[92:95]\n\t"
    "buffer_load_dwordx4 v14, s[44:47], s58 offen offset:2048 nt lds\n\t"
    "v_exp_f32 v83, v83\n\t"
    "v_mfma_f32_16x16x32_bf16 v[92:95], a[124:127], a[60:63], v[92:95]\n\t"
    "v_add_f32 v42, v42, v83\n\t"
    "v_cvt_pk_bf16_f32 v73, v82, v83\n\t"
    "v_mfma_f32_16x16x32_bf16 v[96:99], a[160:163], v[76:79], v[96:99]\n\t"
    "buffer_load_dwordx4 v15, s[44:47], s58 offen offset:3072 nt lds\n\t"
    "v_exp_f32 v84, v84\n\t"
    "v_mfma_f32_16x16x32_bf16 v[100:103], a[164:167], v[76:79], v[100:103]\n\t"
    "ds_read_b64_tr_b16 a[192:193], v8 offset:57344\n\t"
    "v_add_f32 v42, v42, v84\n\t"
    "v_mfma_f32_16x16x32_bf16 v[104:107], a[168:171], v[76:79], v[104:107]\n\t"
    "s_add_u32 m0, s72, 90112\n\t"
    "v_exp_f32 v85, v85\n\t"
    "v_mfma_f32_16x16x32_bf16 v[108:111], a[172:175], v[76:79], v[108:111]\n\t"
    "v_add_f32 v42, v42, v85\n\t"
    "v_cvt_pk_bf16_f32 v74, v84, v85\n\t"
    "v_mfma_f32_16x16x32_bf16 v[112:115], a[176:179], v[76:79], v[112:115]\n\t"
    "s_mov_b32 s59, 106496\n\t"
    "v_exp_f32 v86, v86\n\t"
    "v_mfma_f32_16x16x32_bf16 v[116:119], a[180:183], v[76:79], v[116:119]\n\t"
    "ds_read_b64_tr_b16 a[194:195], v8 offset:61440\n\t"
    "v_add_f32 v42, v42, v86\n\t"
    "v_mfma_f32_16x16x32_bf16 v[120:123], a[184:187], v[76:79], v[120:123]\n\t"
    "v_exp_f32 v87, v87\n\t"
    "v_mfma_f32_16x16x32_bf16 v[124:127], a[188:191], v[76:79], v[124:127]\n\t"
    "v_add_f32 v42, v42, v87\n\t"
    "v_cvt_pk_bf16_f32 v75, v86, v87\n\t"
    "v_mfma_f32_16x16x32_bf16 v[80:83], a[96:99], a[64:67], v[64:67]\n\t"
    "buffer_load_dwordx4 v18, s[48:51], s59 offen nt lds\n\t"
    "v_exp_f32 v88, v88\n\t"
    "v_mfma_f32_16x16x32_bf16 v[80:83], a[100:103], a[68:71], v[80:83]\n\t"
    "ds_read_b64_tr_b16 a[196:197], v9 offset:57344\n\t"
    "v_add_f32 v43, v43, v88\n\t"
    "v_mfma_f32_16x16x32_bf16 v[80:83], a[104:107], a[72:75], v[80:83]\n\t"
    "buffer_load_dwordx4 v18, s[48:51], s59 offen offset:2048 nt lds\n\t"
    "v_exp_f32 v89, v89\n\t"
    "v_mfma_f32_16x16x32_bf16 v[80:83], a[108:111], a[76:79], v[80:83]\n\t"
    "ds_read_b64_tr_b16 a[198:199], v9 offset:61440\n\t"
    "v_add_f32 v43, v43, v89\n\t"
    "v_cvt_pk_bf16_f32 v76, v88, v89\n\t"
    "v_mfma_f32_16x16x32_bf16 v[84:87], a[112:115], a[64:67], v[64:67]\n\t"
    "s_add_u32 m0, s72, 91136\n\t"
    "v_exp_f32 v90, v90\n\t"
    "v_mfma_f32_16x16x32_bf16 v[84:87], a[116:119], a[68:71], v[84:87]\n\t"
    "ds_read_b64_tr_b16 a[200:201], v10 offset:57344\n\t"
    "v_add_f32 v43, v43, v90\n\t"
    "v_mfma_f32_16x16x32_bf16 v[84:87], a[120:123], a[72:75], v[84:87]\n\t"
    "s_mov_b32 s59, 106624\n\t"
    "v_exp_f32 v91, v91\n\t"
    "v_mfma_f32_16x16x32_bf16 v[84:87], a[124:127], a[76:79], v[84:87]\n\t"
    "v_add_f32 v43, v43, v91\n\t"
    "v_cvt_pk_bf16_f32 v77, v90, v91\n\t"
    "v_mfma_f32_16x16x32_bf16 v[128:131], a[160:163], v[72:75], v[128:131]\n\t"
    "buffer_load_dwordx4 v18, s[48:51], s59 offen nt lds\n\t"
    "v_exp_f32 v92, v92\n\t"
    "v_mfma_f32_16x16x32_bf16 v[132:135], a[164:167], v[72:75], v[132:135]\n\t"
    "ds_read_b64_tr_b16 a[202:203], v10 offset:61440\n\t"
    "v_add_f32 v43, v43, v92\n\t"
    "v_mfma_f32_16x16x32_bf16 v[136:139], a[168:171], v[72:75], v[136:139]\n\t"
    "buffer_load_dwordx4 v18, s[48:51], s59 offen offset:2048 nt lds\n\t"
    "v_exp_f32 v93, v93\n\t"
    "v_mfma_f32_16x16x32_bf16 v[140:143], a[172:175], v[72:75], v[140:143]\n\t"
    "v_add_f32 v43, v43, v93\n\t"
    "v_cvt_pk_bf16_f32 v78, v92, v93\n\t"
    "v_mfma_f32_16x16x32_bf16 v[144:147], a[176:179], v[72:75], v[144:147]\n\t"
    "s_add_u32 m0, s72, 94208\n\t"
    "v_exp_f32 v94, v94\n\t"
    "v_mfma_f32_16x16x32_bf16 v[148:151], a[180:183], v[72:75], v[148:151]\n\t"
    "ds_read_b64_tr_b16 a[204:205], v11 offset:57344\n\t"
    "v_add_f32 v43, v43, v94\n\t"
    "v_mfma_f32_16x16x32_bf16 v[152:155], a[184:187], v[72:75], v[152:155]\n\t"
    "v_exp_f32 v95, v95\n\t"
    "v_mfma_f32_16x16x32_bf16 v[156:159], a[188:191], v[72:75], v[156:159]\n\t"
    "s_waitcnt lgkmcnt(7)\n\t"
    "v_add_f32 v43, v43, v95\n\t"
    "v_cvt_pk_bf16_f32 v79, v94, v95\n\t"
    "v_mfma_f32_16x16x32_bf16 v[88:91], a[128:131], a[0:3], v[48:51]\n\t"
    "s_mov_b32 s59, 110592\n\t"
    "v_exp_f32 v80, v80\n\t"
    "v_mfma_f32_16x16x32_bf16 v[88:91], a[132:135], a[4:7], v[88:91]\n\t"
    "ds_read_b64_tr_b16 a[206:207], v11 offset:61440\n\t"
    "v_add_f32 v44, v44, v80\n\t"
    "v_mfma_f32_16x16x32_bf16 v[88:91], a[136:139], a[8:11], v[88:91]\n\t"
    "buffer_load_dwordx4 v18, s[48:51], s59 offen nt lds\n\t"
    "v_exp_f32 v81, v81\n\t"
    "v_mfma_f32_16x16x32_bf16 v[88:91], a[140:143], a[12:15], v[88:91]\n\t"
    "ds_read_b64_tr_b16 a[208:209], v8 offset:58368\n\t"
    "v_add_f32 v44, v44, v81\n\t"
    "v_cvt_pk_bf16_f32 v72, v80, v81\n\t"
    "v_mfma_f32_16x16x32_bf16 v[92:95], a[144:147], a[0:3], v[48:51]\n\t"
    "buffer_load_dwordx4 v18, s[48:51], s59 offen offset:2048 nt lds\n\t"
    "v_exp_f32 v82, v82\n\t"
    "v_mfma_f32_16x16x32_bf16 v[92:95], a[148:151], a[4:7], v[92:95]\n\t"
    "ds_read_b64_tr_b16 a[210:211], v8 offset:62464\n\t"
    "v_add_f32 v44, v44, v82\n\t"
    "v_mfma_f32_16x16x32_bf16 v[92:95], a[152:155], a[8:11], v[92:95]\n\t"
    "s_add_u32 m0, s72, 95232\n\t"
    "v_exp_f32 v83, v83\n\t"
    "v_mfma_f32_16x16x32_bf16 v[92:95], a[156:159], a[12:15], v[92:95]\n\t"
    "v_add_f32 v44, v44, v83\n\t"
    "v_cvt_pk_bf16_f32 v73, v82, v83\n\t"
    "v_mfma_f32_16x16x32_bf16 v[160:163], a[160:163], v[76:79], v[160:163]\n\t"
    "s_mov_b32 s59, 110720\n\t"
    "v_exp_f32 v84, v84\n\t"
    "v_mfma_f32_16x16x32_bf16 v[164:167], a[164:167], v[76:79], v[164:167]\n\t"
    "ds_read_b64_tr_b16 a[212:213], v9 offset:58368\n\t"
    "v_add_f32 v44, v44, v84\n\t"
    "v_mfma_f32_16x16x32_bf16 v[168:171], a[168:171], v[76:79], v[168:171]\n\t"
    "buffer_load_dwordx4 v18, s[48:51], s59 offen nt lds\n\t"
    "v_exp_f32 v85, v85\n\t"
    "v_mfma_f32_16x16x32_bf16 v[172:175], a[172:175], v[76:79], v[172:175]\n\t"
    "v_add_f32 v44, v44, v85\n\t"
    "v_cvt_pk_bf16_f32 v74, v84, v85\n\t"
    "v_mfma_f32_16x16x32_bf16 v[176:179], a[176:179], v[76:79], v[176:179]\n\t"
    "buffer_load_dwordx4 v18, s[48:51], s59 offen offset:2048 nt lds\n\t"
    "v_exp_f32 v86, v86\n\t"
    "v_mfma_f32_16x16x32_bf16 v[180:183], a[180:183], v[76:79], v[180:183]\n\t"
    "ds_read_b64_tr_b16 a[214:215], v9 offset:62464\n\t"
    "v_add_f32 v44, v44, v86\n\t"
    "v_mfma_f32_16x16x32_bf16 v[184:187], a[184:187], v[76:79], v[184:187]\n\t"
    "v_exp_f32 v87, v87\n\t"
    "v_mfma_f32_16x16x32_bf16 v[188:191], a[188:191], v[76:79], v[188:191]\n\t"
    "v_add_f32 v44, v44, v87\n\t"
    "v_cvt_pk_bf16_f32 v75, v86, v87\n\t"
    "v_mfma_f32_16x16x32_bf16 v[80:83], a[128:131], a[16:19], v[52:55]\n\t"
    "v_exp_f32 v88, v88\n\t"
    "v_mfma_f32_16x16x32_bf16 v[80:83], a[132:135], a[20:23], v[80:83]\n\t"
    "ds_read_b64_tr_b16 a[216:217], v10 offset:58368\n\t"
    "v_add_f32 v40, v40, v88\n\t"
    "v_xor_b32 v4, 0x10000, v4\n\t"
    "v_mfma_f32_16x16x32_bf16 v[80:83], a[136:139], a[24:27], v[80:83]\n\t"
    "v_exp_f32 v89, v89\n\t"
    "v_mfma_f32_16x16x32_bf16 v[80:83], a[140:143], a[28:31], v[80:83]\n\t"
    "v_add_f32 v40, v40, v89\n\t"
    "v_cvt_pk_bf16_f32 v76, v88, v89\n\t"
    "v_mfma_f32_16x16x32_bf16 v[84:87], a[144:147], a[16:19], v[52:55]\n\t"
    "v_exp_f32 v90, v90\n\t"
    "v_mfma_f32_16x16x32_bf16 v[84:87], a[148:151], a[20:23], v[84:87]\n\t"
    "ds_read_b64_tr_b16 a[218:219], v10 offset:62464\n\t"
    "v_add_f32 v40, v40, v90\n\t"
    "v_xor_b32 v5, 0x10000, v5\n\t"
    "v_mfma_f32_16x16x32_bf16 v[84:87], a[152:155], a[24:27], v[84:87]\n\t"
    "v_exp_f32 v91, v91\n\t"
    "v_mfma_f32_16x16x32_bf16 v[84:87], a[156:159], a[28:31], v[84:87]\n\t"
    "v_add_f32 v40, v40, v91\n\t"
    "v_cvt_pk_bf16_f32 v77, v90, v91\n\t"
    "v_mfma_f32_16x16x32_bf16 v[192:195], a[160:163], v[72:75], v[192:195]\n\t"
    "v_exp_f32 v92, v92\n\t"
    "v_mfma_f32_16x16x32_bf16 v[196:199], a[164:167], v[72:75], v[196:199]\n\t"
    "ds_read_b64_tr_b16 a[220:221], v11 offset:58368\n\t"
    "v_add_f32 v40, v40, v92\n\t"
    "v_xor_b32 v6, 0x10000, v6\n\t"
    "v_mfma_f32_16x16x32_bf16 v[200:203], a[168:171], v[72:75], v[200:203]\n\t"
    "v_exp_f32 v93, v93\n\t"
    "v_mfma_f32_16x16x32_bf16 v[204:207], a[172:175], v[72:75], v[204:207]\n\t"
    "v_add_f32 v40, v40, v93\n\t"
    "v_cvt_pk_bf16_f32 v78, v92, v93\n\t"
    "v_mfma_f32_16x16x32_bf16 v[208:211], a[176:179], v[72:75], v[208:211]\n\t"
    "v_exp_f32 v94, v94\n\t"
    "v_mfma_f32_16x16x32_bf16 v[212:215], a[180:183], v[72:75], v[212:215]\n\t"
    "ds_read_b64_tr_b16 a[222:223], v11 offset:62464\n\t"
    "v_add_f32 v40, v40, v94\n\t"
    "v_xor_b32 v7, 0x10000, v7\n\t"
    "v_mfma_f32_16x16x32_bf16 v[216:219], a[184:187], v[72:75], v[216:219]\n\t"
    "v_exp_f32 v95, v95\n\t"
    "v_mfma_f32_16x16x32_bf16 v[220:223], a[188:191], v[72:75], v[220:223]\n\t"
    "s_waitcnt vmcnt(16)\n\t"
    "s_barrier\n\t"
    "v_add_f32 v40, v40, v95\n\t"
    "v_cvt_pk_bf16_f32 v79, v94, v95\n\t"
    "v_mfma_f32_16x16x32_bf16 v[88:91], a[128:131], a[32:35], v[56:59]\n\t"
    "s_add_u32 m0, s72, 98304\n\t"
    "v_exp_f32 v80, v80\n\t"
    "v_mfma_f32_16x16x32_bf16 v[88:91], a[132:135], a[36:39], v[88:91]\n\t"
    "ds_read_b128 a[96:99], v4 offset:0\n\t"
    "v_add_f32 v41, v41, v80\n\t"
    "v_xor_b32 v8, 0x10000, v8\n\t"
    "v_mfma_f32_16x16x32_bf16 v[88:91], a[136:139], a[40:43], v[88:91]\n\t"
    "s_mov_b32 s58, 114688\n\t"
    "v_exp_f32 v81, v81\n\t"
    "v_mfma_f32_16x16x32_bf16 v[88:91], a[140:143], a[44:47], v[88:91]\n\t"
    "ds_read_b128 a[100:103], v5 offset:0\n\t"
    "v_add_f32 v41, v41, v81\n\t"
    "v_cvt_pk_bf16_f32 v72, v80, v81\n\t"
    "v_mfma_f32_16x16x32_bf16 v[92:95], a[144:147], a[32:35], v[56:59]\n\t"
    "buffer_load_dwordx4 v12, s[44:47], s58 offen nt lds\n\t"
    "v_exp_f32 v82, v82\n\t"
    "v_mfma_f32_16x16x32_bf16 v[92:95], a[148:151], a[36:39], v[92:95]\n\t"
    "ds_read_b128 a[104:107], v6 offset:0\n\t"
    "v_add_f32 v41, v41, v82\n\t"
    "v_xor_b32 v9, 0x10000, v9\n\t"
    "v_mfma_f32_16x16x32_bf16 v[92:95], a[152:155], a[40:43], v[92:95]\n\t"
    "buffer_load_dwordx4 v13, s[44:47], s58 offen offset:1024 nt lds\n\t"
    "v_exp_f32 v83, v83\n\t"
    "v_mfma_f32_16x16x32_bf16 v[92:95], a[156:159], a[44:47], v[92:95]\n\t"
    "s_waitcnt lgkmcnt(3)\n\t"
    "v_add_f32 v41, v41, v83\n\t"
    "v_cvt_pk_bf16_f32 v73, v82, v83\n\t"
    "v_mfma_f32_16x16x32_bf16 a[224:227], a[192:195], v[76:79], a[224:227]\n\t"
    "buffer_load_dwordx4 v14, s[44:47], s58 offen offset:2048 nt lds\n\t"
    "v_exp_f32 v84, v84\n\t"
    "v_mfma_f32_16x16x32_bf16 a[228:231], a[196:199], v[76:79], a[228:231]\n\t"
    "ds_read_b128 a[108:111], v7 offset:0\n\t"
    "v_add_f32 v41, v41, v84\n\t"
    "v_xor_b32 v10, 0x10000, v10\n\t"
    "v_mfma_f32_16x16x32_bf16 a[232:235], a[200:203], v[76:79], a[232:235]\n\t"
    "buffer_load_dwordx4 v15, s[44:47], s58 offen offset:3072 nt lds\n\t"
    "v_exp_f32 v85, v85\n\t"
    "v_mfma_f32_16x16x32_bf16 a[236:239], a[204:207], v[76:79], a[236:239]\n\t"
    "v_add_f32 v41, v41, v85\n\t"
    "v_cvt_pk_bf16_f32 v74, v84, v85\n\t"
    "v_mfma_f32_16x16x32_bf16 a[240:243], a[208:211], v[76:79], a[240:243]\n\t"
    "s_add_u32 m0, s72, 102400\n\t"
    "v_exp_f32 v86, v86\n\t"
    "v_mfma_f32_16x16x32_bf16 a[244:247], a[212:215], v[76:79], a[244:247]\n\t"
    "ds_read_b128 a[112:115], v4 offset:4096\n\t"
    "v_add_f32 v41, v41, v86\n\t"
    "v_xor_b32 v11, 0x10000, v11\n\t"
    "v_mfma_f32_16x16x32_bf16 a[248:251], a[216:219], v[76:79], a[248:251]\n\t"
    "v_exp_f32 v87, v87\n\t"
    "v_mfma_f32_16x16x32_bf16 a[252:255], a[220:223], v[76:79], a[252:255]\n\t"
    "v_add_f32 v41, v41, v87\n\t"
    "v_cvt_pk_bf16_f32 v75, v86, v87\n\t"
    "v_mfma_f32_16x16x32_bf16 v[80:83], a[128:131], a[48:51], v[60:63]\n\t"
    "s_mov_b32 s58, 118784\n\t"
    "v_exp_f32 v88, v88\n\t"
    "v_mfma_f32_16x16x32_bf16 v[80:83], a[132:135], a[52:55], v[80:83]\n\t"
    "ds_read_b128 a[116:119], v5 offset:4096\n\t"
    "v_add_f32 v42, v42, v88\n\t"
    "v_mfma_f32_16x16x32_bf16 v[80:83], a[136:139], a[56:59], v[80:83]\n\t"
    "buffer_load_dwordx4 v12, s[44:47], s58 offen nt lds\n\t"
    "v_exp_f32 v89, v89\n\t"
    "v_mfma_f32_16x16x32_bf16 v[80:83], a[140:143], a[60:63], v[80:83]\n\t"
    "ds_read_b128 a[120:123], v6 offset:4096\n\t"
    "v_add_f32 v42, v42, v89\n\t"
    "v_cvt_pk_bf16_f32 v76, v88, v89\n\t"
    "v_mfma_f32_16x16x32_bf16 v[84:87], a[144:147], a[48:51], v[60:63]\n\t"
    "buffer_load_dwordx4 v13, s[44:47], s58 offen offset:1024 nt lds\n\t"
    "v_exp_f32 v90, v90\n\t"
    "v_mfma_f32_16x16x32_bf16 v[84:87], a[148:151], a[52:55], v[84:87]\n\t"
    "ds_read_b128 a[124:127], v7 offset:4096\n\t"
    "v_add_f32 v42, v42, v90\n\t"
    "v_mfma_f32_16x16x32_bf16 v[84:87], a[152:155], a[56:59], v[84:87]\n\t"
    "buffer_load_dwordx4 v14, s[44:47], s58 offen offset:2048 nt lds\n\t"
    "v_exp_f32 v91, v91\n\t"
    "v_mfma_f32_16x16x32_bf16 v[84:87], a[156:159], a[60:63], v[84:87]\n\t"
    "v_add_f32 v42, v42, v91\n\t"
    "v_cvt_pk_bf16_f32 v77, v90, v91\n\t"
    "v_mfma_f32_16x16x32_bf16 v[96:99], a[192:195], v[72:75], v[96:99]\n\t"
    "buffer_load_dwordx4 v15, s[44:47], s58 offen offset:3072 nt lds\n\t"
    "v_exp_f32 v92, v92\n\t"
    "v_mfma_f32_16x16x32_bf16 v[100:103], a[196:199], v[72:75], v[100:103]\n\t"
    "ds_read_b64_tr_b16 a[160:161], v8 offset:8192\n\t"
    "v_add_f32 v42, v42, v92\n\t"
    "v_mfma_f32_16x16x32_bf16 v[104:107], a[200:203], v[72:75], v[104:107]\n\t"
    "s_add_u32 m0, s72, 106496\n\t"
    "v_exp_f32 v93, v93\n\t"
    "v_mfma_f32_16x16x32_bf16 v[108:111], a[204:207], v[72:75], v[108:111]\n\t"
    "v_add_f32 v42, v42, v93\n\t"
    "v_cvt_pk_bf16_f32 v78, v92, v93\n\t"
    "v_mfma_f32_16x16x32_bf16 v[112:115], a[208:211], v[72:75], v[112:115]\n\t"
    "s_mov_b32 s59, 114688\n\t"
    "v_exp_f32 v94, v94\n\t"
    "v_mfma_f32_16x16x32_bf16 v[116:119], a[212:215], v[72:75], v[116:119]\n\t"
    "ds_read_b64_tr_b16 a[162:163], v8 offset:12288\n\t"
    "v_add_f32 v42, v42, v94\n\t"
    "v_mfma_f32_16x16x32_bf16 v[120:123], a[216:219], v[72:75], v[120:123]\n\t"
    "v_exp_f32 v95, v95\n\t"
    "v_mfma_f32_16x16x32_bf16 v[124:127], a[220:223], v[72:75], v[124:127]\n\t"
    "v_add_f32 v42, v42, v95\n\t"
    "v_cvt_pk_bf16_f32 v79, v94, v95\n\t"
    "v_mfma_f32_16x16x32_bf16 v[88:91], a[128:131], a[64:67], v[64:67]\n\t"
    "buffer_load_dwordx4 v18, s[48:51], s59 offen nt lds\n\t"
    "v_exp_f32 v80, v80\n\t"
    "v_mfma_f32_16x16x32_bf16 v[88:91], a[132:135], a[68:71], v[88:91]\n\t"
    "ds_read_b64_tr_b16 a[164:165], v9 offset:8192\n\t"
    "v_add_f32 v43, v43, v80\n\t"
    "v_mfma_f32_16x16x32_bf16 v[88:91], a[136:139], a[72:75], v[88:91]\n\t"
    "buffer_load_dwordx4 v18, s[48:51], s59 offen offset:2048 nt lds\n\t"
    "v_exp_f32 v81, v81\n\t"
    "v_mfma_f32_16x16x32_bf16 v[88:91], a[140:143], a[76:79], v[88:91]\n\t"
    "ds_read_b64_tr_b16 a[166:167], v9 offset:12288\n\t"
    "v_add_f32 v43, v43, v81\n\t"
    "v_cvt_pk_bf16_f32 v72, v80, v81\n\t"
    "v_mfma_f32_16x16x32_bf16 v[92:95], a[144:147], a[64:67], v[64:67]\n\t"
    "s_add_u32 m0, s72, 107520\n\t"
    "v_exp_f32 v82, v82\n\t"
    "v_mfma_f32_16x16x32_bf16 v[92:95], a[148:151], a[68:71], v[92:95]\n\t"
    "ds_read_b64_tr_b16 a[168:169], v10 offset:8192\n\t"
    "v_add_f32 v43, v43, v82\n\t"
    "v_mfma_f32_16x16x32_bf16 v[92:95], a[152:155], a[72:75], v[92:95]\n\t"
    "s_mov_b32 s59, 114816\n\t"
    "v_exp_f32 v83, v83\n\t"
    "v_mfma_f32_16x16x32_bf16 v[92:95], a[156:159], a[76:79], v[92:95]\n\t"
    "v_add_f32 v43, v43, v83\n\t"
    "v_cvt_pk_bf16_f32 v73, v82, v83\n\t"
    "v_mfma_f32_16x16x32_bf16 v[128:131], a[192:195], v[76:79], v[128:131]\n\t"
    "buffer_load_dwordx4 v18, s[48:51], s59 offen nt lds\n\t"
    "v_exp_f32 v84, v84\n\t"
    "v_mfma_f32_16x16x32_bf16 v[132:135], a[196:199], v[76:79], v[132:135]\n\t"
    "ds_read_b64_tr_b16 a[170:171], v10 offset:12288\n\t"
    "v_add_f32 v43, v43, v84\n\t"
    "v_mfma_f32_16x16x32_bf16 v[136:139], a[200:203], v[76:79], v[136:139]\n\t"
    "buffer_load_dwordx4 v18, s[48:51], s59 offen offset:2048 nt lds\n\t"
    "v_exp_f32 v85, v85\n\t"
    "v_mfma_f32_16x16x32_bf16 v[140:143], a[204:207], v[76:79], v[140:143]\n\t"
    "v_add_f32 v43, v43, v85\n\t"
    "v_cvt_pk_bf16_f32 v74, v84, v85\n\t"
    "v_mfma_f32_16x16x32_bf16 v[144:147], a[208:211], v[76:79], v[144:147]\n\t"
    "s_add_u32 m0, s72, 110592\n\t"
    "v_exp_f32 v86, v86\n\t"
    "v_mfma_f32_16x16x32_bf16 v[148:151], a[212:215], v[76:79], v[148:151]\n\t"
    "ds_read_b64_tr_b16 a[172:173], v11 offset:8192\n\t"
    "v_add_f32 v43, v43, v86\n\t"
    "v_mfma_f32_16x16x32_bf16 v[152:155], a[216:219], v[76:79], v[152:155]\n\t"
    "v_exp_f32 v87, v87\n\t"
    "v_mfma_f32_16x16x32_bf16 v[156:159], a[220:223], v[76:79], v[156:159]\n\t"
    "s_waitcnt lgkmcnt(7)\n\t"
    "v_add_f32 v43, v43, v87\n\t"
    "v_cvt_pk_bf16_f32 v75, v86, v87\n\t"
    "v_mfma_f32_16x16x32_bf16 v[80:83], a[96:99], a[0:3], v[48:51]\n\t"
    "s_mov_b32 s59, 118784\n\t"
    "v_exp_f32 v88, v88\n\t"
    "v_mfma_f32_16x16x32_bf16 v[80:83], a[100:103], a[4:7], v[80:83]\n\t"
    "ds_read_b64_tr_b16 a[174:175], v11 offset:12288\n\t"
    "v_add_f32 v44, v44, v88\n\t"
    "v_mfma_f32_16x16x32_bf16 v[80:83], a[104:107], a[8:11], v[80:83]\n\t"
    "buffer_load_dwordx4 v18, s[48:51], s59 offen nt lds\n\t"
    "v_exp_f32 v89, v89\n\t"
    "v_mfma_f32_16x16x32_bf16 v[80:83], a[108:111], a[12:15], v[80:83]\n\t"
    "ds_read_b64_tr_b16 a[176:177], v8 offset:9216\n\t"
    "v_add_f32 v44, v44, v89\n\t"
    "v_cvt_pk_bf16_f32 v76, v88, v89\n\t"
    "v_mfma_f32_16x16x32_bf16 v[84:87], a[112:115], a[0:3], v[48:51]\n\t"
    "buffer_load_dwordx4 v18, s[48:51], s59 offen offset:2048 nt lds\n\t"
    "v_exp_f32 v90, v90\n\t"
    "v_mfma_f32_16x16x32_bf16 v[84:87], a[116:119], a[4:7], v[84:87]\n\t"
    "ds_read_b64_tr_b16 a[178:179], v8 offset:13312\n\t"
    "v_add_f32 v44, v44, v90\n\t"
    "v_mfma_f32_16x16x32_bf16 v[84:87], a[120:123], a[8:11], v[84:87]\n\t"
    "s_add_u32 m0, s72, 111616\n\t"
    "v_exp_f32 v91, v91\n\t"
    "v_mfma_f32_16x16x32_bf16 v[84:87], a[124:127], a[12:15], v[84:87]\n\t"
    "v_add_f32 v44, v44, v91\n\t"
    "v_cvt_pk_bf16_f32 v77, v90, v91\n\t"
    "v_mfma_f32_16x16x32_bf16 v[160:163], a[192:195], v[72:75], v[160:163]\n\t"
    "s_mov_b32 s59, 118912\n\t"
    "v_exp_f32 v92, v92\n\t"
    "v_mfma_f32_16x16x32_bf16 v[164:167], a[196:199], v[72:75], v[164:167]\n\t"
    "ds_read_b64_tr_b16 a[180:181], v9 offset:9216\n\t"
    "v_add_f32 v44, v44, v92\n\t"
    "v_mfma_f32_16x16x32_bf16 v[168:171], a[200:203], v[72:75], v[168:171]\n\t"
    "buffer_load_dwordx4 v18, s[48:51], s59 offen nt lds\n\t"
    "v_exp_f32 v93, v93\n\t"
    "v_mfma_f32_16x16x32_bf16 v[172:175], a[204:207], v[72:75], v[172:175]\n\t"
    "v_add_f32 v44, v44, v93\n\t"
    "v_cvt_pk_bf16_f32 v78, v92, v93\n\t"
    "v_mfma_f32_16x16x32_bf16 v[176:179], a[208:211], v[72:75], v[176:179]\n\t"
    "buffer_load_dwordx4 v18, s[48:51], s59 offen offset:2048 nt lds\n\t"
    "v_exp_f32 v94, v94\n\t"
    "v_mfma_f32_16x16x32_bf16 v[180:183], a[212:215], v[72:75], v[180:183]\n\t"
    "ds_read_b64_tr_b16 a[182:183], v9 offset:13312\n\t"
    "v_add_f32 v44, v44, v94\n\t"
    "v_mfma_f32_16x16x32_bf16 v[184:187], a[216:219], v[72:75], v[184:187]\n\t"
    "v_exp_f32 v95, v95\n\t"
    "v_mfma_f32_16x16x32_bf16 v[188:191], a[220:223], v[72:75], v[188:191]\n\t"
    "v_add_f32 v44, v44, v95\n\t"
    "v_cvt_pk_bf16_f32 v79, v94, v95\n\t"
    "v_mfma_f32_16x16x32_bf16 v[88:91], a[96:99], a[16:19], v[52:55]\n\t"
    "v_exp_f32 v80, v80\n\t"
    "v_mfma_f32_16x16x32_bf16 v[88:91], a[100:103], a[20:23], v[88:91]\n\t"
    "ds_read_b64_tr_b16 a[184:185], v10 offset:9216\n\t"
    "v_add_f32 v40, v40, v80\n\t"
    "v_mfma_f32_16x16x32_bf16 v[88:91], a[104:107], a[24:27], v[88:91]\n\t"
    "v_exp_f32 v81, v81\n\t"
    "v_mfma_f32_16x16x32_bf16 v[88:91], a[108:111], a[28:31], v[88:91]\n\t"
    "v_add_f32 v40, v40, v81\n\t"
    "v_cvt_pk_bf16_f32 v72, v80, v81\n\t"
    "v_mfma_f32_16x16x32_bf16 v[92:95], a[112:115], a[16:19], v[52:55]\n\t"
    "v_exp_f32 v82, v82\n\t"
    "v_mfma_f32_16x16x32_bf16 v[92:95], a[116:119], a[20:23], v[92:95]\n\t"
    "ds_read_b64_tr_b16 a[186:187], v10 offset:13312\n\t"
    "v_add_f32 v40, v40, v82\n\t"
    "v_mfma_f32_16x16x32_bf16 v[92:95], a[120:123], a[24:27], v[92:95]\n\t"
    "v_exp_f32 v83, v83\n\t"
    "v_mfma_f32_16x16x32_bf16 v[92:95], a[124:127], a[28:31], v[92:95]\n\t"
    "v_add_f32 v40, v40, v83\n\t"
    "v_cvt_pk_bf16_f32 v73, v82, v83\n\t"
    "v_mfma_f32_16x16x32_bf16 v[192:195], a[192:195], v[76:79], v[192:195]\n\t"
    "v_exp_f32 v84, v84\n\t"
    "v_mfma_f32_16x16x32_bf16 v[196:199], a[196:199], v[76:79], v[196:199]\n\t"
    "ds_read_b64_tr_b16 a[188:189], v11 offset:9216\n\t"
    "v_add_f32 v40, v40, v84\n\t"
    "v_mfma_f32_16x16x32_bf16 v[200:203], a[200:203], v[76:79], v[200:203]\n\t"
    "v_exp_f32 v85, v85\n\t"
    "v_mfma_f32_16x16x32_bf16 v[204:207], a[204:207], v[76:79], v[204:207]\n\t"
    "v_add_f32 v40, v40, v85\n\t"
    "v_cvt_pk_bf16_f32 v74, v84, v85\n\t"
    "v_mfma_f32_16x16x32_bf16 v[208:211], a[208:211], v[76:79], v[208:211]\n\t"
    "v_exp_f32 v86, v86\n\t"
    "v_mfma_f32_16x16x32_bf16 v[212:215], a[212:215], v[76:79], v[212:215]\n\t"
    "ds_read_b64_tr_b16 a[190:191], v11 offset:13312\n\t"
    "v_add_f32 v40, v40, v86\n\t"
    "v_mfma_f32_16x16x32_bf16 v[216:219], a[216:219], v[76:79], v[216:219]\n\t"
    "v_exp_f32 v87, v87\n\t"
    "v_mfma_f32_16x16x32_bf16 v[220:223], a[220:223], v[76:79], v[220:223]\n\t"
    "s_waitcnt vmcnt(16)\n\t"
    "s_barrier\n\t"
    "v_add_f32 v40, v40, v87\n\t"
    "v_cvt_pk_bf16_f32 v75, v86, v87\n\t"
    "v_mfma_f32_16x16x32_bf16 v[80:83], a[96:99], a[32:35], v[56:59]\n\t"
    "s_add_u32 m0, s72, 114688\n\t"
    "v_exp_f32 v88, v88\n\t"
    "v_mfma_f32_16x16x32_bf16 v[80:83], a[100:103], a[36:39], v[80:83]\n\t"
    "ds_read_b128 a[128:131], v4 offset:16384\n\t"
    "v_add_f32 v41, v41, v88\n\t"
    "v_mfma_f32_16x16x32_bf16 v[80:83], a[104:107], a[40:43], v[80:83]\n\t"
    "s_mov_b32 s58, 122880\n\t"
    "v_exp_f32 v89, v89\n\t"
    "v_mfma_f32_16x16x32_bf16 v[80:83], a[108:111], a[44:47], v[80:83]\n\t"
    "ds_read_b128 a[132:135], v5 offset:16384\n\t"
    "v_add_f32 v41, v41, v89\n\t"
    "v_cvt_pk_bf16_f32 v76, v88, v89\n\t"
    "v_mfma_f32_16x16x32_bf16 v[84:87], a[112:115], a[32:35], v[56:59]\n\t"
    "buffer_load_dwordx4 v12, s[44:47], s58 offen nt lds\n\t"
    "v_exp_f32 v90, v90\n\t"
    "v_mfma_f32_16x16x32_bf16 v[84:87], a[116:119], a[36:39], v[84:87]\n\t"
    "ds_read_b128 a[136:139], v6 offset:16384\n\t"
    "v_add_f32 v41, v41, v90\n\t"
    "v_mfma_f32_16x16x32_bf16 v[84:87], a[120:123], a[40:43], v[84:87]\n\t"
    "buffer_load_dwordx4 v13, s[44:47], s58 offen offset:1024 nt lds\n\t"
    "v_exp_f32 v91, v91\n\t"
    "v_mfma_f32_16x16x32_bf16 v[84:87], a[124:127], a[44:47], v[84:87]\n\t"
    "s_waitcnt lgkmcnt(3)\n\t"
    "v_add_f32 v41, v41, v91\n\t"
    "v_cvt_pk_bf16_f32 v77, v90, v91\n\t"
    "v_mfma_f32_16x16x32_bf16 a[224:227], a[160:163], v[72:75], a[224:227]\n\t"
    "buffer_load_dwordx4 v14, s[44:47], s58 offen offset:2048 nt lds\n\t"
    "v_exp_f32 v92, v92\n\t"
    "v_mfma_f32_16x16x32_bf16 a[228:231], a[164:167], v[72:75], a[228:231]\n\t"
    "ds_read_b128 a[140:143], v7 offset:16384\n\t"
    "v_add_f32 v41, v41, v92\n\t"
    "v_mfma_f32_16x16x32_bf16 a[232:235], a[168:171], v[72:75], a[232:235]\n\t"
    "buffer_load_dwordx4 v15, s[44:47], s58 offen offset:3072 nt lds\n\t"
    "v_exp_f32 v93, v93\n\t"
    "v_mfma_f32_16x16x32_bf16 a[236:239], a[172:175], v[72:75], a[236:239]\n\t"
    "v_add_f32 v41, v41, v93\n\t"
    "v_cvt_pk_bf16_f32 v78, v92, v93\n\t"
    "v_mfma_f32_16x16x32_bf16 a[240:243], a[176:179], v[72:75], a[240:243]\n\t"
    "s_add_u32 m0, s72, 118784\n\t"
    "v_exp_f32 v94, v94\n\t"
    "v_mfma_f32_16x16x32_bf16 a[244:247], a[180:183], v[72:75], a[244:247]\n\t"
    "ds_read_b128 a[144:147], v4 offset:20480\n\t"
    "v_add_f32 v41, v41, v94\n\t"
    "v_mfma_f32_16x16x32_bf16 a[248:251], a[184:187], v[72:75], a[248:251]\n\t"
    "v_exp_f32 v95, v95\n\t"
    "v_mfma_f32_16x16x32_bf16 a[252:255], a[188:191], v[72:75], a[252:255]\n\t"
    "v_add_f32 v41, v41, v95\n\t"
    "v_cvt_pk_bf16_f32 v79, v94, v95\n\t"
    "v_mfma_f32_16x16x32_bf16 v[88:91], a[96:99], a[48:51], v[60:63]\n\t"
    "s_mov_b32 s58, 126976\n\t"
    "v_exp_f32 v80, v80\n\t"
    "v_mfma_f32_16x16x32_bf16 v[88:91], a[100:103], a[52:55], v[88:91]\n\t"
    "ds_read_b128 a[148:151], v5 offset:20480\n\t"
    "v_add_f32 v42, v42, v80\n\t"
    "v_mfma_f32_16x16x32_bf16 v[88:91], a[104:107], a[56:59], v[88:91]\n\t"
    "buffer_load_dwordx4 v12, s[44:47], s58 offen nt lds\n\t"
    "v_exp_f32 v81, v81\n\t"
    "v_mfma_f32_16x16x32_bf16 v[88:91], a[108:111], a[60:63], v[88:91]\n\t"
    "ds_read_b128 a[152:155], v6 offset:20480\n\t"
    "v_add_f32 v42, v42, v81\n\t"
    "v_cvt_pk_bf16_f32 v72, v80, v81\n\t"
    "v_mfma_f32_16x16x32_bf16 v[92:95], a[112:115], a[48:51], v[60:63]\n\t"
    "buffer_load_dwordx4 v13, s[44:47], s58 offen offset:1024 nt lds\n\t"
    "v_exp_f32 v82, v82\n\t"
    "v_mfma_f32_16x16x32_bf16 v[92:95], a[116:119], a[52:55], v[92:95]\n\t"
    "ds_read_b128 a[156:159], v7 offset:20480\n\t"
    "v_add_f32 v42, v42, v82\n\t"
    "v_mfma_f32_16x16x32_bf16 v[92:95], a[120:123], a[56:59], v[92:95]\n\t"
    "buffer_load_dwordx4 v14, s[44:47], s58 offen offset:2048 nt lds\n\t"
    "v_exp_f32 v83, v83\n\t"
    "v_mfma_f32_16x16x32_bf16 v[92:95], a[124:127], a[60:63], v[92:95]\n\t"
    "v_add_f32 v42, v42, v83\n\t"
    "v_cvt_pk_bf16_f32 v73, v82, v83\n\t"
    "v_mfma_f32_16x16x32_bf16 v[96:99], a[160:163], v[76:79], v[96:99]\n\t"
    "buffer_load_dwordx4 v15, s[44:47], s58 offen offset:3072 nt lds\n\t"
    "v_exp_f32 v84, v84\n\t"
    "v_mfma_f32_16x16x32_bf16 v[100:103], a[164:167], v[76:79], v[100:103]\n\t"
    "ds_read_b64_tr_b16 a[192:193], v8 offset:24576\n\t"
    "v_add_f32 v42, v42, v84\n\t"
    "v_mfma_f32_16x16x32_bf16 v[104:107], a[168:171], v[76:79], v[104:107]\n\t"
    "s_add_u32 m0, s72, 122880\n\t"
    "v_exp_f32 v85, v85\n\t"
    "v_mfma_f32_16x16x32_bf16 v[108:111], a[172:175], v[76:79], v[108:111]\n\t"
    "v_add_f32 v42, v42, v85\n\t"
    "v_cvt_pk_bf16_f32 v74, v84, v85\n\t"
    "v_mfma_f32_16x16x32_bf16 v[112:115], a[176:179], v[76:79], v[112:115]\n\t"
    "s_mov_b32 s59, 122880\n\t"
    "v_exp_f32 v86, v86\n\t"
    "v_mfma_f32_16x16x32_bf16 v[116:119], a[180:183], v[76:79], v[116:119]\n\t"
    "ds_read_b64_tr_b16 a[194:195], v8 offset:28672\n\t"
    "v_add_f32 v42, v42, v86\n\t"
    "v_mfma_f32_16x16x32_bf16 v[120:123], a[184:187], v[76:79], v[120:123]\n\t"
    "v_exp_f32 v87, v87\n\t"
    "v_mfma_f32_16x16x32_bf16 v[124:127], a[188:191], v[76:79], v[124:127]\n\t"
    "v_add_f32 v42, v42, v87\n\t"
    "v_cvt_pk_bf16_f32 v75, v86, v87\n\t"
    "v_mfma_f32_16x16x32_bf16 v[80:83], a[96:99], a[64:67], v[64:67]\n\t"
    "buffer_load_dwordx4 v18, s[48:51], s59 offen nt lds\n\t"
    "v_exp_f32 v88, v88\n\t"
    "v_mfma_f32_16x16x32_bf16 v[80:83], a[100:103], a[68:71], v[80:83]\n\t"
    "ds_read_b64_tr_b16 a[196:197], v9 offset:24576\n\t"
    "v_add_f32 v43, v43, v88\n\t"
    "v_mfma_f32_16x16x32_bf16 v[80:83], a[104:107], a[72:75], v[80:83]\n\t"
    "buffer_load_dwordx4 v18, s[48:51], s59 offen offset:2048 nt lds\n\t"
    "v_exp_f32 v89, v89\n\t"
    "v_mfma_f32_16x16x32_bf16 v[80:83], a[108:111], a[76:79], v[80:83]\n\t"
    "ds_read_b64_tr_b16 a[198:199], v9 offset:28672\n\t"
    "v_add_f32 v43, v43, v89\n\t"
    "v_cvt_pk_bf16_f32 v76, v88, v89\n\t"
    "v_mfma_f32_16x16x32_bf16 v[84:87], a[112:115], a[64:67], v[64:67]\n\t"
    "s_add_u32 m0, s72, 123904\n\t"
    "v_exp_f32 v90, v90\n\t"
    "v_mfma_f32_16x16x32_bf16 v[84:87], a[116:119], a[68:71], v[84:87]\n\t"
    "ds_read_b64_tr_b16 a[200:201], v10 offset:24576\n\t"
    "v_add_f32 v43, v43, v90\n\t"
    "v_mfma_f32_16x16x32_bf16 v[84:87], a[120:123], a[72:75], v[84:87]\n\t"
    "s_mov_b32 s59, 123008\n\t"
    "v_exp_f32 v91, v91\n\t"
    "v_mfma_f32_16x16x32_bf16 v[84:87], a[124:127], a[76:79], v[84:87]\n\t"
    "v_add_f32 v43, v43, v91\n\t"
    "v_cvt_pk_bf16_f32 v77, v90, v91\n\t"
    "v_mfma_f32_16x16x32_bf16 v[128:131], a[160:163], v[72:75], v[128:131]\n\t"
    "buffer_load_dwordx4 v18, s[48:51], s59 offen nt lds\n\t"
    "v_exp_f32 v92, v92\n\t"
    "v_mfma_f32_16x16x32_bf16 v[132:135], a[164:167], v[72:75], v[132:135]\n\t"
    "ds_read_b64_tr_b16 a[202:203], v10 offset:28672\n\t"
    "v_add_f32 v43, v43, v92\n\t"
    "v_mfma_f32_16x16x32_bf16 v[136:139], a[168:171], v[72:75], v[136:139]\n\t"
    "buffer_load_dwordx4 v18, s[48:51], s59 offen offset:2048 nt lds\n\t"
    "v_exp_f32 v93, v93\n\t"
    "v_mfma_f32_16x16x32_bf16 v[140:143], a[172:175], v[72:75], v[140:143]\n\t"
    "v_add_f32 v43, v43, v93\n\t"
    "v_cvt_pk_bf16_f32 v78, v92, v93\n\t"
    "v_mfma_f32_16x16x32_bf16 v[144:147], a[176:179], v[72:75], v[144:147]\n\t"
    "s_add_u32 m0, s72, 126976\n\t"
    "v_exp_f32 v94, v94\n\t"
    "v_mfma_f32_16x16x32_bf16 v[148:151], a[180:183], v[72:75], v[148:151]\n\t"
    "ds_read_b64_tr_b16 a[204:205], v11 offset:24576\n\t"
    "v_add_f32 v43, v43, v94\n\t"
    "v_mfma_f32_16x16x32_bf16 v[152:155], a[184:187], v[72:75], v[152:155]\n\t"
    "v_exp_f32 v95, v95\n\t"
    "v_mfma_f32_16x16x32_bf16 v[156:159], a[188:191], v[72:75], v[156:159]\n\t"
    "s_waitcnt lgkmcnt(7)\n\t"
    "v_add_f32 v43, v43, v95\n\t"
    "v_cvt_pk_bf16_f32 v79, v94, v95\n\t"
    "v_mfma_f32_16x16x32_bf16 v[88:91], a[128:131], a[0:3], v[48:51]\n\t"
    "s_mov_b32 s59, 126976\n\t"
    "v_exp_f32 v80, v80\n\t"
    "v_mfma_f32_16x16x32_bf16 v[88:91], a[132:135], a[4:7], v[88:91]\n\t"
    "ds_read_b64_tr_b16 a[206:207], v11 offset:28672\n\t"
    "v_add_f32 v44, v44, v80\n\t"
    "v_mfma_f32_16x16x32_bf16 v[88:91], a[136:139], a[8:11], v[88:91]\n\t"
    "buffer_load_dwordx4 v18, s[48:51], s59 offen nt lds\n\t"
    "v_exp_f32 v81, v81\n\t"
    "v_mfma_f32_16x16x32_bf16 v[88:91], a[140:143], a[12:15], v[88:91]\n\t"
    "ds_read_b64_tr_b16 a[208:209], v8 offset:25600\n\t"
    "v_add_f32 v44, v44, v81\n\t"
    "v_cvt_pk_bf16_f32 v72, v80, v81\n\t"
    "v_mfma_f32_16x16x32_bf16 v[92:95], a[144:147], a[0:3], v[48:51]\n\t"
    "buffer_load_dwordx4 v18, s[48:51], s59 offen offset:2048 nt lds\n\t"
    "v_exp_f32 v82, v82\n\t"
    "v_mfma_f32_16x16x32_bf16 v[92:95], a[148:151], a[4:7], v[92:95]\n\t"
    "ds_read_b64_tr_b16 a[210:211], v8 offset:29696\n\t"
    "v_add_f32 v44, v44, v82\n\t"
    "v_mfma_f32_16x16x32_bf16 v[92:95], a[152:155], a[8:11], v[92:95]\n\t"
    "s_add_u32 m0, s72, 128000\n\t"
    "v_exp_f32 v83, v83\n\t"
    "v_mfma_f32_16x16x32_bf16 v[92:95], a[156:159], a[12:15], v[92:95]\n\t"
    "v_add_f32 v44, v44, v83\n\t"
    "v_cvt_pk_bf16_f32 v73, v82, v83\n\t"
    "v_mfma_f32_16x16x32_bf16 v[160:163], a[160:163], v[76:79], v[160:163]\n\t"
    "s_mov_b32 s59, 127104\n\t"
    "v_exp_f32 v84, v84\n\t"
    "v_mfma_f32_16x16x32_bf16 v[164:167], a[164:167], v[76:79], v[164:167]\n\t"
    "ds_read_b64_tr_b16 a[212:213], v9 offset:25600\n\t"
    "v_add_f32 v44, v44, v84\n\t"
    "v_mfma_f32_16x16x32_bf16 v[168:171], a[168:171], v[76:79], v[168:171]\n\t"
    "buffer_load_dwordx4 v18, s[48:51], s59 offen nt lds\n\t"
    "v_exp_f32 v85, v85\n\t"
    "v_mfma_f32_16x16x32_bf16 v[172:175], a[172:175], v[76:79], v[172:175]\n\t"
    "v_add_f32 v44, v44, v85\n\t"
    "v_cvt_pk_bf16_f32 v74, v84, v85\n\t"
    "v_mfma_f32_16x16x32_bf16 v[176:179], a[176:179], v[76:79], v[176:179]\n\t"
    "buffer_load_dwordx4 v18, s[48:51], s59 offen offset:2048 nt lds\n\t"
    "v_exp_f32 v86, v86\n\t"
    "v_mfma_f32_16x16x32_bf16 v[180:183], a[180:183], v[76:79], v[180:183]\n\t"
    "ds_read_b64_tr_b16 a[214:215], v9 offset:29696\n\t"
    "v_add_f32 v44, v44, v86\n\t"
    "v_mfma_f32_16x16x32_bf16 v[184:187], a[184:187], v[76:79], v[184:187]\n\t"
    "v_exp_f32 v87, v87\n\t"
    "v_mfma_f32_16x16x32_bf16 v[188:191], a[188:191], v[76:79], v[188:191]\n\t"
    "v_add_f32 v44, v44, v87\n\t"
    "v_cvt_pk_bf16_f32 v75, v86, v87\n\t"
    "v_mfma_f32_16x16x32_bf16 v[80:83], a[128:131], a[16:19], v[52:55]\n\t"
    "v_exp_f32 v88, v88\n\t"
    "v_mfma_f32_16x16x32_bf16 v[80:83], a[132:135], a[20:23], v[80:83]\n\t"
    "ds_read_b64_tr_b16 a[216:217], v10 offset:25600\n\t"
    "v_add_f32 v40, v40, v88\n\t"
    "v_mfma_f32_16x16x32_bf16 v[80:83], a[136:139], a[24:27], v[80:83]\n\t"
    "v_exp_f32 v89, v89\n\t"
    "v_mfma_f32_16x16x32_bf16 v[80:83], a[140:143], a[28:31], v[80:83]\n\t"
    "v_add_f32 v40, v40, v89\n\t"
    "v_cvt_pk_bf16_f32 v76, v88, v89\n\t"
    "v_mfma_f32_16x16x32_bf16 v[84:87], a[144:147], a[16:19], v[52:55]\n\t"
    "v_exp_f32 v90, v90\n\t"
    "v_mfma_f32_16x16x32_bf16 v[84:87], a[148:151], a[20:23], v[84:87]\n\t"
    "ds_read_b64_tr_b16 a[218:219], v10 offset:29696\n\t"
    "v_add_f32 v40, v40, v90\n\t"
    "v_mfma_f32_16x16x32_bf16 v[84:87], a[152:155], a[24:27], v[84:87]\n\t"
    "v_exp_f32 v91, v91\n\t"
    "v_mfma_f32_16x16x32_bf16 v[84:87], a[156:159], a[28:31], v[84:87]\n\t"
    "v_add_f32 v40, v40, v91\n\t"
    "v_cvt_pk_bf16_f32 v77, v90, v91\n\t"
    "v_mfma_f32_16x16x32_bf16 v[192:195], a[160:163], v[72:75], v[192:195]\n\t"
    "v_exp_f32 v92, v92\n\t"
    "v_mfma_f32_16x16x32_bf16 v[196:199], a[164:167], v[72:75], v[196:199]\n\t"
    "ds_read_b64_tr_b16 a[220:221], v11 offset:25600\n\t"
    "v_add_f32 v40, v40, v92\n\t"
    "v_mfma_f32_16x16x32_bf16 v[200:203], a[168:171], v[72:75], v[200:203]\n\t"
    "v_exp_f32 v93, v93\n\t"
    "v_mfma_f32_16x16x32_bf16 v[204:207], a[172:175], v[72:75], v[204:207]\n\t"
    "v_add_f32 v40, v40, v93\n\t"
    "v_cvt_pk_bf16_f32 v78, v92, v93\n\t"
    "v_mfma_f32_16x16x32_bf16 v[208:211], a[176:179], v[72:75], v[208:211]\n\t"
    "v_exp_f32 v94, v94\n\t"
    "v_mfma_f32_16x16x32_bf16 v[212:215], a[180:183], v[72:75], v[212:215]\n\t"
    "ds_read_b64_tr_b16 a[222:223], v11 offset:29696\n\t"
    "v_add_f32 v40, v40, v94\n\t"
    "v_mfma_f32_16x16x32_bf16 v[216:219], a[184:187], v[72:75], v[216:219]\n\t"
    "v_exp_f32 v95, v95\n\t"
    "v_mfma_f32_16x16x32_bf16 v[220:223], a[188:191], v[72:75], v[220:223]\n\t"
    "s_waitcnt vmcnt(8)\n\t"
    "s_barrier\n\t"
    "v_add_f32 v40, v40, v95\n\t"
    "v_cvt_pk_bf16_f32 v79, v94, v95\n\t"
    "v_mfma_f32_16x16x32_bf16 v[88:91], a[128:131], a[32:35], v[56:59]\n\t"
    "v_exp_f32 v80, v80\n\t"
    "v_mfma_f32_16x16x32_bf16 v[88:91], a[132:135], a[36:39], v[88:91]\n\t"
    "ds_read_b128 a[96:99], v4 offset:32768\n\t"
    "v_add_f32 v41, v41, v80\n\t"
    "v_mfma_f32_16x16x32_bf16 v[88:91], a[136:139], a[40:43], v[88:91]\n\t"
    "v_exp_f32 v81, v81\n\t"
    "v_mfma_f32_16x16x32_bf16 v[88:91], a[140:143], a[44:47], v[88:91]\n\t"
    "ds_read_b128 a[100:103], v5 offset:32768\n\t"
    "v_add_f32 v41, v41, v81\n\t"
    "v_cvt_pk_bf16_f32 v72, v80, v81\n\t"
    "v_mfma_f32_16x16x32_bf16 v[92:95], a[144:147], a[32:35], v[56:59]\n\t"
    "v_exp_f32 v82, v82\n\t"
    "v_mfma_f32_16x16x32_bf16 v[92:95], a[148:151], a[36:39], v[92:95]\n\t"
    "ds_read_b128 a[104:107], v6 offset:32768\n\t"
    "v_add_f32 v41, v41, v82\n\t"
    "v_mfma_f32_16x16x32_bf16 v[92:95], a[152:155], a[40:43], v[92:95]\n\t"
    "v_exp_f32 v83, v83\n\t"
    "v_mfma_f32_16x16x32_bf16 v[92:95], a[156:159], a[44:47], v[92:95]\n\t"
    "s_waitcnt lgkmcnt(3)\n\t"
    "v_add_f32 v41, v41, v83\n\t"
    "v_cvt_pk_bf16_f32 v73, v82, v83\n\t"
    "v_mfma_f32_16x16x32_bf16 a[224:227], a[192:195], v[76:79], a[224:227]\n\t"
    "v_exp_f32 v84, v84\n\t"
    "v_mfma_f32_16x16x32_bf16 a[228:231], a[196:199], v[76:79], a[228:231]\n\t"
    "ds_read_b128 a[108:111], v7 offset:32768\n\t"
    "v_add_f32 v41, v41, v84\n\t"
    "v_mfma_f32_16x16x32_bf16 a[232:235], a[200:203], v[76:79], a[232:235]\n\t"
    "v_exp_f32 v85, v85\n\t"
    "v_mfma_f32_16x16x32_bf16 a[236:239], a[204:207], v[76:79], a[236:239]\n\t"
    "v_add_f32 v41, v41, v85\n\t"
    "v_cvt_pk_bf16_f32 v74, v84, v85\n\t"
    "v_mfma_f32_16x16x32_bf16 a[240:243], a[208:211], v[76:79], a[240:243]\n\t"
    "v_exp_f32 v86, v86\n\t"
    "v_mfma_f32_16x16x32_bf16 a[244:247], a[212:215], v[76:79], a[244:247]\n\t"
    "ds_read_b128 a[112:115], v4 offset:36864\n\t"
    "v_add_f32 v41, v41, v86\n\t"
    "v_mfma_f32_16x16x32_bf16 a[248:251], a[216:219], v[76:79], a[248:251]\n\t"
    "v_exp_f32 v87, v87\n\t"
    "v_mfma_f32_16x16x32_bf16 a[252:255], a[220:223], v[76:79], a[252:255]\n\t"
    "v_add_f32 v41, v41, v87\n\t"
    "v_cvt_pk_bf16_f32 v75, v86, v87\n\t"
    "v_mfma_f32_16x16x32_bf16 v[80:83], a[128:131], a[48:51], v[60:63]\n\t"
    "v_exp_f32 v88, v88\n\t"
    "v_mfma_f32_16x16x32_bf16 v[80:83], a[132:135], a[52:55], v[80:83]\n\t"
    "ds_read_b128 a[116:119], v5 offset:36864\n\t"
    "v_add_f32 v42, v42, v88\n\t"
    "v_mfma_f32_16x16x32_bf16 v[80:83], a[136:139], a[56:59], v[80:83]\n\t"
    "v_exp_f32 v89, v89\n\t"
    "v_mfma_f32_16x16x32_bf16 v[80:83], a[140:143], a[60:63], v[80:83]\n\t"
    "ds_read_b128 a[120:123], v6 offset:36864\n\t"
    "v_add_f32 v42, v42, v89\n\t"
    "v_cvt_pk_bf16_f32 v76, v88, v89\n\t"
    "v_mfma_f32_16x16x32_bf16 v[84:87], a[144:147], a[48:51], v[60:63]\n\t"
    "v_exp_f32 v90, v90\n\t"
    "v_mfma_f32_16x16x32_bf16 v[84:87], a[148:151], a[52:55], v[84:87]\n\t"
    "ds_read_b128 a[124:127], v7 offset:36864\n\t"
    "v_add_f32 v42, v42, v90\n\t"
    "v_mfma_f32_16x16x32_bf16 v[84:87], a[152:155], a[56:59], v[84:87]\n\t"
    "v_exp_f32 v91, v91\n\t"
    "v_mfma_f32_16x16x32_bf16 v[84:87], a[156:159], a[60:63], v[84:87]\n\t"
    "v_add_f32 v42, v42, v91\n\t"
    "v_cvt_pk_bf16_f32 v77, v90, v91\n\t"
    "v_mfma_f32_16x16x32_bf16 v[96:99], a[192:195], v[72:75], v[96:99]\n\t"
    "v_exp_f32 v92, v92\n\t"
    "v_mfma_f32_16x16x32_bf16 v[100:103], a[196:199], v[72:75], v[100:103]\n\t"
    "ds_read_b64_tr_b16 a[160:161], v8 offset:40960\n\t"
    "v_add_f32 v42, v42, v92\n\t"
    "v_mfma_f32_16x16x32_bf16 v[104:107], a[200:203], v[72:75], v[104:107]\n\t"
    "v_exp_f32 v93, v93\n\t"
    "v_mfma_f32_16x16x32_bf16 v[108:111], a[204:207], v[72:75], v[108:111]\n\t"
    "v_add_f32 v42, v42, v93\n\t"
    "v_cvt_pk_bf16_f32 v78, v92, v93\n\t"
    "v_mfma_f32_16x16x32_bf16 v[112:115], a[208:211], v[72:75], v[112:115]\n\t"
    "v_exp_f32 v94, v94\n\t"
    "v_mfma_f32_16x16x32_bf16 v[116:119], a[212:215], v[72:75], v[116:119]\n\t"
    "ds_read_b64_tr_b16 a[162:163], v8 offset:45056\n\t"
    "v_add_f32 v42, v42, v94\n\t"
    "v_mfma_f32_16x16x32_bf16 v[120:123], a[216:219], v[72:75], v[120:123]\n\t"
    "v_exp_f32 v95, v95\n\t"
    "v_mfma_f32_16x16x32_bf16 v[124:127], a[220:223], v[72:75], v[124:127]\n\t"
    "v_add_f32 v42, v42, v95\n\t"
    "v_cvt_pk_bf16_f32 v79, v94, v95\n\t"
    "v_mfma_f32_16x16x32_bf16 v[88:91], a[128:131], a[64:67], v[64:67]\n\t"
    "v_exp_f32 v80, v80\n\t"
    "v_mfma_f32_16x16x32_bf16 v[88:91], a[132:135], a[68:71], v[88:91]\n\t"
    "ds_read_b64_tr_b16 a[164:165], v9 offset:40960\n\t"
    "v_add_f32 v43, v43, v80\n\t"
    "v_mfma_f32_16x16x32_bf16 v[88:91], a[136:139], a[72:75], v[88:91]\n\t"
    "v_exp_f32 v81, v81\n\t"
    "v_mfma_f32_16x16x32_bf16 v[88:91], a[140:143], a[76:79], v[88:91]\n\t"
    "ds_read_b64_tr_b16 a[166:167], v9 offset:45056\n\t"
    "v_add_f32 v43, v43, v81\n\t"
    "v_cvt_pk_bf16_f32 v72, v80, v81\n\t"
    "v_mfma_f32_16x16x32_bf16 v[92:95], a[144:147], a[64:67], v[64:67]\n\t"
    "v_exp_f32 v82, v82\n\t"
    "v_mfma_f32_16x16x32_bf16 v[92:95], a[148:151], a[68:71], v[92:95]\n\t"
    "ds_read_b64_tr_b16 a[168:169], v10 offset:40960\n\t"
    "v_add_f32 v43, v43, v82\n\t"
    "v_mfma_f32_16x16x32_bf16 v[92:95], a[152:155], a[72:75], v[92:95]\n\t"
    "v_exp_f32 v83, v83\n\t"
    "v_mfma_f32_16x16x32_bf16 v[92:95], a[156:159], a[76:79], v[92:95]\n\t"
    "v_add_f32 v43, v43, v83\n\t"
    "v_cvt_pk_bf16_f32 v73, v82, v83\n\t"
    "v_mfma_f32_16x16x32_bf16 v[128:131], a[192:195], v[76:79], v[128:131]\n\t"
    "ds_read_b128 a[128:131], v4 offset:49152\n\t"
    "v_exp_f32 v84, v84\n\t"
    "v_mfma_f32_16x16x32_bf16 v[132:135], a[196:199], v[76:79], v[132:135]\n\t"
    "ds_read_b64_tr_b16 a[170:171], v10 offset:45056\n\t"
    "v_add_f32 v43, v43, v84\n\t"
    "v_mfma_f32_16x16x32_bf16 v[136:139], a[200:203], v[76:79], v[136:139]\n\t"
    "ds_read_b128 a[132:135], v5 offset:49152\n\t"
    "v_exp_f32 v85, v85\n\t"
    "v_mfma_f32_16x16x32_bf16 v[140:143], a[204:207], v[76:79], v[140:143]\n\t"
    "v_add_f32 v43, v43, v85\n\t"
    "v_cvt_pk_bf16_f32 v74, v84, v85\n\t"
    "v_mfma_f32_16x16x32_bf16 v[144:147], a[208:211], v[76:79], v[144:147]\n\t"
    "ds_read_b128 a[136:139], v6 offset:49152\n\t"
    "v_exp_f32 v86, v86\n\t"
    "v_mfma_f32_16x16x32_bf16 v[148:151], a[212:215], v[76:79], v[148:151]\n\t"
    "ds_read_b64_tr_b16 a[172:173], v11 offset:40960\n\t"
    "v_add_f32 v43, v43, v86\n\t"
    "v_mfma_f32_16x16x32_bf16 v[152:155], a[216:219], v[76:79], v[152:155]\n\t"
    "ds_read_b128 a[140:143], v7 offset:49152\n\t"
    "v_exp_f32 v87, v87\n\t"
    "v_mfma_f32_16x16x32_bf16 v[156:159], a[220:223], v[76:79], v[156:159]\n\t"
    "s_waitcnt lgkmcnt(11)\n\t"
    "v_add_f32 v43, v43, v87\n\t"
    "v_cvt_pk_bf16_f32 v75, v86, v87\n\t"
    "v_mfma_f32_16x16x32_bf16 v[80:83], a[96:99], a[0:3], v[48:51]\n\t"
    "ds_read_b128 a[144:147], v4 offset:53248\n\t"
    "v_exp_f32 v88, v88\n\t"
    "v_mfma_f32_16x16x32_bf16 v[80:83], a[100:103], a[4:7], v[80:83]\n\t"
    "ds_read_b64_tr_b16 a[174:175], v11 offset:45056\n\t"
    "v_add_f32 v44, v44, v88\n\t"
    "v_mfma_f32_16x16x32_bf16 v[80:83], a[104:107], a[8:11], v[80:83]\n\t"
    "ds_read_b128 a[148:151], v5 offset:53248\n\t"
    "v_exp_f32 v89, v89\n\t"
    "v_mfma_f32_16x16x32_bf16 v[80:83], a[108:111], a[12:15], v[80:83]\n\t"
    "ds_read_b64_tr_b16 a[176:177], v8 offset:41984\n\t"
    "v_add_f32 v44, v44, v89\n\t"
    "v_cvt_pk_bf16_f32 v76, v88, v89\n\t"
    "v_mfma_f32_16x16x32_bf16 v[84:87], a[112:115], a[0:3], v[48:51]\n\t"
    "ds_read_b128 a[152:155], v6 offset:53248\n\t"
    "v_exp_f32 v90, v90\n\t"
    "v_mfma_f32_16x16x32_bf16 v[84:87], a[116:119], a[4:7], v[84:87]\n\t"
    "ds_read_b64_tr_b16 a[178:179], v8 offset:46080\n\t"
    "v_add_f32 v44, v44, v90\n\t"
    "v_mfma_f32_16x16x32_bf16 v[84:87], a[120:123], a[8:11], v[84:87]\n\t"
    "ds_read_b128 a[156:159], v7 offset:53248\n\t"
    "v_exp_f32 v91, v91\n\t"
    "v_mfma_f32_16x16x32_bf16 v[84:87], a[124:127], a[12:15], v[84:87]\n\t"
    "v_add_f32 v44, v44, v91\n\t"
    "v_cvt_pk_bf16_f32 v77, v90, v91\n\t"
    "v_mfma_f32_16x16x32_bf16 v[160:163], a[192:195], v[72:75], v[160:163]\n\t"
    "v_exp_f32 v92, v92\n\t"
    "v_mfma_f32_16x16x32_bf16 v[164:167], a[196:199], v[72:75], v[164:167]\n\t"
    "ds_read_b64_tr_b16 a[180:181], v9 offset:41984\n\t"
    "v_add_f32 v44, v44, v92\n\t"
    "v_mfma_f32_16x16x32_bf16 v[168:171], a[200:203], v[72:75], v[168:171]\n\t"
    "v_exp_f32 v93, v93\n\t"
    "v_mfma_f32_16x16x32_bf16 v[172:175], a[204:207], v[72:75], v[172:175]\n\t"
    "v_add_f32 v44, v44, v93\n\t"
    "v_cvt_pk_bf16_f32 v78, v92, v93\n\t"
    "v_mfma_f32_16x16x32_bf16 v[176:179], a[208:211], v[72:75], v[176:179]\n\t"
    "v_exp_f32 v94, v94\n\t"
    "v_mfma_f32_16x16x32_bf16 v[180:183], a[212:215], v[72:75], v[180:183]\n\t"
    "ds_read_b64_tr_b16 a[182:183], v9 offset:46080\n\t"
    "v_add_f32 v44, v44, v94\n\t"
    "v_mfma_f32_16x16x32_bf16 v[184:187], a[216:219], v[72:75], v[184:187]\n\t"
    "v_exp_f32 v95, v95\n\t"
    "v_mfma_f32_16x16x32_bf16 v[188:191], a[220:223], v[72:75], v[188:191]\n\t"
    "s_waitcnt lgkmcnt(2)\n\t"
    "v_add_f32 v44, v44, v95\n\t"
    "v_cvt_pk_bf16_f32 v79, v94, v95\n\t"
    "v_mfma_f32_16x16x32_bf16 v[88:91], a[128:131], a[0:3], v[48:51]\n\t"
    "v_exp_f32 v80, v80\n\t"
    "s_lshl_b32 s69, s60, 14\n\t"
    "v_mfma_f32_16x16x32_bf16 v[88:91], a[132:135], a[4:7], v[88:91]\n\t"
    "ds_read_b64_tr_b16 a[184:185], v10 offset:41984\n\t"
    "v_add_f32 v40, v40, v80\n\t"
    "s_add_u32 s69, s69, s72\n\t"
    "v_mfma_f32_16x16x32_bf16 v[88:91], a[136:139], a[8:11], v[88:91]\n\t"
    "v_exp_f32 v81, v81\n\t"
    "v_and_b32 v2, 15, v0\n\t"
    "v_mfma_f32_16x16x32_bf16 v[88:91], a[140:143], a[12:15], v[88:91]\n\t"
    "v_add_f32 v40, v40, v81\n\t"
    "v_cvt_pk_bf16_f32 v72, v80, v81\n\t"
    "v_lshrrev_b32 v3, 4, v0\n\t"
    "v_mfma_f32_16x16x32_bf16 v[92:95], a[144:147], a[0:3], v[48:51]\n\t"
    "v_exp_f32 v82, v82\n\t"
    "v_mul_u32_u24 v20, 272, v2\n\t"
    "v_mfma_f32_16x16x32_bf16 v[92:95], a[148:151], a[4:7], v[92:95]\n\t"
    "ds_read_b64_tr_b16 a[186:187], v10 offset:46080\n\t"
    "v_add_f32 v40, v40, v82\n\t"
    "v_mfma_f32_16x16x32_bf16 v[92:95], a[152:155], a[8:11], v[92:95]\n\t"
    "v_exp_f32 v83, v83\n\t"
    "v_lshl_add_u32 v20, v3, 3, v20\n\t"
    "v_mfma_f32_16x16x32_bf16 v[92:95], a[156:159], a[12:15], v[92:95]\n\t"
    "v_add_f32 v40, v40, v83\n\t"
    "v_cvt_pk_bf16_f32 v73, v82, v83\n\t"
    "v_and_b32 v22, 8, v0\n\t"
    "v_mfma_f32_16x16x32_bf16 v[192:195], a[192:195], v[76:79], v[192:195]\n\t"
    "v_exp_f32 v84, v84\n\t"
    "v_xor_b32 v20, v22, v20\n\t"
    "v_mfma_f32_16x16x32_bf16 v[196:199], a[196:199], v[76:79], v[196:199]\n\t"
    "ds_read_b64_tr_b16 a[188:189], v11 offset:41984\n\t"
    "v_add_f32 v40, v40, v84\n\t"
    "v_add_u32 v20, s69, v20\n\t"
    "v_mfma_f32_16x16x32_bf16 v[200:203], a[200:203], v[76:79], v[200:203]\n\t"
    "v_exp_f32 v85, v85\n\t"
    "v_mfma_f32_16x16x32_bf16 v[204:207], a[204:207], v[76:79], v[204:207]\n\t"
    "v_add_f32 v40, v40, v85\n\t"
    "v_cvt_pk_bf16_f32 v74, v84, v85\n\t"
    "v_mul_u32_u24 v21, 272, v3\n\t"
    "v_mfma_f32_16x16x32_bf16 v[208:211], a[208:211], v[76:79], v[208:211]\n\t"
    "v_exp_f32 v86, v86\n\t"
    "v_lshl_add_u32 v21, v2, 4, v21\n\t"
    "v_mfma_f32_16x16x32_bf16 v[212:215], a[212:215], v[76:79], v[212:215]\n\t"
    "ds_read_b64_tr_b16 a[190:191], v11 offset:46080\n\t"
    "v_add_f32 v40, v40, v86\n\t"
    "v_add_u32 v21, s69, v21\n\t"
    "v_mfma_f32_16x16x32_bf16 v[216:219], a[216:219], v[76:79], v[216:219]\n\t"
    "v_exp_f32 v87, v87\n\t"
    "v_lshlrev_b32 v22, 4, v0\n\t"
    "v_mfma_f32_16x16x32_bf16 v[220:223], a[220:223], v[76:79], v[220:223]\n\t"
    "s_waitcnt vmcnt(0)\n\t"
    "s_barrier\n\t"
    "v_add_f32 v40, v40, v87\n\t"
    "v_cvt_pk_bf16_f32 v75, v86, v87\n\t"
    "v_mfma_f32_16x16x32_bf16 v[80:83], a[96:99], a[16:19], v[52:55]\n\t"
    "ds_read_b64_tr_b16 a[192:193], v8 offset:57344\n\t"
    "v_exp_f32 v88, v88\n\t"
    "v_mfma_f32_16x16x32_bf16 v[80:83], a[100:103], a[20:23], v[80:83]\n\t"
    "ds_read_b64_tr_b16 a[194:195], v8 offset:61440\n\t"
    "v_add_f32 v40, v40, v88\n\t"
    "v_mfma_f32_16x16x32_bf16 v[80:83], a[104:107], a[24:27], v[80:83]\n\t"
    "ds_read_b64_tr_b16 a[196:197], v9 offset:57344\n\t"
    "v_exp_f32 v89, v89\n\t"
    "v_mfma_f32_16x16x32_bf16 v[80:83], a[108:111], a[28:31], v[80:83]\n\t"
    "ds_read_b64_tr_b16 a[198:199], v9 offset:61440\n\t"
    "v_add_f32 v40, v40, v89\n\t"
    "v_cvt_pk_bf16_f32 v76, v88, v89\n\t"
    "v_mfma_f32_16x16x32_bf16 v[84:87], a[112:115], a[16:19], v[52:55]\n\t"
    "ds_read_b64_tr_b16 a[200:201], v10 offset:57344\n\t"
    "v_exp_f32 v90, v90\n\t"
    "v_mfma_f32_16x16x32_bf16 v[84:87], a[116:119], a[20:23], v[84:87]\n\t"
    "ds_read_b64_tr_b16 a[202:203], v10 offset:61440\n\t"
    "v_add_f32 v40, v40, v90\n\t"
    "v_mfma_f32_16x16x32_bf16 v[84:87], a[120:123], a[24:27], v[84:87]\n\t"
    "ds_read_b64_tr_b16 a[204:205], v11 offset:57344\n\t"
    "v_exp_f32 v91, v91\n\t"
    "v_mfma_f32_16x16x32_bf16 v[84:87], a[124:127], a[28:31], v[84:87]\n\t"
    "s_waitcnt lgkmcnt(7)\n\t"
    "ds_read_b64_tr_b16 a[206:207], v11 offset:61440\n\t"
    "v_add_f32 v40, v40, v91\n\t"
    "v_cvt_pk_bf16_f32 v77, v90, v91\n\t"
    "v_mfma_f32_16x16x32_bf16 a[224:227], a[160:163], v[72:75], a[224:227]\n\t"
    "ds_read_b64_tr_b16 a[208:209], v8 offset:58368\n\t"
    "v_exp_f32 v92, v92\n\t"
    "v_mfma_f32_16x16x32_bf16 a[228:231], a[164:167], v[72:75], a[228:231]\n\t"
    "ds_read_b64_tr_b16 a[210:211], v8 offset:62464\n\t"
    "v_add_f32 v40, v40, v92\n\t"
    "v_mfma_f32_16x16x32_bf16 a[232:235], a[168:171], v[72:75], a[232:235]\n\t"
    "ds_read_b64_tr_b16 a[212:213], v9 offset:58368\n\t"
    "v_exp_f32 v93, v93\n\t"
    "v_mfma_f32_16x16x32_bf16 a[236:239], a[172:175], v[72:75], a[236:239]\n\t"
    "ds_read_b64_tr_b16 a[214:215], v9 offset:62464\n\t"
    "v_add_f32 v40, v40, v93\n\t"
    "v_cvt_pk_bf16_f32 v78, v92, v93\n\t"
    "v_mfma_f32_16x16x32_bf16 a[240:243], a[176:179], v[72:75], a[240:243]\n\t"
    "ds_read_b64_tr_b16 a[216:217], v10 offset:58368\n\t"
    "v_exp_f32 v94, v94\n\t"
    "v_mfma_f32_16x16x32_bf16 a[244:247], a[180:183], v[72:75], a[244:247]\n\t"
    "ds_read_b64_tr_b16 a[218:219], v10 offset:62464\n\t"
    "v_add_f32 v40, v40, v94\n\t"
    "v_mfma_f32_16x16x32_bf16 a[248:251], a[184:187], v[72:75], a[248:251]\n\t"
    "ds_read_b64_tr_b16 a[220:221], v11 offset:58368\n\t"
    "v_exp_f32 v95, v95\n\t"
    "v_mfma_f32_16x16x32_bf16 a[252:255], a[188:191], v[72:75], a[252:255]\n\t"
    "ds_read_b64_tr_b16 a[222:223], v11 offset:62464\n\t"
    "v_add_f32 v40, v40, v95\n\t"
    "v_cvt_pk_bf16_f32 v79, v94, v95\n\t"
    "v_mfma_f32_16x16x32_bf16 v[88:91], a[128:131], a[16:19], v[52:55]\n\t"
    "v_exp_f32 v80, v80\n\t"
    "v_mov_b32 v2, v40\n\t"
    "v_mfma_f32_16x16x32_bf16 v[88:91], a[132:135], a[20:23], v[88:91]\n\t"
    "v_add_f32 v41, v41, v80\n\t"
    "v_permlane16_swap_b32 v40, v2\n\t"
    "v_mfma_f32_16x16x32_bf16 v[88:91], a[136:139], a[24:27], v[88:91]\n\t"
    "v_exp_f32 v81, v81\n\t"
    "v_add_f32 v40, v40, v2\n\t"
    "v_mfma_f32_16x16x32_bf16 v[88:91], a[140:143], a[28:31], v[88:91]\n\t"
    "v_add_f32 v41, v41, v81\n\t"
    "v_cvt_pk_bf16_f32 v72, v80, v81\n\t"
    "v_mov_b32 v2, v40\n\t"
    "v_mfma_f32_16x16x32_bf16 v[92:95], a[144:147], a[16:19], v[52:55]\n\t"
    "v_exp_f32 v82, v82\n\t"
    "v_mfma_f32_16x16x32_bf16 v[92:95], a[148:151], a[20:23], v[92:95]\n\t"
    "v_add_f32 v41, v41, v82\n\t"
    "v_permlane32_swap_b32 v40, v2\n\t"
    "v_mfma_f32_16x16x32_bf16 v[92:95], a[152:155], a[24:27], v[92:95]\n\t"
    "v_exp_f32 v83, v83\n\t"
    "v_add_f32 v40, v40, v2\n\t"
    "v_mfma_f32_16x16x32_bf16 v[92:95], a[156:159], a[28:31], v[92:95]\n\t"
    "s_waitcnt lgkmcnt(0)\n\t"
    "v_add_f32 v41, v41, v83\n\t"
    "v_cvt_pk_bf16_f32 v73, v82, v83\n\t"
    "v_cmp_ngt_f32 vcc, s71, v40\n\t"
    "v_mfma_f32_16x16x32_bf16 a[224:227], a[192:195], v[76:79], a[224:227]\n\t"
    "v_exp_f32 v84, v84\n\t"
    "v_rcp_f32 v23, v40\n\t"
    "v_mfma_f32_16x16x32_bf16 a[228:231], a[196:199], v[76:79], a[228:231]\n\t"
    "v_add_f32 v41, v41, v84\n\t"
    "v_mfma_f32_16x16x32_bf16 a[232:235], a[200:203], v[76:79], a[232:235]\n\t"
    "v_exp_f32 v85, v85\n\t"
    "s_cmp_lg_u64 vcc, 0\n\t"
    "v_mfma_f32_16x16x32_bf16 a[236:239], a[204:207], v[76:79], a[236:239]\n\t"
    "v_add_f32 v41, v41, v85\n\t"
    "v_cvt_pk_bf16_f32 v74, v84, v85\n\t"
    "s_cselect_b32 s70, 1, 0\n\t"
    "v_mfma_f32_16x16x32_bf16 a[240:243], a[208:211], v[76:79], a[240:243]\n\t"
    "v_exp_f32 v86, v86\n\t"
    "s_or_b32 s68, s68, s70\n\t"
    "v_mfma_f32_16x16x32_bf16 a[244:247], a[212:215], v[76:79], a[244:247]\n\t"
    "v_add_f32 v41, v41, v86\n\t"
    "v_mfma_f32_16x16x32_bf16 a[248:251], a[216:219], v[76:79], a[248:251]\n\t"
    "v_exp_f32 v87, v87\n\t"
    "v_mfma_f32_16x16x32_bf16 a[252:255], a[220:223], v[76:79], a[252:255]\n\t"
    "v_add_f32 v41, v41, v87\n\t"
    "v_cvt_pk_bf16_f32 v75, v86, v87\n\t"
    "v_mov_b32 v46, v23\n\t"
    "v_accvgpr_read_b32 v24, a224\n\t"
    "v_accvgpr_read_b32 v25, a225\n\t"
    "v_accvgpr_read_b32 v26, a226\n\t"
    "v_accvgpr_read_b32 v27, a227\n\t"
    "v_accvgpr_read_b32 v28, a228\n\t"
    "v_accvgpr_read_b32 v29, a229\n\t"
    "v_accvgpr_read_b32 v30, a230\n\t"
    "v_accvgpr_read_b32 v31, a231\n\t"
    "v_accvgpr_read_b32 v32, a232\n\t"
    "v_accvgpr_read_b32 v33, a233\n\t"
    "v_accvgpr_read_b32 v34, a234\n\t"
    "v_accvgpr_read_b32 v35, a235\n\t"
    "v_accvgpr_read_b32 v36, a236\n\t"
    "v_accvgpr_read_b32 v37, a237\n\t"
    "v_accvgpr_read_b32 v38, a238\n\t"
    "v_accvgpr_read_b32 v39, a239\n\t"
    "v_pk_mul_f32 v[24:25], v[24:25], v[46:47] op_sel_hi:[1,0]\n\t"
    "v_pk_mul_f32 v[26:27], v[26:27], v[46:47] op_sel_hi:[1,0]\n\t"
    "v_pk_mul_f32 v[28:29], v[28:29], v[46:47] op_sel_hi:[1,0]\n\t"
    "v_pk_mul_f32 v[30:31], v[30:31], v[46:47] op_sel_hi:[1,0]\n\t"
    "v_pk_mul_f32 v[32:33], v[32:33], v[46:47] op_sel_hi:[1,0]\n\t"
    "v_pk_mul_f32 v[34:35], v[34:35], v[46:47] op_sel_hi:[1,0]\n\t"
    "v_cvt_pk_bf16_f32 v24, v24, v25\n\t"
    "v_cvt_pk_bf16_f32 v25, v26, v27\n\t"
    "v_pk_mul_f32 v[36:37], v[36:37], v[46:47] op_sel_hi:[1,0]\n\t"
    "v_pk_mul_f32 v[38:39], v[38:39], v[46:47] op_sel_hi:[1,0]\n\t"
    "v_cvt_pk_bf16_f32 v28, v28, v29\n\t"
    "v_cvt_pk_bf16_f32 v29, v30, v31\n\t"
    "ds_write_b64 v20, v[24:25] offset:0\n\t"
    "v_cvt_pk_bf16_f32 v32, v32, v33\n\t"
    "v_cvt_pk_bf16_f32 v33, v34, v35\n\t"
    "ds_write_b64 v20, v[28:29] offset:32\n\t"
    "v_cvt_pk_bf16_f32 v36, v36, v37\n\t"
    "v_cvt_pk_bf16_f32 v37, v38, v39\n\t"
    "ds_write_b64 v20, v[32:33] offset:64\n\t"
    "ds_write_b64 v20, v[36:37] offset:96\n\t"
    "v_accvgpr_read_b32 v24, a240\n\t"
    "v_accvgpr_read_b32 v25, a241\n\t"
    "v_accvgpr_read_b32 v26, a242\n\t"
    "v_accvgpr_read_b32 v27, a243\n\t"
    "v_accvgpr_read_b32 v28, a244\n\t"
    "v_accvgpr_read_b32 v29, a245\n\t"
    "v_accvgpr_read_b32 v30, a246\n\t"
    "v_accvgpr_read_b32 v31, a247\n\t"
    "v_accvgpr_read_b32 v32, a248\n\t"
    "v_accvgpr_read_b32 v33, a249\n\t"
    "v_accvgpr_read_b32 v34, a250\n\t"
    "v_accvgpr_read_b32 v35, a251\n\t"
    "v_accvgpr_read_b32 v36, a252\n\t"
    "v_accvgpr_read_b32 v37, a253\n\t"
    "v_accvgpr_read_b32 v38, a254\n\t"
    "v_accvgpr_read_b32 v39, a255\n\t"
    "v_pk_mul_f32 v[24:25], v[24:25], v[46:47] op_sel_hi:[1,0]\n\t"
    "v_pk_mul_f32 v[26:27], v[26:27], v[46:47] op_sel_hi:[1,0]\n\t"
    "v_pk_mul_f32 v[28:29], v[28:29], v[46:47] op_sel_hi:[1,0]\n\t"
    "v_pk_mul_f32 v[30:31], v[30:31], v[46:47] op_sel_hi:[1,0]\n\t"
    "v_pk_mul_f32 v[32:33], v[32:33], v[46:47] op_sel_hi:[1,0]\n\t"
    "v_pk_mul_f32 v[34:35], v[34:35], v[46:47] op_sel_hi:[1,0]\n\t"
    "v_cvt_pk_bf16_f32 v24, v24, v25\n\t"
    "v_cvt_pk_bf16_f32 v25, v26, v27\n\t"
    "v_pk_mul_f32 v[36:37], v[36:37], v[46:47] op_sel_hi:[1,0]\n\t"
    "v_pk_mul_f32 v[38:39], v[38:39], v[46:47] op_sel_hi:[1,0]\n\t"
    "v_cvt_pk_bf16_f32 v28, v28, v29\n\t"
    "v_cvt_pk_bf16_f32 v29, v30, v31\n\t"
    "ds_write_b64 v20, v[24:25] offset:128\n\t"
    "v_cvt_pk_bf16_f32 v32, v32, v33\n\t"
    "v_cvt_pk_bf16_f32 v33, v34, v35\n\t"
    "ds_write_b64 v20, v[28:29] offset:160\n\t"
    "v_cvt_pk_bf16_f32 v36, v36, v37\n\t"
    "v_cvt_pk_bf16_f32 v37, v38, v39\n\t"
    "ds_write_b64 v20, v[32:33] offset:192\n\t"
    "ds_write_b64 v20, v[36:37] offset:224\n\t"
    "v_mfma_f32_16x16x32_bf16 v[80:83], a[96:99], a[32:35], v[56:59]\n\t"
    "v_exp_f32 v88, v88\n\t"
    "v_mfma_f32_16x16x32_bf16 v[80:83], a[100:103], a[36:39], v[80:83]\n\t"
    "v_add_f32 v41, v41, v88\n\t"
    "v_mfma_f32_16x16x32_bf16 v[80:83], a[104:107], a[40:43], v[80:83]\n\t"
    "v_exp_f32 v89, v89\n\t"
    "v_mfma_f32_16x16x32_bf16 v[80:83], a[108:111], a[44:47], v[80:83]\n\t"
    "v_add_f32 v41, v41, v89\n\t"
    "v_cvt_pk_bf16_f32 v76, v88, v89\n\t"
    "v_mfma_f32_16x16x32_bf16 v[84:87], a[112:115], a[32:35], v[56:59]\n\t"
    "s_waitcnt lgkmcnt(0)\n\t"
    "v_exp_f32 v90, v90\n\t"
    "v_mfma_f32_16x16x32_bf16 v[84:87], a[116:119], a[36:39], v[84:87]\n\t"
    "ds_read_b128 v[12:15], v21 offset:0\n\t"
    "v_add_f32 v41, v41, v90\n\t"
    "v_mfma_f32_16x16x32_bf16 v[84:87], a[120:123], a[40:43], v[84:87]\n\t"
    "ds_read_b128 v[48:51], v21 offset:1088\n\t"
    "v_exp_f32 v91, v91\n\t"
    "v_mfma_f32_16x16x32_bf16 v[84:87], a[124:127], a[44:47], v[84:87]\n\t"
    "v_add_u32 v47, 2176, v21\n\t"
    "v_add_f32 v41, v41, v91\n\t"
    "v_cvt_pk_bf16_f32 v77, v90, v91\n\t"
    "v_mfma_f32_16x16x32_bf16 v[96:99], a[160:163], v[72:75], v[96:99]\n\t"
    "ds_read2_b64 v[24:27], v47 offset0:1 offset1:0\n\t"
    "v_exp_f32 v92, v92\n\t"
    "v_mfma_f32_16x16x32_bf16 v[100:103], a[164:167], v[72:75], v[100:103]\n\t"
    "ds_read2_b64 v[28:31], v47 offset0:137 offset1:136\n\t"
    "s_add_u32 s73, s66, 0\n\t"
    "v_add_f32 v41, v41, v92\n\t"
    "v_mfma_f32_16x16x32_bf16 v[104:107], a[168:171], v[72:75], v[104:107]\n\t"
    "v_exp_f32 v93, v93\n\t"
    "v_mfma_f32_16x16x32_bf16 v[108:111], a[172:175], v[72:75], v[108:111]\n\t"
    "s_waitcnt lgkmcnt(3)\n\t"
    "buffer_store_dwordx4 v[12:15], v22, s[52:55], s73 offen offset:0 nt\n\t"
    "v_add_f32 v41, v41, v93\n\t"
    "v_cvt_pk_bf16_f32 v78, v92, v93\n\t"
    "v_mfma_f32_16x16x32_bf16 v[112:115], a[176:179], v[72:75], v[112:115]\n\t"
    "v_exp_f32 v94, v94\n\t"
    "v_mfma_f32_16x16x32_bf16 v[116:119], a[180:183], v[72:75], v[116:119]\n\t"
    "s_waitcnt lgkmcnt(2)\n\t"
    "buffer_store_dwordx4 v[48:51], v22, s[52:55], s73 offen offset:1024 nt\n\t"
    "v_add_f32 v41, v41, v94\n\t"
    "v_mfma_f32_16x16x32_bf16 v[120:123], a[184:187], v[72:75], v[120:123]\n\t"
    "s_waitcnt lgkmcnt(1)\n\t"
    "buffer_store_dwordx4 v[24:27], v22, s[52:55], s73 offen offset:2048 nt\n\t"
    "v_exp_f32 v95, v95\n\t"
    "v_mfma_f32_16x16x32_bf16 v[124:127], a[188:191], v[72:75], v[124:127]\n\t"
    "s_waitcnt lgkmcnt(0)\n\t"
    "buffer_store_dwordx4 v[28:31], v22, s[52:55], s73 offen offset:3072 nt\n\t"
    "v_add_f32 v41, v41, v95\n\t"
    "v_cvt_pk_bf16_f32 v79, v94, v95\n\t"
    "v_mfma_f32_16x16x32_bf16 v[88:91], a[128:131], a[32:35], v[56:59]\n\t"
    "v_exp_f32 v80, v80\n\t"
    "v_mov_b32 v2, v41\n\t"
    "v_mfma_f32_16x16x32_bf16 v[88:91], a[132:135], a[36:39], v[88:91]\n\t"
    "v_add_f32 v42, v42, v80\n\t"
    "v_permlane16_swap_b32 v41, v2\n\t"
    "v_mfma_f32_16x16x32_bf16 v[88:91], a[136:139], a[40:43], v[88:91]\n\t"
    "v_exp_f32 v81, v81\n\t"
    "v_add_f32 v41, v41, v2\n\t"
    "v_mfma_f32_16x16x32_bf16 v[88:91], a[140:143], a[44:47], v[88:91]\n\t"
    "v_add_f32 v42, v42, v81\n\t"
    "v_cvt_pk_bf16_f32 v72, v80, v81\n\t"
    "v_mov_b32 v2, v41\n\t"
    "v_mfma_f32_16x16x32_bf16 v[92:95], a[144:147], a[32:35], v[56:59]\n\t"
    "v_exp_f32 v82, v82\n\t"
    "v_mfma_f32_16x16x32_bf16 v[92:95], a[148:151], a[36:39], v[92:95]\n\t"
    "v_add_f32 v42, v42, v82\n\t"
    "v_permlane32_swap_b32 v41, v2\n\t"
    "v_mfma_f32_16x16x32_bf16 v[92:95], a[152:155], a[40:43], v[92:95]\n\t"
    "v_exp_f32 v83, v83\n\t"
    "v_add_f32 v41, v41, v2\n\t"
    "v_mfma_f32_16x16x32_bf16 v[92:95], a[156:159], a[44:47], v[92:95]\n\t"
    "v_add_f32 v42, v42, v83\n\t"
    "v_cvt_pk_bf16_f32 v73, v82, v83\n\t"
    "v_cmp_ngt_f32 vcc, s71, v41\n\t"
    "v_mfma_f32_16x16x32_bf16 v[96:99], a[192:195], v[76:79], v[96:99]\n\t"
    "v_exp_f32 v84, v84\n\t"
    "v_rcp_f32 v3, v41\n\t"
    "v_mfma_f32_16x16x32_bf16 v[100:103], a[196:199], v[76:79], v[100:103]\n\t"
    "v_add_f32 v42, v42, v84\n\t"
    "v_mfma_f32_16x16x32_bf16 v[104:107], a[200:203], v[76:79], v[104:107]\n\t"
    "v_exp_f32 v85, v85\n\t"
    "s_cmp_lg_u64 vcc, 0\n\t"
    "v_mfma_f32_16x16x32_bf16 v[108:111], a[204:207], v[76:79], v[108:111]\n\t"
    "v_add_f32 v42, v42, v85\n\t"
    "v_cvt_pk_bf16_f32 v74, v84, v85\n\t"
    "s_cselect_b32 s70, 2, 0\n\t"
    "v_mfma_f32_16x16x32_bf16 v[112:115], a[208:211], v[76:79], v[112:115]\n\t"
    "v_exp_f32 v86, v86\n\t"
    "s_or_b32 s68, s68, s70\n\t"
    "v_mfma_f32_16x16x32_bf16 v[116:119], a[212:215], v[76:79], v[116:119]\n\t"
    "v_add_f32 v42, v42, v86\n\t"
    "v_mfma_f32_16x16x32_bf16 v[120:123], a[216:219], v[76:79], v[120:123]\n\t"
    "v_exp_f32 v87, v87\n\t"
    "v_mfma_f32_16x16x32_bf16 v[124:127], a[220:223], v[76:79], v[124:127]\n\t"
    "v_add_f32 v42, v42, v87\n\t"
    "v_cvt_pk_bf16_f32 v75, v86, v87\n\t"
    "v_mov_b32 v46, v3\n\t"
    "v_pk_mul_f32 v[96:97], v[96:97], v[46:47] op_sel_hi:[1,0]\n\t"
    "v_pk_mul_f32 v[98:99], v[98:99], v[46:47] op_sel_hi:[1,0]\n\t"
    "v_pk_mul_f32 v[100:101], v[100:101], v[46:47] op_sel_hi:[1,0]\n\t"
    "v_pk_mul_f32 v[102:103], v[102:103], v[46:47] op_sel_hi:[1,0]\n\t"
    "v_pk_mul_f32 v[104:105], v[104:105], v[46:47] op_sel_hi:[1,0]\n\t"
    "v_pk_mul_f32 v[106:107], v[106:107], v[46:47] op_sel_hi:[1,0]\n\t"
    "v_cvt_pk_bf16_f32 v96, v96, v97\n\t"
    "v_cvt_pk_bf16_f32 v97, v98, v99\n\t"
    "v_pk_mul_f32 v[108:109], v[108:109], v[46:47] op_sel_hi:[1,0]\n\t"
    "v_pk_mul_f32 v[110:111], v[110:111], v[46:47] op_sel_hi:[1,0]\n\t"
    "v_cvt_pk_bf16_f32 v100, v100, v101\n\t"
    "v_cvt_pk_bf16_f32 v101, v102, v103\n\t"
    "ds_write_b64 v20, v[96:97] offset:4352\n\t"
    "v_pk_mul_f32 v[112:113], v[112:113], v[46:47] op_sel_hi:[1,0]\n\t"
    "v_pk_mul_f32 v[114:115], v[114:115], v[46:47] op_sel_hi:[1,0]\n\t"
    "v_cvt_pk_bf16_f32 v104, v104, v105\n\t"
    "v_cvt_pk_bf16_f32 v105, v106, v107\n\t"
    "ds_write_b64 v20, v[100:101] offset:4384\n\t"
    "v_pk_mul_f32 v[116:117], v[116:117], v[46:47] op_sel_hi:[1,0]\n\t"
    "v_pk_mul_f32 v[118:119], v[118:119], v[46:47] op_sel_hi:[1,0]\n\t"
    "v_cvt_pk_bf16_f32 v108, v108, v109\n\t"
    "v_cvt_pk_bf16_f32 v109, v110, v111\n\t"
    "ds_write_b64 v20, v[104:105] offset:4416\n\t"
    "v_pk_mul_f32 v[120:121], v[120:121], v[46:47] op_sel_hi:[1,0]\n\t"
    "v_pk_mul_f32 v[122:123], v[122:123], v[46:47] op_sel_hi:[1,0]\n\t"
    "v_cvt_pk_bf16_f32 v112, v112, v113\n\t"
    "v_cvt_pk_bf16_f32 v113, v114, v115\n\t"
    "ds_write_b64 v20, v[108:109] offset:4448\n\t"
    "v_pk_mul_f32 v[124:125], v[124:125], v[46:47] op_sel_hi:[1,0]\n\t"
    "v_pk_mul_f32 v[126:127], v[126:127], v[46:47] op_sel_hi:[1,0]\n\t"
    "v_cvt_pk_bf16_f32 v116, v116, v117\n\t"
    "v_cvt_pk_bf16_f32 v117, v118, v119\n\t"
    "ds_write_b64 v20, v[112:113] offset:4480\n\t"
    "v_cvt_pk_bf16_f32 v120, v120, v121\n\t"
    "v_cvt_pk_bf16_f32 v121, v122, v123\n\t"
    "ds_write_b64 v20, v[116:117] offset:4512\n\t"
    "v_cvt_pk_bf16_f32 v124, v124, v125\n\t"
    "v_cvt_pk_bf16_f32 v125, v126, v127\n\t"
    "ds_write_b64 v20, v[120:121] offset:4544\n\t"
    "ds_write_b64 v20, v[124:125] offset:4576\n\t"
    "v_mfma_f32_16x16x32_bf16 v[80:83], a[96:99], a[48:51], v[60:63]\n\t"
    "v_exp_f32 v88, v88\n\t"
    "v_mfma_f32_16x16x32_bf16 v[80:83], a[100:103], a[52:55], v[80:83]\n\t"
    "v_add_f32 v42, v42, v88\n\t"
    "v_mfma_f32_16x16x32_bf16 v[80:83], a[104:107], a[56:59], v[80:83]\n\t"
    "v_exp_f32 v89, v89\n\t"
    "v_mfma_f32_16x16x32_bf16 v[80:83], a[108:111], a[60:63], v[80:83]\n\t"
    "v_add_f32 v42, v42, v89\n\t"
    "v_cvt_pk_bf16_f32 v76, v88, v89\n\t"
    "v_mfma_f32_16x16x32_bf16 v[84:87], a[112:115], a[48:51], v[60:63]\n\t"
    "s_waitcnt lgkmcnt(0)\n\t"
    "v_exp_f32 v90, v90\n\t"
    "v_mfma_f32_16x16x32_bf16 v[84:87], a[116:119], a[52:55], v[84:87]\n\t"
    "ds_read_b128 v[32:35], v21 offset:4352\n\t"
    "v_add_f32 v42, v42, v90\n\t"
    "v_mfma_f32_16x16x32_bf16 v[84:87], a[120:123], a[56:59], v[84:87]\n\t"
    "ds_read_b128 v[36:39], v21 offset:5440\n\t"
    "v_exp_f32 v91, v91\n\t"
    "v_mfma_f32_16x16x32_bf16 v[84:87], a[124:127], a[60:63], v[84:87]\n\t"
    "v_add_u32 v47, 6528, v21\n\t"
    "v_add_f32 v42, v42, v91\n\t"
    "v_cvt_pk_bf16_f32 v77, v90, v91\n\t"
    "v_mfma_f32_16x16x32_bf16 v[128:131], a[160:163], v[72:75], v[128:131]\n\t"
    "ds_read2_b64 v[52:55], v47 offset0:1 offset1:0\n\t"
    "v_exp_f32 v92, v92\n\t"
    "v_mfma_f32_16x16x32_bf16 v[132:135], a[164:167], v[72:75], v[132:135]\n\t"
    "ds_read2_b64 v[48:51], v47 offset0:137 offset1:136\n\t"
    "s_add_u32 s73, s66, 16384\n\t"
    "v_add_f32 v42, v42, v92\n\t"
    "v_mfma_f32_16x16x32_bf16 v[136:139], a[168:171], v[72:75], v[136:139]\n\t"
    "v_exp_f32 v93, v93\n\t"
    "v_mfma_f32_16x16x32_bf16 v[140:143], a[172:175], v[72:75], v[140:143]\n\t"
    "s_waitcnt lgkmcnt(3)\n\t"
    "buffer_store_dwordx4 v[32:35], v22, s[52:55], s73 offen offset:0 nt\n\t"
    "v_add_f32 v42, v42, v93\n\t"
    "v_cvt_pk_bf16_f32 v78, v92, v93\n\t"
    "v_mfma_f32_16x16x32_bf16 v[144:147], a[176:179], v[72:75], v[144:147]\n\t"
    "v_exp_f32 v94, v94\n\t"
    "v_mfma_f32_16x16x32_bf16 v[148:151], a[180:183], v[72:75], v[148:151]\n\t"
    "s_waitcnt lgkmcnt(2)\n\t"
    "buffer_store_dwordx4 v[36:39], v22, s[52:55], s73 offen offset:1024 nt\n\t"
    "v_add_f32 v42, v42, v94\n\t"
    "v_mfma_f32_16x16x32_bf16 v[152:155], a[184:187], v[72:75], v[152:155]\n\t"
    "s_waitcnt lgkmcnt(1)\n\t"
    "buffer_store_dwordx4 v[52:55], v22, s[52:55], s73 offen offset:2048 nt\n\t"
    "v_exp_f32 v95, v95\n\t"
    "v_mfma_f32_16x16x32_bf16 v[156:159], a[188:191], v[72:75], v[156:159]\n\t"
    "s_waitcnt lgkmcnt(0)\n\t"
    "buffer_store_dwordx4 v[48:51], v22, s[52:55], s73 offen offset:3072 nt\n\t"
    "v_add_f32 v42, v42, v95\n\t"
    "v_cvt_pk_bf16_f32 v79, v94, v95\n\t"
    "v_mfma_f32_16x16x32_bf16 v[88:91], a[128:131], a[48:51], v[60:63]\n\t"
    "v_exp_f32 v80, v80\n\t"
    "v_mov_b32 v2, v42\n\t"
    "v_mfma_f32_16x16x32_bf16 v[88:91], a[132:135], a[52:55], v[88:91]\n\t"
    "v_add_f32 v43, v43, v80\n\t"
    "v_permlane16_swap_b32 v42, v2\n\t"
    "v_mfma_f32_16x16x32_bf16 v[88:91], a[136:139], a[56:59], v[88:91]\n\t"
    "v_exp_f32 v81, v81\n\t"
    "v_add_f32 v42, v42, v2\n\t"
    "v_mfma_f32_16x16x32_bf16 v[88:91], a[140:143], a[60:63], v[88:91]\n\t"
    "v_add_f32 v43, v43, v81\n\t"
    "v_cvt_pk_bf16_f32 v72, v80, v81\n\t"
    "v_mov_b32 v2, v42\n\t"
    "v_mfma_f32_16x16x32_bf16 v[92:95], a[144:147], a[48:51], v[60:63]\n\t"
    "v_exp_f32 v82, v82\n\t"
    "v_mfma_f32_16x16x32_bf16 v[92:95], a[148:151], a[52:55], v[92:95]\n\t"
    "v_add_f32 v43, v43, v82\n\t"
    "v_permlane32_swap_b32 v42, v2\n\t"
    "v_mfma_f32_16x16x32_bf16 v[92:95], a[152:155], a[56:59], v[92:95]\n\t"
    "v_exp_f32 v83, v83\n\t"
    "v_add_f32 v42, v42, v2\n\t"
    "v_mfma_f32_16x16x32_bf16 v[92:95], a[156:159], a[60:63], v[92:95]\n\t"
    "v_add_f32 v43, v43, v83\n\t"
    "v_cvt_pk_bf16_f32 v73, v82, v83\n\t"
    "v_cmp_ngt_f32 vcc, s71, v42\n\t"
    "v_mfma_f32_16x16x32_bf16 v[128:131], a[192:195], v[76:79], v[128:131]\n\t"
    "v_exp_f32 v84, v84\n\t"
    "v_rcp_f32 v23, v42\n\t"
    "v_mfma_f32_16x16x32_bf16 v[132:135], a[196:199], v[76:79], v[132:135]\n\t"
    "v_add_f32 v43, v43, v84\n\t"
    "v_mfma_f32_16x16x32_bf16 v[136:139], a[200:203], v[76:79], v[136:139]\n\t"
    "v_exp_f32 v85, v85\n\t"
    "s_cmp_lg_u64 vcc, 0\n\t"
    "v_mfma_f32_16x16x32_bf16 v[140:143], a[204:207], v[76:79], v[140:143]\n\t"
    "v_add_f32 v43, v43, v85\n\t"
    "v_cvt_pk_bf16_f32 v74, v84, v85\n\t"
    "s_cselect_b32 s70, 4, 0\n\t"
    "v_mfma_f32_16x16x32_bf16 v[144:147], a[208:211], v[76:79], v[144:147]\n\t"
    "v_exp_f32 v86, v86\n\t"
    "s_or_b32 s68, s68, s70\n\t"
    "v_mfma_f32_16x16x32_bf16 v[148:151], a[212:215], v[76:79], v[148:151]\n\t"
    "v_add_f32 v43, v43, v86\n\t"
    "v_mfma_f32_16x16x32_bf16 v[152:155], a[216:219], v[76:79], v[152:155]\n\t"
    "v_exp_f32 v87, v87\n\t"
    "v_mfma_f32_16x16x32_bf16 v[156:159], a[220:223], v[76:79], v[156:159]\n\t"
    "v_add_f32 v43, v43, v87\n\t"
    "v_cvt_pk_bf16_f32 v75, v86, v87\n\t"
    "v_mov_b32 v46, v23\n\t"
    "v_pk_mul_f32 v[128:129], v[128:129], v[46:47] op_sel_hi:[1,0]\n\t"
    "v_pk_mul_f32 v[130:131], v[130:131], v[46:47] op_sel_hi:[1,0]\n\t"
    "v_pk_mul_f32 v[132:133], v[132:133], v[46:47] op_sel_hi:[1,0]\n\t"
    "v_pk_mul_f32 v[134:135], v[134:135], v[46:47] op_sel_hi:[1,0]\n\t"
    "v_pk_mul_f32 v[136:137], v[136:137], v[46:47] op_sel_hi:[1,0]\n\t"
    "v_pk_mul_f32 v[138:139], v[138:139], v[46:47] op_sel_hi:[1,0]\n\t"
    "v_cvt_pk_bf16_f32 v128, v128, v129\n\t"
    "v_cvt_pk_bf16_f32 v129, v130, v131\n\t"
    "v_pk_mul_f32 v[140:141], v[140:141], v[46:47] op_sel_hi:[1,0]\n\t"
    "v_pk_mul_f32 v[142:143], v[142:143], v[46:47] op_sel_hi:[1,0]\n\t"
    "v_cvt_pk_bf16_f32 v132, v132, v133\n\t"
    "v_cvt_pk_bf16_f32 v133, v134, v135\n\t"
    "ds_write_b64 v20, v[128:129] offset:8704\n\t"
    "v_pk_mul_f32 v[144:145], v[144:145], v[46:47] op_sel_hi:[1,0]\n\t"
    "v_pk_mul_f32 v[146:147], v[146:147], v[46:47] op_sel_hi:[1,0]\n\t"
    "v_cvt_pk_bf16_f32 v136, v136, v137\n\t"
    "v_cvt_pk_bf16_f32 v137, v138, v139\n\t"
    "ds_write_b64 v20, v[132:133] offset:8736\n\t"
    "v_pk_mul_f32 v[148:149], v[148:149], v[46:47] op_sel_hi:[1,0]\n\t"
    "v_pk_mul_f32 v[150:151], v[150:151], v[46:47] op_sel_hi:[1,0]\n\t"
    "v_cvt_pk_bf16_f32 v140, v140, v141\n\t"
    "v_cvt_pk_bf16_f32 v141, v142, v143\n\t"
    "ds_write_b64 v20, v[136:137] offset:8768\n\t"
    "v_pk_mul_f32 v[152:153], v[152:153], v[46:47] op_sel_hi:[1,0]\n\t"
    "v_pk_mul_f32 v[154:155], v[154:155], v[46:47] op_sel_hi:[1,0]\n\t"
    "v_cvt_pk_bf16_f32 v144, v144, v145\n\t"
    "v_cvt_pk_bf16_f32 v145, v146, v147\n\t"
    "ds_write_b64 v20, v[140:141] offset:8800\n\t"
    "v_pk_mul_f32 v[156:157], v[156:157], v[46:47] op_sel_hi:[1,0]\n\t"
    "v_pk_mul_f32 v[158:159], v[158:159], v[46:47] op_sel_hi:[1,0]\n\t"
    "v_cvt_pk_bf16_f32 v148, v148, v149\n\t"
    "v_cvt_pk_bf16_f32 v149, v150, v151\n\t"
    "ds_write_b64 v20, v[144:145] offset:8832\n\t"
    "v_cvt_pk_bf16_f32 v152, v152, v153\n\t"
    "v_cvt_pk_bf16_f32 v153, v154, v155\n\t"
    "ds_write_b64 v20, v[148:149] offset:8864\n\t"
    "v_cvt_pk_bf16_f32 v156, v156, v157\n\t"
    "v_cvt_pk_bf16_f32 v157, v158, v159\n\t"
    "ds_write_b64 v20, v[152:153] offset:8896\n\t"
    "ds_write_b64 v20, v[156:157] offset:8928\n\t"
    "v_mfma_f32_16x16x32_bf16 v[80:83], a[96:99], a[64:67], v[64:67]\n\t"
    "v_exp_f32 v88, v88\n\t"
    "v_mfma_f32_16x16x32_bf16 v[80:83], a[100:103], a[68:71], v[80:83]\n\t"
    "v_add_f32 v43, v43, v88\n\t"
    "v_mfma_f32_16x16x32_bf16 v[80:83], a[104:107], a[72:75], v[80:83]\n\t"
    "v_exp_f32 v89, v89\n\t"
    "v_mfma_f32_16x16x32_bf16 v[80:83], a[108:111], a[76:79], v[80:83]\n\t"
    "v_add_f32 v43, v43, v89\n\t"
    "v_cvt_pk_bf16_f32 v76, v88, v89\n\t"
    "v_mfma_f32_16x16x32_bf16 v[84:87], a[112:115], a[64:67], v[64:67]\n\t"
    "s_waitcnt lgkmcnt(0)\n\t"
    "v_exp_f32 v90, v90\n\t"
    "v_mfma_f32_16x16x32_bf16 v[84:87], a[116:119], a[68:71], v[84:87]\n\t"
    "ds_read_b128 v[32:35], v21 offset:8704\n\t"
    "v_add_f32 v43, v43, v90\n\t"
    "v_mfma_f32_16x16x32_bf16 v[84:87], a[120:123], a[72:75], v[84:87]\n\t"
    "ds_read_b128 v[36:39], v21 offset:9792\n\t"
    "v_exp_f32 v91, v91\n\t"
    "v_mfma_f32_16x16x32_bf16 v[84:87], a[124:127], a[76:79], v[84:87]\n\t"
    "v_add_u32 v47, 10880, v21\n\t"
    "v_add_f32 v43, v43, v91\n\t"
    "v_cvt_pk_bf16_f32 v77, v90, v91\n\t"
    "v_mfma_f32_16x16x32_bf16 v[160:163], a[160:163], v[72:75], v[160:163]\n\t"
    "ds_read2_b64 v[56:59], v47 offset0:1 offset1:0\n\t"
    "v_exp_f32 v92, v92\n\t"
    "v_mfma_f32_16x16x32_bf16 v[164:167], a[164:167], v[72:75], v[164:167]\n\t"
    "ds_read2_b64 v[52:55], v47 offset0:137 offset1:136\n\t"
    "s_add_u32 s73, s66, 32768\n\t"
    "v_add_f32 v43, v43, v92\n\t"
    "v_mfma_f32_16x16x32_bf16 v[168:171], a[168:171], v[72:75], v[168:171]\n\t"
    "v_exp_f32 v93, v93\n\t"
    "v_mfma_f32_16x16x32_bf16 v[172:175], a[172:175], v[72:75], v[172:175]\n\t"
    "s_waitcnt lgkmcnt(3)\n\t"
    "buffer_store_dwordx4 v[32:35], v22, s[52:55], s73 offen offset:0 nt\n\t"
    "v_add_f32 v43, v43, v93\n\t"
    "v_cvt_pk_bf16_f32 v78, v92, v93\n\t"
    "v_mfma_f32_16x16x32_bf16 v[176:179], a[176:179], v[72:75], v[176:179]\n\t"
    "v_exp_f32 v94, v94\n\t"
    "v_mfma_f32_16x16x32_bf16 v[180:183], a[180:183], v[72:75], v[180:183]\n\t"
    "s_waitcnt lgkmcnt(2)\n\t"
    "buffer_store_dwordx4 v[36:39], v22, s[52:55], s73 offen offset:1024 nt\n\t"
    "v_add_f32 v43, v43, v94\n\t"
    "v_mfma_f32_16x16x32_bf16 v[184:187], a[184:187], v[72:75], v[184:187]\n\t"
    "s_waitcnt lgkmcnt(1)\n\t"
    "buffer_store_dwordx4 v[56:59], v22, s[52:55], s73 offen offset:2048 nt\n\t"
    "v_exp_f32 v95, v95\n\t"
    "v_mfma_f32_16x16x32_bf16 v[188:191], a[188:191], v[72:75], v[188:191]\n\t"
    "s_waitcnt lgkmcnt(0)\n\t"
    "buffer_store_dwordx4 v[52:55], v22, s[52:55], s73 offen offset:3072 nt\n\t"
    "v_add_f32 v43, v43, v95\n\t"
    "v_cvt_pk_bf16_f32 v79, v94, v95\n\t"
    "v_mfma_f32_16x16x32_bf16 v[88:91], a[128:131], a[64:67], v[64:67]\n\t"
    "v_exp_f32 v80, v80\n\t"
    "v_mov_b32 v2, v43\n\t"
    "v_mfma_f32_16x16x32_bf16 v[88:91], a[132:135], a[68:71], v[88:91]\n\t"
    "v_add_f32 v44, v44, v80\n\t"
    "v_permlane16_swap_b32 v43, v2\n\t"
    "v_mfma_f32_16x16x32_bf16 v[88:91], a[136:139], a[72:75], v[88:91]\n\t"
    "v_exp_f32 v81, v81\n\t"
    "v_add_f32 v43, v43, v2\n\t"
    "v_mfma_f32_16x16x32_bf16 v[88:91], a[140:143], a[76:79], v[88:91]\n\t"
    "v_add_f32 v44, v44, v81\n\t"
    "v_cvt_pk_bf16_f32 v72, v80, v81\n\t"
    "v_mov_b32 v2, v43\n\t"
    "v_mfma_f32_16x16x32_bf16 v[92:95], a[144:147], a[64:67], v[64:67]\n\t"
    "v_exp_f32 v82, v82\n\t"
    "v_mfma_f32_16x16x32_bf16 v[92:95], a[148:151], a[68:71], v[92:95]\n\t"
    "v_add_f32 v44, v44, v82\n\t"
    "v_permlane32_swap_b32 v43, v2\n\t"
    "v_mfma_f32_16x16x32_bf16 v[92:95], a[152:155], a[72:75], v[92:95]\n\t"
    "v_exp_f32 v83, v83\n\t"
    "v_add_f32 v43, v43, v2\n\t"
    "v_mfma_f32_16x16x32_bf16 v[92:95], a[156:159], a[76:79], v[92:95]\n\t"
    "v_add_f32 v44, v44, v83\n\t"
    "v_cvt_pk_bf16_f32 v73, v82, v83\n\t"
    "v_cmp_ngt_f32 vcc, s71, v43\n\t"
    "v_mfma_f32_16x16x32_bf16 v[160:163], a[192:195], v[76:79], v[160:163]\n\t"
    "v_exp_f32 v84, v84\n\t"
    "v_rcp_f32 v3, v43\n\t"
    "v_mfma_f32_16x16x32_bf16 v[164:167], a[196:199], v[76:79], v[164:167]\n\t"
    "v_add_f32 v44, v44, v84\n\t"
    "v_mfma_f32_16x16x32_bf16 v[168:171], a[200:203], v[76:79], v[168:171]\n\t"
    "v_exp_f32 v85, v85\n\t"
    "s_cmp_lg_u64 vcc, 0\n\t"
    "v_mfma_f32_16x16x32_bf16 v[172:175], a[204:207], v[76:79], v[172:175]\n\t"
    "v_add_f32 v44, v44, v85\n\t"
    "v_cvt_pk_bf16_f32 v74, v84, v85\n\t"
    "s_cselect_b32 s70, 8, 0\n\t"
    "v_mfma_f32_16x16x32_bf16 v[176:179], a[208:211], v[76:79], v[176:179]\n\t"
    "v_exp_f32 v86, v86\n\t"
    "s_or_b32 s68, s68, s70\n\t"
    "v_mfma_f32_16x16x32_bf16 v[180:183], a[212:215], v[76:79], v[180:183]\n\t"
    "v_add_f32 v44, v44, v86\n\t"
    "v_mfma_f32_16x16x32_bf16 v[184:187], a[216:219], v[76:79], v[184:187]\n\t"
    "v_exp_f32 v87, v87\n\t"
    "v_mfma_f32_16x16x32_bf16 v[188:191], a[220:223], v[76:79], v[188:191]\n\t"
    "v_add_f32 v44, v44, v87\n\t"
    "v_cvt_pk_bf16_f32 v75, v86, v87\n\t"
    "v_mov_b32 v46, v3\n\t"
    "v_pk_mul_f32 v[160:161], v[160:161], v[46:47] op_sel_hi:[1,0]\n\t"
    "v_pk_mul_f32 v[162:163], v[162:163], v[46:47] op_sel_hi:[1,0]\n\t"
    "v_pk_mul_f32 v[164:165], v[164:165], v[46:47] op_sel_hi:[1,0]\n\t"
    "v_pk_mul_f32 v[166:167], v[166:167], v[46:47] op_sel_hi:[1,0]\n\t"
    "v_pk_mul_f32 v[168:169], v[168:169], v[46:47] op_sel_hi:[1,0]\n\t"
    "v_pk_mul_f32 v[170:171], v[170:171], v[46:47] op_sel_hi:[1,0]\n\t"
    "v_cvt_pk_bf16_f32 v160, v160, v161\n\t"
    "v_cvt_pk_bf16_f32 v161, v162, v163\n\t"
    "v_pk_mul_f32 v[172:173], v[172:173], v[46:47] op_sel_hi:[1,0]\n\t"
    "v_pk_mul_f32 v[174:175], v[174:175], v[46:47] op_sel_hi:[1,0]\n\t"
    "v_cvt_pk_bf16_f32 v164, v164, v165\n\t"
    "v_cvt_pk_bf16_f32 v165, v166, v167\n\t"
    "ds_write_b64 v20, v[160:161] offset:0\n\t"
    "v_pk_mul_f32 v[176:177], v[176:177], v[46:47] op_sel_hi:[1,0]\n\t"
    "v_pk_mul_f32 v[178:179], v[178:179], v[46:47] op_sel_hi:[1,0]\n\t"
    "v_cvt_pk_bf16_f32 v168, v168, v169\n\t"
    "v_cvt_pk_bf16_f32 v169, v170, v171\n\t"
    "ds_write_b64 v20, v[164:165] offset:32\n\t"
    "v_pk_mul_f32 v[180:181], v[180:181], v[46:47] op_sel_hi:[1,0]\n\t"
    "v_pk_mul_f32 v[182:183], v[182:183], v[46:47] op_sel_hi:[1,0]\n\t"
    "v_cvt_pk_bf16_f32 v172, v172, v173\n\t"
    "v_cvt_pk_bf16_f32 v173, v174, v175\n\t"
    "ds_write_b64 v20, v[168:169] offset:64\n\t"
    "v_pk_mul_f32 v[184:185], v[184:185], v[46:47] op_sel_hi:[1,0]\n\t"
    "v_pk_mul_f32 v[186:187], v[186:187], v[46:47] op_sel_hi:[1,0]\n\t"
    "v_cvt_pk_bf16_f32 v176, v176, v177\n\t"
    "v_cvt_pk_bf16_f32 v177, v178, v179\n\t"
    "ds_write_b64 v20, v[172:173] offset:96\n\t"
    "v_pk_mul_f32 v[188:189], v[188:189], v[46:47] op_sel_hi:[1,0]\n\t"
    "v_pk_mul_f32 v[190:191], v[190:191], v[46:47] op_sel_hi:[1,0]\n\t"
    "v_cvt_pk_bf16_f32 v180, v180, v181\n\t"
    "v_cvt_pk_bf16_f32 v181, v182, v183\n\t"
    "ds_write_b64 v20, v[176:177] offset:128\n\t"
    "v_cvt_pk_bf16_f32 v184, v184, v185\n\t"
    "v_cvt_pk_bf16_f32 v185, v186, v187\n\t"
    "ds_write_b64 v20, v[180:181] offset:160\n\t"
    "v_cvt_pk_bf16_f32 v188, v188, v189\n\t"
    "v_cvt_pk_bf16_f32 v189, v190, v191\n\t"
    "ds_write_b64 v20, v[184:185] offset:192\n\t"
    "ds_write_b64 v20, v[188:189] offset:224\n\t"
    "v_exp_f32 v88, v88\n\t"
    "s_nop 0\n\t"
    "v_add_f32 v44, v44, v88\n\t"
    "v_exp_f32 v89, v89\n\t"
    "s_nop 0\n\t"
    "v_add_f32 v44, v44, v89\n\t"
    "v_cvt_pk_bf16_f32 v76, v88, v89\n\t"
    "s_waitcnt lgkmcnt(0)\n\t"
    "v_exp_f32 v90, v90\n\t"
    "ds_read_b128 v[32:35], v21 offset:0\n\t"
    "v_add_f32 v44, v44, v90\n\t"
    "ds_read_b128 v[36:39], v21 offset:1088\n\t"
    "v_exp_f32 v91, v91\n\t"
    "v_add_u32 v47, 2176, v21\n\t"
    "v_add_f32 v44, v44, v91\n\t"
    "v_cvt_pk_bf16_f32 v77, v90, v91\n\t"
    "v_mfma_f32_16x16x32_bf16 v[192:195], a[160:163], v[72:75], v[192:195]\n\t"
    "ds_read2_b64 v[60:63], v47 offset0:1 offset1:0\n\t"
    "v_exp_f32 v92, v92\n\t"
    "v_mfma_f32_16x16x32_bf16 v[196:199], a[164:167], v[72:75], v[196:199]\n\t"
    "ds_read2_b64 v[56:59], v47 offset0:137 offset1:136\n\t"
    "s_add_u32 s73, s66, 49152\n\t"
    "v_add_f32 v44, v44, v92\n\t"
    "v_mfma_f32_16x16x32_bf16 v[200:203], a[168:171], v[72:75], v[200:203]\n\t"
    "v_exp_f32 v93, v93\n\t"
    "v_mfma_f32_16x16x32_bf16 v[204:207], a[172:175], v[72:75], v[204:207]\n\t"
    "s_waitcnt lgkmcnt(3)\n\t"
    "buffer_store_dwordx4 v[32:35], v22, s[52:55], s73 offen offset:0 nt\n\t"
    "v_add_f32 v44, v44, v93\n\t"
    "v_cvt_pk_bf16_f32 v78, v92, v93\n\t"
    "v_mfma_f32_16x16x32_bf16 v[208:211], a[176:179], v[72:75], v[208:211]\n\t"
    "v_exp_f32 v94, v94\n\t"
    "v_mfma_f32_16x16x32_bf16 v[212:215], a[180:183], v[72:75], v[212:215]\n\t"
    "s_waitcnt lgkmcnt(2)\n\t"
    "buffer_store_dwordx4 v[36:39], v22, s[52:55], s73 offen offset:1024 nt\n\t"
    "v_add_f32 v44, v44, v94\n\t"
    "v_mfma_f32_16x16x32_bf16 v[216:219], a[184:187], v[72:75], v[216:219]\n\t"
    "s_waitcnt lgkmcnt(1)\n\t"
    "buffer_store_dwordx4 v[60:63], v22, s[52:55], s73 offen offset:2048 nt\n\t"
    "v_exp_f32 v95, v95\n\t"
    "v_mfma_f32_16x16x32_bf16 v[220:223], a[188:191], v[72:75], v[220:223]\n\t"
    "s_waitcnt lgkmcnt(0)\n\t"
    "buffer_store_dwordx4 v[56:59], v22, s[52:55], s73 offen offset:3072 nt\n\t"
    "v_add_f32 v44, v44, v95\n\t"
    "v_cvt_pk_bf16_f32 v79, v94, v95\n\t"
    "v_mov_b32 v2, v44\n\t"
    "s_nop 1\n\t"
    "v_permlane16_swap_b32 v44, v2\n\t"
    "v_add_f32 v44, v44, v2\n\t"
    "v_mov_b32 v2, v44\n\t"
    "s_nop 1\n\t"
    "v_permlane32_swap_b32 v44, v2\n\t"
    "v_add_f32 v44, v44, v2\n\t"
    "v_cmp_ngt_f32 vcc, s71, v44\n\t"
    "v_mfma_f32_16x16x32_bf16 v[192:195], a[192:195], v[76:79], v[192:195]\n\t"
    "v_rcp_f32 v23, v44\n\t"
    "v_mfma_f32_16x16x32_bf16 v[196:199], a[196:199], v[76:79], v[196:199]\n\t"
    "v_mfma_f32_16x16x32_bf16 v[200:203], a[200:203], v[76:79], v[200:203]\n\t"
    "s_cmp_lg_u64 vcc, 0\n\t"
    "v_mfma_f32_16x16x32_bf16 v[204:207], a[204:207], v[76:79], v[204:207]\n\t"
    "s_cselect_b32 s70, 16, 0\n\t"
    "v_mfma_f32_16x16x32_bf16 v[208:211], a[208:211], v[76:79], v[208:211]\n\t"
    "s_or_b32 s68, s68, s70\n\t"
    "v_mfma_f32_16x16x32_bf16 v[212:215], a[212:215], v[76:79], v[212:215]\n\t"
    "v_mfma_f32_16x16x32_bf16 v[216:219], a[216:219], v[76:79], v[216:219]\n\t"
    "v_mfma_f32_16x16x32_bf16 v[220:223], a[220:223], v[76:79], v[220:223]\n\t"
    "v_mov_b32 v46, v23\n\t"
    "v_pk_mul_f32 v[192:193], v[192:193], v[46:47] op_sel_hi:[1,0]\n\t"
    "v_pk_mul_f32 v[194:195], v[194:195], v[46:47] op_sel_hi:[1,0]\n\t"
    "v_pk_mul_f32 v[196:197], v[196:197], v[46:47] op_sel_hi:[1,0]\n\t"
    "v_pk_mul_f32 v[198:199], v[198:199], v[46:47] op_sel_hi:[1,0]\n\t"
    "v_pk_mul_f32 v[200:201], v[200:201], v[46:47] op_sel_hi:[1,0]\n\t"
    "v_pk_mul_f32 v[202:203], v[202:203], v[46:47] op_sel_hi:[1,0]\n\t"
    "v_cvt_pk_bf16_f32 v192, v192, v193\n\t"
    "v_cvt_pk_bf16_f32 v193, v194, v195\n\t"
    "v_pk_mul_f32 v[204:205], v[204:205], v[46:47] op_sel_hi:[1,0]\n\t"
    "v_pk_mul_f32 v[206:207], v[206:207], v[46:47] op_sel_hi:[1,0]\n\t"
    "v_cvt_pk_bf16_f32 v196, v196, v197\n\t"
    "v_cvt_pk_bf16_f32 v197, v198, v199\n\t"
    "ds_write_b64 v20, v[192:193] offset:4352\n\t"
    "v_pk_mul_f32 v[208:209], v[208:209], v[46:47] op_sel_hi:[1,0]\n\t"
    "v_pk_mul_f32 v[210:211], v[210:211], v[46:47] op_sel_hi:[1,0]\n\t"
    "v_cvt_pk_bf16_f32 v200, v200, v201\n\t"
    "v_cvt_pk_bf16_f32 v201, v202, v203\n\t"
    "ds_write_b64 v20, v[196:197] offset:4384\n\t"
    "v_pk_mul_f32 v[212:213], v[212:213], v[46:47] op_sel_hi:[1,0]\n\t"
    "v_pk_mul_f32 v[214:215], v[214:215], v[46:47] op_sel_hi:[1,0]\n\t"
    "v_cvt_pk_bf16_f32 v204, v204, v205\n\t"
    "v_cvt_pk_bf16_f32 v205, v206, v207\n\t"
    "ds_write_b64 v20, v[200:201] offset:4416\n\t"
    "v_pk_mul_f32 v[216:217], v[216:217], v[46:47] op_sel_hi:[1,0]\n\t"
    "v_pk_mul_f32 v[218:219], v[218:219], v[46:47] op_sel_hi:[1,0]\n\t"
    "v_cvt_pk_bf16_f32 v208, v208, v209\n\t"
    "v_cvt_pk_bf16_f32 v209, v210, v211\n\t"
    "ds_write_b64 v20, v[204:205] offset:4448\n\t"
    "v_pk_mul_f32 v[220:221], v[220:221], v[46:47] op_sel_hi:[1,0]\n\t"
    "v_pk_mul_f32 v[222:223], v[222:223], v[46:47] op_sel_hi:[1,0]\n\t"
    "v_cvt_pk_bf16_f32 v212, v212, v213\n\t"
    "v_cvt_pk_bf16_f32 v213, v214, v215\n\t"
    "ds_write_b64 v20, v[208:209] offset:4480\n\t"
    "v_cvt_pk_bf16_f32 v216, v216, v217\n\t"
    "v_cvt_pk_bf16_f32 v217, v218, v219\n\t"
    "ds_write_b64 v20, v[212:213] offset:4512\n\t"
    "v_cvt_pk_bf16_f32 v220, v220, v221\n\t"
    "v_cvt_pk_bf16_f32 v221, v222, v223\n\t"
    "ds_write_b64 v20, v[216:217] offset:4544\n\t"
    "ds_write_b64 v20, v[220:221] offset:4576\n\t"
    "s_waitcnt lgkmcnt(0)\n\t"
    "ds_read_b128 v[32:35], v21 offset:4352\n\t"
    "ds_read_b128 v[36:39], v21 offset:5440\n\t"
    "v_add_u32 v47, 6528, v21\n\t"
    "ds_read2_b64 v[64:67], v47 offset0:1 offset1:0\n\t"
    "ds_read2_b64 v[60:63], v47 offset0:137 offset1:136\n\t"
    "s_add_u32 s73, s66, 65536\n\t"
    "s_waitcnt lgkmcnt(3)\n\t"
    "buffer_store_dwordx4 v[32:35], v22, s[52:55], s73 offen offset:0 nt\n\t"
    "s_waitcnt lgkmcnt(2)\n\t"
    "buffer_store_dwordx4 v[36:39], v22, s[52:55], s73 offen offset:1024 nt\n\t"
    "s_waitcnt lgkmcnt(1)\n\t"
    "buffer_store_dwordx4 v[64:67], v22, s[52:55], s73 offen offset:2048 nt\n\t"
    "s_waitcnt lgkmcnt(0)\n\t"
    "buffer_store_dwordx4 v[60:63], v22, s[52:55], s73 offen offset:3072 nt\n\t"
    "4:\n\t"
    "s_waitcnt lgkmcnt(0)\n\t"
    "s_mov_b32 %0, s68\n\t"
    "s_mov_b64 %1, s[84:85]\n\t"
    "s_mov_b64 %2, s[86:87]\n\t"
    "s_mov_b64 %3, s[88:89]\n\t"
    "s_mov_b64 %4, s[90:91]\n\t"
    "s_mov_b32 %5, s60\n\t"
    : "=&s"(flag), "=&s"(Qh), "=&s"(Kh), "=&s"(Vh), "=&s"(Oh), "=&s"(wave)
    : "s"(__builtin_amdgcn_kernarg_segment_ptr()), "s"(blockIdx.x), "s"(__builtin_amdgcn_readfirstlane(threadIdx.x >> 6)), "s"((unsigned)(uintptr_t)lds)
    : "v0", "v1", "v2", "v3", "v4", "v5", "v6", "v7", "v8", "v9", "v10", "v11", "v12", "v13", "v14", "v15", "v16", "v17", "v18", "v19", "v20", "v21", "v22", "v23", "v24", "v25", "v26", "v27", "v28", "v29", "v30", "v31", "v32", "v33", "v34", "v35", "v36", "v37", "v38", "v39", "v40", "v41", "v42", "v43", "v44", "v45", "v46", "v47", "v48", "v49", "v50", "v51", "v52", "v53", "v54", "v55", "v56", "v57", "v58", "v59", "v60", "v61", "v62", "v63", "v64", "v65", "v66", "v67", "v68", "v69", "v70", "v71", "v72", "v73", "v74", "v75", "v76", "v77", "v78", "v79", "v80", "v81", "v82", "v83", "v84", "v85", "v86", "v87", "v88", "v89", "v90", "v91", "v92", "v93", "v94", "v95", "v96", "v97", "v98", "v99", "v100", "v101", "v102", "v103", "v104", "v105", "v106", "v107", "v108", "v109", "v110", "v111", "v112", "v113", "v114", "v115", "v116", "v117", "v118", "v119", "v120", "v121", "v122", "v123", "v124", "v125", "v126", "v127", "v128", "v129", "v130", "v131", "v132", "v133", "v134", "v135", "v136", "v137", "v138", "v139", "v140", "v141", "v142", "v143", "v144", "v145", "v146", "v147", "v148", "v149", "v150", "v151", "v152", "v153", "v154", "v155", "v156", "v157", "v158", "v159", "v160", "v161", "v162", "v163", "v164", "v165", "v166", "v167", "v168", "v169", "v170", "v171", "v172", "v173", "v174", "v175", "v176", "v177", "v178", "v179", "v180", "v181", "v182", "v183", "v184", "v185", "v186", "v187", "v188", "v189", "v190", "v191", "v192", "v193", "v194", "v195", "v196", "v197", "v198", "v199", "v200", "v201", "v202", "v203", "v204", "v205", "v206", "v207", "v208", "v209", "v210", "v211", "v212", "v213", "v214", "v215", "v216", "v217", "v218", "v219", "v220", "v221", "v222", "v223", "v224", "v225", "v226", "v227", "v228", "v229", "v230", "v231", "v232", "v233", "v234", "v235", "v236", "v237", "v238", "v239", "v240", "v241", "v242", "v243", "v244", "v245", "v246", "v247", "v248", "v249", "v250", "v251", "v252", "v253", "v254", "v255", "a0", "a1", "a2", "a3", "a4", "a5", "a6", "a7", "a8", "a9", "a10", "a11", "a12", "a13", "a14", "a15", "a16", "a17", "a18", "a19", "a20", "a21", "a22", "a23", "a24", "a25", "a26", "a27", "a28", "a29", "a30", "a31", "a32", "a33", "a34", "a35", "a36", "a37", "a38", "a39", "a40", "a41", "a42", "a43", "a44", "a45", "a46", "a47", "a48", "a49", "a50", "a51", "a52", "a53", "a54", "a55", "a56", "a57", "a58", "a59", "a60", "a61", "a62", "a63", "a64", "a65", "a66", "a67", "a68", "a69", "a70", "a71", "a72", "a73", "a74", "a75", "a76", "a77", "a78", "a79", "a80", "a81", "a82", "a83", "a84", "a85", "a86", "a87", "a88", "a89", "a90", "a91", "a92", "a93", "a94", "a95", "a96", "a97", "a98", "a99", "a100", "a101", "a102", "a103", "a104", "a105", "a106", "a107", "a108", "a109", "a110", "a111", "a112", "a113", "a114", "a115", "a116", "a117", "a118", "a119", "a120", "a121", "a122", "a123", "a124", "a125", "a126", "a127", "a128", "a129", "a130", "a131", "a132", "a133", "a134", "a135", "a136", "a137", "a138", "a139", "a140", "a141", "a142", "a143", "a144", "a145", "a146", "a147", "a148", "a149", "a150", "a151", "a152", "a153", "a154", "a155", "a156", "a157", "a158", "a159", "a160", "a161", "a162", "a163", "a164", "a165", "a166", "a167", "a168", "a169", "a170", "a171", "a172", "a173", "a174", "a175", "a176", "a177", "a178", "a179", "a180", "a181", "a182", "a183", "a184", "a185", "a186", "a187", "a188", "a189", "a190", "a191", "a192", "a193", "a194", "a195", "a196", "a197", "a198", "a199", "a200", "a201", "a202", "a203", "a204", "a205", "a206", "a207", "a208", "a209", "a210", "a211", "a212", "a213", "a214", "a215", "a216", "a217", "a218", "a219", "a220", "a221", "a222", "a223", "a224", "a225", "a226", "a227", "a228", "a229", "a230", "a231", "a232", "a233", "a234", "a235", "a236", "a237", "a238", "a239", "a240", "a241", "a242", "a243", "a244", "a245", "a246", "a247", "a248", "a249", "a250", "a251", "a252", "a253", "a254", "a255", "s40", "s41", "s42", "s43", "s44", "s45", "s46", "s47", "s48", "s49", "s50", "s51", "s52", "s53", "s54", "s55", "s56", "s57", "s58", "s59", "s60", "s61", "s62", "s63", "s64", "s65", "s66", "s67", "s68", "s69", "s70", "s71", "s72", "s73", "s74", "s75", "s76", "s77", "s78", "s79", "s80", "s81", "s82", "s83", "s84", "s85", "s86", "s87", "s88", "s89", "s90", "s91", "s92", "s93", "s94", "s95", "s96", "s97", "s98", "s99", "vcc", "scc", "m0", "memory");
  if (flag) {
    asm volatile("s_waitcnt vmcnt(0)" ::: "memory");
    unsigned lane;
    asm volatile("v_mbcnt_lo_u32_b32 %0, -1, 0\n\tv_mbcnt_hi_u32_b32 %0, -1, %0" : "=v"(lane));
    for (int j = 0; j < 6; ++j)
      if ((flag & (1u << j)) && 16 * (wave + 4 * j) < SQ) slow_qblock(Qh, Kh, Vh, Oh, 16 * (wave + 4 * j), (int)lane);
  }
}

extern "C" void kernel_launch(void* const* d_in, const int* in_sizes, int n_in, void* d_out, int out_size, void* d_ws, size_t ws_size, hipStream_t stream) {
  attn_fwd_kernel<<<256, 256, 0, stream>>>((const unsigned short*)d_in[0], (const unsigned short*)d_in[1], (const unsigned short*)d_in[2], (unsigned short*)d_out,
                                           (unsigned long long*)d_ws);
}
```
